```python
import torch

def _softmax(x, axis=-1):
    return torch.softmax(x, dim=axis)
def _gelu(x):
    return torch.nn.functional.gelu(x, approximate="tanh")
def _silu(x):
    return torch.nn.functional.silu(x)
def _sigmoid(x):
    return torch.sigmoid(x)
def _layernorm(x, axis=-1, eps=1e-5):
    m = x.mean(dim=axis, keepdim=True)
    v = x.var(dim=axis, keepdim=True, unbiased=False)
    return (x - m) / torch.sqrt(v + eps)
def _scatter_add(tgt, idx, src, axis=0):
    return tgt.clone().index_add_(axis, idx, src)
def _segment_sum(src, seg, n):
    out = torch.zeros((n,) + src.shape[1:], dtype=src.dtype)
    return out.index_add_(0, seg, src)
def _pad(x, pw):
    flat = tuple(v for pair in reversed(pw) for v in pair)
    return torch.nn.functional.pad(x, flat)

kernel_name = 'concat_heads__linear_attn_block__spectral_gfnet__prompt'

def setup_inputs():

    _w = (lambda a: (lambda t: t.to(torch.bfloat16) if t.is_floating_point() else t)(torch.as_tensor(a)))
    def _A_setup():
        B, S, D = 1, 2048, 512
        return {
            "x": _w((lambda *s: torch.randn(*s, dtype=torch.bfloat16) * 0.05)(B, S, D)),
            "wqkv": _w((lambda *s: torch.randn(*s, dtype=torch.bfloat16) * 0.4)(D, 3 * D)),
            "wo": _w((lambda *s: torch.randn(*s, dtype=torch.bfloat16) * 0.05)(D, D)),
        }
    _A_in = _A_setup()

    def _B_setup():
        B, S, D = 1, 2048, 512
        Df = D // 2 + 1
        return {
            "x": _w((lambda *s: torch.randn(*s, dtype=torch.bfloat16) * 0.05)(B, S, D)),
            "w_r": _w((lambda *s: torch.randn(*s, dtype=torch.bfloat16) * 1.6)(S, Df) * 0.02),
            "w_i": _w((lambda *s: torch.randn(*s, dtype=torch.bfloat16) * 1.6)(S, Df) * 0.02),
        }
    _B_in = _B_setup()

    return {
        "A_x": _A_in["x"],
        "A_wqkv": _A_in["wqkv"],
        "A_wo": _A_in["wo"],
        "B_x": _B_in["x"],
        "B_w_r": _B_in["w_r"],
        "B_w_i": _B_in["w_i"],
    }

def reference(A_x, A_wqkv, A_wo, B_x, B_w_r, B_w_i):
    def _A_ref(x, wqkv, wo):
        B, S, D = x.shape
        H = 32; Dh = D // H
        qkv = (lambda x, *d: x.permute(*d))((x @ wqkv).reshape(B, S, 3, H, Dh), 2, 0, 3, 1, 4)
        q, k, v = qkv[0], qkv[1], qkv[2]
        phi = lambda t: torch.where(t > 0, t + 1, torch.exp(t)) + 1e-6
        pq, pk = phi(q), phi(k)
        kv = torch.einsum("bhsd,bhse->bhde", pk, v)
        z = torch.einsum("bhsd->bhd", pk)
        num = torch.einsum("bhqd,bhde->bhqe", pq, kv)
        den = torch.einsum("bhqd,bhd->bhq", pq, z)[..., None] + 1e-6
        o = num / den
        return (lambda x, *d: x.permute(*d))(o, 0, 2, 1, 3).reshape(B, S, D) @ wo

    def _B_ref(x, w_r, w_i):
        B, S, D = x.shape
        X = (lambda x, axes=(-2,-1): torch.fft.rfft2(x.float(), dim=axes))(x, axes=(-2, -1))
        W = w_r + 1j * w_i
        X = X * W
        y = (lambda x, s, axes=(-2,-1): torch.fft.irfft2(x, s=s, dim=axes).to(torch.bfloat16))(X, (S, D), axes=(-2, -1))
        return y

    a = _A_ref(A_x, A_wqkv, A_wo)
    b = _B_ref(B_x, B_w_r, B_w_i)
    return torch.concatenate([a, b], axis=-1)
```

```cpp
#include <hip/hip_runtime.h>
#include <stdint.h>

typedef short    s16x4 __attribute__((ext_vector_type(4)));
typedef __bf16   bf16x8 __attribute__((ext_vector_type(8)));
typedef __bf16   bf16x2 __attribute__((ext_vector_type(2)));
typedef float    f32x2 __attribute__((ext_vector_type(2)));
typedef float    f32x4 __attribute__((ext_vector_type(4)));
typedef unsigned u32x2 __attribute__((ext_vector_type(2)));
typedef unsigned u32x4 __attribute__((ext_vector_type(4)));
typedef __amdgpu_buffer_rsrc_t rsrc_t;
#define LAS __attribute__((address_space(3)))
#define DI __device__ __forceinline__

constexpr unsigned WS_EPOCH = 0, WS_FX1 = 1024, WS_FPQ = 2048, WS_FX2 = 3072;
constexpr unsigned PART_STRIDE = 2304, WS_PART = 4096;
constexpr unsigned TB = 16512;
constexpr unsigned WS_T1 = 1u << 19, WS_T2 = WS_T1 + 128 * TB;
constexpr unsigned WS_PQ = WS_T2 + 128 * TB, WS_O = WS_PQ + (1u << 21), WS_END = WS_O + (1u << 21);
constexpr unsigned POISON = 0xFFFFFFFFu;
constexpr int SPIN_CAP = 1 << 15;
constexpr unsigned LDS_BYTES = 163840;
#define STAMP(i) do {} while (0)
#define STP_PARAM
#define STP_ARG

struct Params {
    const unsigned short *ax, *wqkv, *wo, *bx, *wr, *wi;
    unsigned short* out;
    unsigned char* ws;
};

DI rsrc_t mkrsrc(const void* ptr, unsigned bytes) { return __builtin_amdgcn_make_buffer_rsrc((void*)ptr, 0, bytes, 0x00020000); }
DI u32x4 ld128(rsrc_t r, unsigned off)  { return __builtin_amdgcn_raw_buffer_load_b128(r, off, 0, 16); }
DI u32x2 ld64(rsrc_t r, unsigned off)   { return __builtin_amdgcn_raw_buffer_load_b64(r, off, 0, 16); }
DI unsigned ld32(rsrc_t r, unsigned off){ return __builtin_amdgcn_raw_buffer_load_b32(r, off, 0, 16); }
DI u32x4 ld128s(rsrc_t r, unsigned off, unsigned so)  { return __builtin_amdgcn_raw_buffer_load_b128(r, off, so, 16); }
DI unsigned ld32s(rsrc_t r, unsigned off, unsigned so){ return __builtin_amdgcn_raw_buffer_load_b32(r, off, so, 16); }
DI void st128(rsrc_t r, unsigned off, u32x4 v) { __builtin_amdgcn_raw_buffer_store_b128(v, r, off, 0, 16); }
DI void st64(rsrc_t r, unsigned off, u32x2 v) { __builtin_amdgcn_raw_buffer_store_b64(v, r, off, 0, 16); }
DI void st32(rsrc_t r, unsigned off, unsigned v) { __builtin_amdgcn_raw_buffer_store_b32(v, r, off, 0, 16); }
#define FENCE() asm volatile("" ::: "memory")
#define BAR() asm volatile("s_waitcnt lgkmcnt(0)\n\ts_barrier" ::: "memory")
#define VMWAIT(n) asm volatile("s_waitcnt vmcnt(" #n ")" ::: "memory")
#define PIN(x) asm volatile("" : "+v"(x))
#define SCHED_FENCE() __builtin_amdgcn_sched_barrier(0)
DI void dma(rsrc_t r, unsigned voff, unsigned soff, unsigned lds) {
    asm volatile("s_mov_b32 m0, %0\n\ts_nop 0\n\tbuffer_load_dwordx4 %1, %2, %3 offen lds"
                 :: "s"(lds), "v"(voff), "s"(r), "s"(soff) : "memory");
}
DI unsigned pk(float lo, float hi) { f32x2 v = {lo, hi}; return __builtin_bit_cast(unsigned, __builtin_convertvector(v, bf16x2)); }
DI u32x2 pkv(f32x4 v) { return u32x2{pk(v[0], v[1]), pk(v[2], v[3])}; }
DI u32x4 cat(u32x2 a, u32x2 b) { return u32x4{a[0], a[1], b[0], b[1]}; }
DI f32x4 mma(u32x4 a, u32x4 b, f32x4 c) {
    return __builtin_amdgcn_mfma_f32_16x16x32_bf16(__builtin_bit_cast(bf16x8, a), __builtin_bit_cast(bf16x8, b), c, 0, 0, 0); }
DI f32x4 mma16(u32x2 a, u32x2 b, f32x4 c) {
    return __builtin_amdgcn_mfma_f32_16x16x16bf16_1k(__builtin_bit_cast(s16x4, a), __builtin_bit_cast(s16x4, b), c, 0, 0, 0); }
#define LP(T, a) ((LAS T*)(size_t)(a))
DI u32x4 lr128(unsigned a) { return *LP(const u32x4, a); }
DI f32x4 lrf128(unsigned a) { return *LP(const f32x4, a); }
DI unsigned lr32(unsigned a) { return *LP(const unsigned, a); }
DI float bf(unsigned short v) { return __builtin_bit_cast(float, (unsigned)v << 16); }
DI float lrbf(unsigned a) { return bf(*LP(const unsigned short, a)); }
DI void lw128(unsigned a, u32x4 v) { *LP(u32x4, a) = v; }
DI void lwf128(unsigned a, f32x4 v) { *LP(f32x4, a) = v; }
DI void lw64(unsigned a, u32x2 v) { *LP(u32x2, a) = v; }
DI void lw32(unsigned a, unsigned v) { *LP(unsigned, a) = v; }
DI u32x2 ltr(unsigned a) { return __builtin_bit_cast(u32x2, __builtin_amdgcn_ds_read_tr16_b64_v4i16(LP(s16x4, a))); }
DI u32x4 ltr2(unsigned a, unsigned b) { return cat(ltr(a), ltr(b)); }
DI unsigned umax3(unsigned a, unsigned b, unsigned c) { return max(max(a, b), c); }
DI unsigned umax4(unsigned m, u32x4 v) { return umax3(umax3(m, v[0], v[1]), v[2], v[3]); }
DI u32x2 phi4(f32x4 t) {
    f32x4 e, m;
#pragma unroll
    for (int r = 0; r < 4; ++r) {
        e[r] = __builtin_amdgcn_fmed3f(__builtin_amdgcn_exp2f(t[r] * 1.44269504f), 0.f, 1.f);
        m[r] = __builtin_elementwise_maximum(t[r], 0.f);
    }
    return pkv((m + e) + 1e-6f);
}
constexpr f32x4 Z4 = {0.f, 0.f, 0.f, 0.f};

struct Mat { u32x4 re, im; };
struct Cx  { f32x4 re, im; };
DI Cx angles(int n0, int dn, int mask, float inv) {
    Cx t;
#pragma unroll
    for (int r = 0; r < 4; ++r) { float a = (float)((n0 + r * dn) & mask) * inv;
        t.re[r] = __builtin_amdgcn_cosf(a); t.im[r] = __builtin_amdgcn_sinf(a); }
    return t;
}
DI u32x4 zip(f32x4 a, f32x4 b) { return u32x4{pk(a[0], b[0]), pk(a[1], b[1]), pk(a[2], b[2]), pk(a[3], b[3])}; }
DI Mat tomat(Cx t) { return Mat{zip(t.re, t.im), zip(-t.im, t.re)}; }
DI Cx dftA(u32x4 d, const Mat& f) { return Cx{mma(d, f.re, Z4), mma(d, f.im, Z4)}; }
DI Cx dftB(u32x4 d, const Mat& f) { return Cx{mma(f.re, d, Z4), mma(f.im, d, Z4)}; }
DI u32x4 twpk(Cx v, const Cx& w) { return zip(v.re * w.re + v.im * w.im, v.im * w.re - v.re * w.im); }
DI u32x4 pk4(Cx v)  { return zip(v.re, v.im); }
DI u32x4 pk4c(Cx v) { return zip(v.re, v.im) ^ 0x80000000u; }

DI void wait_flags(rsrc_t rws, unsigned base, unsigned ep, int lane) {
#pragma unroll 1
    for (int it = 0; it < SPIN_CAP; ++it) {
        FENCE();
        u32x2 f = ld64(rws, base + lane * 8);
        if (__all(f[0] == ep && f[1] == ep)) break;
        __builtin_amdgcn_s_sleep(2);
    }
    FENCE();
}
DI void dma_x(rsrc_t rx, unsigned v0, unsigned v1, unsigned soff, unsigned lds) {
    dma(rx, v0, soff, lds); dma(rx, v1, soff + 8192, lds + 1024); dma(rx, v0, soff + 16384, lds + 2048); dma(rx, v1, soff + 24576, lds + 3072);
}
template <int NB> struct Frag { u32x4 a[4], b[NB]; };
template <int NB> DI void frag_read(Frag<NB>& f, unsigned xa, unsigned w0) {
    const unsigned w1 = w0 ^ 32;
    f.a[0] = lr128(xa); f.b[0] = ltr2(w0, w0 + 512);
    f.a[1] = lr128(xa + 2048); f.a[2] = lr128(xa + 4096); f.a[3] = lr128(xa + 6144);
    f.b[1] = ltr2(w1, w1 + 512);
    if constexpr (NB == 4) { f.b[2] = ltr2(w0 + 8192, w0 + 8704); f.b[3] = ltr2(w1 + 8192, w1 + 8704); }
}
template <int NB> DI void frag_mma(const Frag<NB>& f, f32x4 (&acc)[4][NB]) {
#pragma unroll
    for (int j = 0; j < NB; ++j)
#pragma unroll
        for (int i = 0; i < 4; ++i) acc[i][j] = (NB == 4) ? mma(f.a[i], f.b[j], acc[i][j]) : mma(f.b[j], f.a[i], acc[i][j]);
}
template <int NB> DI void interleave() {
#pragma unroll
    for (int i = 0; i < 2 + NB; ++i) { __builtin_amdgcn_sched_group_barrier(0x8, 1, 0); __builtin_amdgcn_sched_group_barrier(0x100, 2, 0); }
    __builtin_amdgcn_sched_group_barrier(0x8, 4 * NB, 0);
}

DI void role_kv(const Params& p, rsrc_t rws, unsigned ep, int xcd, int li, int tid, int lane, int w, int ct STP_PARAM) {
    const int rp = (xcd >> 1) * 4 + (li & 3), hg = (xcd & 1) * 4 + (li >> 2);
    const int rot = 2 * (li >> 2) + 2 * (li & 3);
    const int g = lane >> 4, c16 = lane & 15;
    if (w >= 4) {
        const int lw = w - 4, r8 = lane >> 3, ch = lane & 7;
        const rsrc_t rx = mkrsrc(p.ax, 2048 * 512 * 2), rw = mkrsrc(p.wqkv, 512 * 1536 * 2), ro = mkrsrc(p.wo, 512 * 512 * 2);
        const unsigned xrow = (rp * 128 + lw * 32 + r8) * 1024;
        const unsigned vx0 = xrow + ((ch ^ (r8 >> 1)) << 4), vx1 = vx0 ^ 64;
        const unsigned vw0 = (lw * 16 + r8) * 3072 + hg * 128 + 1024 + ((ch ^ (r8 & 2)) << 4), vw1 = (vw0 ^ 64) + 8 * 3072;
        const unsigned vo = (lw * 64 + r8) * 1024 + ct * 128 + ((ch ^ (r8 & 6)) << 4);
#pragma unroll 1
        for (int s = -4; s < 8; ++s) {
            if (s >= 0) { if (s < 7) VMWAIT(24); else VMWAIT(16); BAR(); }
            if (s < 4) {
                const unsigned kb = (s + 4 + rot) & 7, slot = (s > 0 ? s - 1 : s + 4) * 32768, so = kb * (64 * 3072);
                dma_x(rx, vx0, vx1, kb * 128, slot + lw * 4096);
                dma(rw, vw0, so, slot + 16384 + lw * 2048); dma(rw, vw1, so, slot + 16384 + lw * 2048 + 1024);
                dma(rw, vw0, so + 1024, slot + 24576 + lw * 2048); dma(rw, vw1, so + 1024, slot + 24576 + lw * 2048 + 1024);
            } else if (s < 6) {
#pragma unroll 1
                for (int q = 0; q < 8; ++q) dma(ro, vo, (s - 4) * 262144 + q * 8192, 98304 + (s - 4) * 32768 + lw * 8192 + q * 1024);
            }
        }
        STAMP(1);
        BAR();
    } else {
        const int rh = w >> 1, hp = w & 1;
        f32x4 acc[4][4];
#pragma unroll
        for (int i = 0; i < 4; ++i)
#pragma unroll
            for (int j = 0; j < 4; ++j) { acc[i][j] = Z4; PIN(acc[i][j]); }
        const unsigned xa = (rh * 64 + c16) * 128 + ((g ^ (c16 >> 1)) << 4);
        const int q = c16 >> 2, fk = (q >> 1) | ((g & 1) << 1);
        const unsigned wa = 16384 + (8 * g + q) * 128 + (((2 * hp) ^ fk) << 5) + 8 * (lane & 3);
        Frag<4> f0, f1;
        unsigned slot = 0;
        BAR();
        frag_read(f0, xa, wa);
#pragma unroll 1
        for (int s = 0; s < 7; ++s) {
            SCHED_FENCE();
            frag_read(f1, slot + (xa ^ 64), slot + wa + 4096); frag_mma(f0, acc); interleave<4>();
            slot = (slot == 4 * 32768) ? 0 : slot + 32768;
            SCHED_FENCE(); BAR();
            frag_read(f0, slot + xa, slot + wa); frag_mma(f1, acc); interleave<4>();
        }
        SCHED_FENCE();
        u32x2 fx = {ep, ep};
        if (w == 2) fx = ld64(rws, WS_FX1 + lane * 8);
        frag_read(f1, slot + (xa ^ 64), slot + wa + 4096); frag_mma(f0, acc); interleave<4>();
        SCHED_FENCE();
        frag_mma(f1, acc);
        STAMP(1);
        f32x4 kv[2] = {Z4, Z4}, zz[2] = {Z4, Z4};
        const u32x4 ones = {0x3F803F80u, 0x3F803F80u, 0x3F803F80u, 0x3F803F80u};
#pragma unroll
        for (int b = 0; b < 2; ++b)
#pragma unroll
            for (int u = 0; u < 2; ++u) {
                const u32x4 A = cat(phi4(acc[2 * u][b]), phi4(acc[2 * u + 1][b])), B = cat(pkv(acc[2 * u][2 + b]), pkv(acc[2 * u + 1][2 + b]));
                kv[b] = mma(A, B, kv[b]); zz[b] = mma(A, ones, zz[b]);
            }
        const unsigned red = hp * 4096 + lane * 16;
        if (rh) { lwf128(red, kv[0]); lwf128(red + 1024, kv[1]); lwf128(red + 2048, zz[0]); lwf128(red + 3072, zz[1]); }
        if (!__all(fx[0] == ep && fx[1] == ep)) wait_flags(rws, WS_FX1, ep, lane);
        BAR();
        if (!rh) {
            kv[0] += lrf128(red); kv[1] += lrf128(red + 1024); zz[0] += lrf128(red + 2048); zz[1] += lrf128(red + 3072);
            const unsigned pb = WS_PART + (rp * 8 + hg) * PART_STRIDE;
            st128(rws, pb + hp * 1024 + lane * 16, cat(pkv(kv[0]), pkv(kv[1])));
            if (c16 == 0) { const unsigned zb = pb + 2048 + hp * 64 + g * 8; st64(rws, zb, pkv(zz[0])); st64(rws, zb + 32, pkv(zz[1])); }
        }
    }
    STAMP(2);
    if (w == 3) {
#pragma unroll 1
        for (int it = 0; it < SPIN_CAP; ++it) {
            FENCE();
            if (__builtin_amdgcn_readfirstlane(ld32(rws, WS_FPQ + (rp * 8 + hg) * 4)) == ep) break;
            __builtin_amdgcn_s_sleep(2);
        }
    }
    const int rg = w >> 1, hp = w & 1;
    unsigned pqb = WS_PQ + (((rp * 8 + hg) * 8 + 2 * rg) * 2 + hp) * 1024 + lane * 16;
    unsigned pb = WS_PART + hg * PART_STRIDE + tid * 4, pz = WS_PART + hg * PART_STRIDE + (tid >> 5) * 8 * PART_STRIDE + 2048 + (tid & 31) * 4;
    unsigned za = 32768 + 2048 + (4 * g + (c16 >> 2)) * 128 + hp * 64 + 8 * (lane & 3);
    unsigned ob = WS_O + ((rp * 8 + 2 * rg) * 16 + hg * 2 + hp) * 1024 + lane * 16;
    PIN(pqb); PIN(pb); PIN(pz); PIN(za); PIN(ob);
    if (w < 2) VMWAIT(0);
    BAR();
    STAMP(3);
    unsigned d[16], zr;
    u32x4 pq0, pq1;
#pragma unroll 1
    for (int it = 0; it < SPIN_CAP; ++it) {
        FENCE();
#pragma unroll
        for (int j = 0; j < 16; ++j) d[j] = ld32s(rws, pb, j * 8 * PART_STRIDE);
        zr = ld32(rws, pz);
        pq0 = ld128(rws, pqb); pq1 = ld128(rws, pqb + 2048);
        unsigned m = zr;
#pragma unroll
        for (int j = 0; j < 16; j += 2) m = umax3(m, d[j], d[j + 1]);
        if (!__any(m == POISON)) break;
    }
    f32x2 sm = {0.f, 0.f};
#pragma unroll
    for (int j = 0; j < 16; ++j) sm += f32x2{__builtin_bit_cast(float, d[j] << 16), __builtin_bit_cast(float, d[j] & 0xFFFF0000u)};
    lw32(32768 + tid * 4, pk(sm[0], sm[1])); lw32(32768 + 2048 + tid * 4, zr);
    BAR();
    STAMP(4);
    {
        const u32x4 kvA = lr128(32768 + hp * 1024 + lane * 16);
        const u32x2 one2 = {0x3F803F80u, 0x3F803F80u};
        const u32x2 zA[2] = {pkv(mma16(ltr(za), one2, Z4)), pkv(mma16(ltr(za + 32), one2, Z4))};
#pragma unroll
        for (int a = 0; a < 2; ++a) {
            const u32x4 pq = a ? pq1 : pq0;
            u32x2 o[2];
#pragma unroll
            for (int b = 0; b < 2; ++b) {
                const u32x2 B = {pq[2 * b], pq[2 * b + 1]};
                const f32x4 num = mma16(u32x2{kvA[2 * b], kvA[2 * b + 1]}, B, Z4), den = mma16(zA[b], B, Z4);
                o[b] = pkv(num * __builtin_amdgcn_rcpf(den[0] + 1e-6f));
            }
            st128(rws, ob + a * 16384, cat(o[0], o[1]));
        }
    }
    STAMP(5);
    VMWAIT(0);
}

constexpr unsigned L_U = 98304;
constexpr unsigned L_W = L_U + 19200;
constexpr unsigned L_SP = L_W + 20480;
constexpr unsigned L_C = L_SP + 512;
DI void role_qb(const Params& p, rsrc_t rws, unsigned ep, int xcd, int li, int tid, int lane, int w, int ct STP_PARAM) {
    const int rp = (xcd >> 1) * 4 + (li & 3), hg = (xcd & 1) * 4 + (li >> 2);
    const int rot = 2 * (li >> 2) + 2 * (li & 3) + 1;
    const int g = lane >> 4, c16 = lane & 15;
    const int me = xcd * 16 + li;
    const int jh = li >> 3, j1 = jh ? (xcd ? 16 - xcd : 8) : xcd, kb = li & 7;
    const int lw = w - 4, r8 = lane >> 3, ch = lane & 7;
    u32x4 x0, x1;
    {
        const rsrc_t rb = mkrsrc(p.bx, 2048 * 512 * 2);
        const unsigned vo = (256 * w + me) * 1024 + g * 256 + c16 * 4;
        asm volatile("buffer_load_dword %0, %8, %9, 0 offen\n\tbuffer_load_dword %1, %8, %9, 0 offen offset:64\n\t"
                     "buffer_load_dword %2, %8, %9, 0 offen offset:128\n\tbuffer_load_dword %3, %8, %9, 0 offen offset:192\n\t"
                     "buffer_load_dword %4, %8, %9, %10 offen\n\tbuffer_load_dword %5, %8, %9, %10 offen offset:64\n\t"
                     "buffer_load_dword %6, %8, %9, %10 offen offset:128\n\tbuffer_load_dword %7, %8, %9, %10 offen offset:192"
                     : "=&v"(x0[0]), "=&v"(x0[1]), "=&v"(x0[2]), "=&v"(x0[3]), "=&v"(x1[0]), "=&v"(x1[1]), "=&v"(x1[2]), "=&v"(x1[3])
                     : "v"(vo), "s"(rb), "s"(131072u) : "memory");
    }
    const rsrc_t rx = mkrsrc(p.ax, 2048 * 512 * 2), rw = mkrsrc(p.wqkv, 512 * 1536 * 2);
    const unsigned vx0 = (rp * 128 + lw * 32 + r8) * 1024 + ((ch ^ (r8 >> 1)) << 4), vx1 = vx0 ^ 64;
    const unsigned vw0 = (lw * 16 + r8) * 3072 + hg * 128 + ((ch ^ (r8 & 2)) << 4), vw1 = (vw0 ^ 64) + 8 * 3072;
    auto stage = [&](int s) {
        const unsigned kq = (s + rot) & 7, slot = (s & 3) * 24576;
        dma_x(rx, vx0, vx1, kq * 128, slot + lw * 4096);
        dma(rw, vw0, kq * (64 * 3072), slot + 16384 + lw * 2048); dma(rw, vw1, kq * (64 * 3072), slot + 16384 + lw * 2048 + 1024);
    };
    if (w >= 4) {
#pragma unroll 1
        for (int s = 0; s < 2; ++s) stage(s);
    }
    if (w < 5) {
        const int k = w, k4 = 4 * k, sU = (0x04004 >> k4) & 15, sQ = (0x40040 >> k4) & 15;
        const int P = (((c16 & ((0x7F7FF >> k4) & 15)) << sU) + (sU ? j1 : 0)) << ((0x00833 >> k4) & 15);
        const int n0 = P * (((4 * (g & ((0x31133 >> k4) & 15))) << sQ) + (sQ ? j1 : 0)), dn = P << sQ;
        const Cx t = angles(n0, dn, 2047, 1.f / 2048);
        Mat m = tomat(t);
        if (k > 2) m = Mat{__builtin_bit_cast(u32x4, t.re), __builtin_bit_cast(u32x4, t.im)};
        if (k == 2 && (g >> 1) != (c16 >> 3)) m = Mat{u32x4{0, 0, 0, 0}, u32x4{0, 0, 0, 0}};
        lw128(L_C + k * 2048 + lane * 16, m.re); lw128(L_C + k * 2048 + 1024 + lane * 16, m.im);
    }
    Mat F = tomat(angles(4 * g * c16, c16, 15, 1.f / 16));
    Cx tw1 = angles(4 * g * c16, c16, 255, 1.f / 256);
    const int km0 = 16 * w + c16, km1 = km0 ? 256 - km0 : 128;
    unsigned ua = L_U + (2 * w) * 1072 + g * 272 + c16 * 16;
    unsigned a0 = L_U + 4 * g * 1072 + ((km0 >> 2) & 3) * 272 + (km0 >> 4) * 16 + (km0 & 3) * 4;
    unsigned a1s = L_U + 4 * g * 1072 + ((km1 >> 2) & 3) * 272 + (km1 >> 4) * 16 + (km1 & 3) * 4;
    unsigned sw0 = L_W + w * 2304 + c16 * 144 + g * 16, sw1 = L_W + w * 2304 + ((16 - c16) & 15) * 144 + 64 + g * 16;
    unsigned sr0 = L_W + w * 2304 + r8 * 144 + ch * 16;
    unsigned tb1 = WS_T1 + me * TB + w * 2048 + lane * 16, obp = WS_O + ((rp * 8 + w) * 16 + hg * 2) * 1024 + lane * 16;
    PIN(F.re); PIN(F.im); PIN(tw1.re); PIN(tw1.im); PIN(ua); PIN(a0); PIN(a1s); PIN(sw0); PIN(sw1); PIN(sr0); PIN(tb1); PIN(obp);
    asm volatile("s_cmp_lt_u32 %2, 4\n\ts_cbranch_scc1 1f\n\ts_waitcnt vmcnt(12)\n\ts_branch 2f\n1:\n\ts_waitcnt vmcnt(0)\n2:"
                 : "+v"(x0), "+v"(x1) : "s"(w) : "memory", "scc");
    STAMP(1);
    {
        lw128(ua, pk4(dftA(twpk(dftA(x0, F), tw1), F))); lw128(ua + 1072, pk4(dftA(twpk(dftA(x1, F), tw1), F)));
        BAR();
        const u32x4 d0 = {lr32(a0), lr32(a0 + 1072), lr32(a0 + 2144), lr32(a0 + 3216)};
        const u32x4 d1 = {lr32(a1s), lr32(a1s + 1072), lr32(a1s + 2144), lr32(a1s + 3216)};
        lw128(sw0, pk4(dftA(d0, F))); lw128(sw1, pk4c(dftA(d1, F)));
        const u32x4 s0 = lr128(sr0), s1 = lr128(sr0 + 8 * 144);
        st128(rws, tb1, s0); st128(rws, tb1 + 1024, s1);
        const u32x4 P4 = {POISON, POISON, POISON, POISON};
        st128(rws, obp, P4); st128(rws, obp + 1024, P4);
        if (tid < 136) st128(rws, WS_PART + (rp * 8 + hg) * PART_STRIDE + tid * 16, P4);
    }
    STAMP(2);
    const bool special = (kb == 0 && w == 0);
    float sp[6][4];
    if (w >= 4) {
#pragma unroll 1
        for (int s = -2; s < 5; ++s) {
            if (s >= 0) { if (s == 0) VMWAIT(22); else if (s == 1) VMWAIT(16); else VMWAIT(12); BAR(); }
            if (s != 0) stage(s < 0 ? s + 4 : s + 3);
        }
        struct __attribute__((packed, aligned(2))) U4 { u32x4 v; };
        const int j2 = (tid >> 1) & 127, ks = j1 + 16 * j2, hf = (tid & 1) * 8;
        const int op = ks * 257 + 16 * kb + hf, oq = ((2048 - ks) & 2047) * 257 + 241 - 16 * kb + hf;
        const u32x4 l0 = ((const U4*)(p.wr + op))->v, l1 = ((const U4*)(p.wi + op))->v, l2 = ((const U4*)(p.wr + oq))->v, l3 = ((const U4*)(p.wi + oq))->v;
        FENCE();
        VMWAIT(16); BAR(); VMWAIT(10); BAR(); VMWAIT(4); BAR();
        const unsigned wa = L_W + j2 * 40 + (tid & 1) * 16;
        lw64(wa, u32x2{l0[0], l0[1]}); lw64(wa + 8, u32x2{l0[2], l0[3]});
        lw64(wa + 5120, u32x2{l1[0], l1[1]}); lw64(wa + 5128, u32x2{l1[2], l1[3]});
        lw64(wa + 10240, u32x2{l2[0], l2[1]}); lw64(wa + 10248, u32x2{l2[2], l2[3]});
        lw64(wa + 15360, u32x2{l3[0], l3[1]}); lw64(wa + 15368, u32x2{l3[2], l3[3]});
        if (w == 7) wait_flags(rws, WS_FX1, ep, lane);
    } else {
        const int rh = w >> 1, hp = w & 1;
        f32x4 acc[4][2];
#pragma unroll
        for (int i = 0; i < 4; ++i) { acc[i][0] = Z4; acc[i][1] = Z4; PIN(acc[i][0]); PIN(acc[i][1]); }
        const unsigned xa = (rh * 64 + c16) * 128 + ((g ^ (c16 >> 1)) << 4);
        const int q = c16 >> 2, fk = (q >> 1) | ((g & 1) << 1);
        const unsigned wa = 16384 + (8 * g + q) * 128 + (((2 * hp) ^ fk) << 5) + 8 * (lane & 3);
        Frag<2> f0, f1;
        BAR();
        frag_read(f0, xa, wa);
#pragma unroll 1
        for (int s = 0; s < 7; ++s) {
            const unsigned slot = (s & 3) * 24576, nslot = ((s + 1) & 3) * 24576;
            SCHED_FENCE();
            frag_read(f1, slot + (xa ^ 64), slot + wa + 4096); frag_mma(f0, acc); interleave<2>();
            SCHED_FENCE();
            if (s == 1) VMWAIT(0);
            BAR();
            if (s == 1 && tid == 0) st32(rws, WS_FX1 + me * 4, ep);
            frag_read(f0, nslot + xa, nslot + wa); frag_mma(f1, acc); interleave<2>();
        }
        SCHED_FENCE();
        if (special) {
#pragma unroll
            for (int r = 0; r < 4; ++r) {
                const int ks = j1 + 16 * (c16 + 64 * (g & 1) + 16 * r), kn = (2048 - ks) & 2047;
                sp[0][r] = bf(p.wr[kn * 257]); sp[1][r] = bf(p.wi[kn * 257]);
                sp[2][r] = bf(p.wr[ks * 257 + 256]); sp[3][r] = bf(p.wi[ks * 257 + 256]);
                sp[4][r] = bf(p.wr[ks * 257 + 128]); sp[5][r] = bf(p.wi[ks * 257 + 128]);
            }
        }
        frag_read(f1, 3 * 24576 + (xa ^ 64), 3 * 24576 + wa + 4096); frag_mma(f0, acc); interleave<2>();
        SCHED_FENCE();
        frag_mma(f1, acc);
        const unsigned qb = WS_PQ + (((rp * 8 + hg) * 8 + 4 * rh) * 2 + hp) * 1024 + lane * 16;
#pragma unroll
        for (int i = 0; i < 4; ++i) st128(rws, qb + i * 2048, cat(phi4(acc[i][0]), phi4(acc[i][1])));
    }
    STAMP(3);
    const int lam = tid >> 3, s2a = (lam >> 5) * 32 + (lam & 3) * 8 + ((lam >> 2) & 7);
    unsigned sb = L_U + ((ch & 3) * 2 + (ch >> 2)) * 1040 + ((lam >> 5) * 16 + ((lam >> 2) & 7)) * 16 + (lam & 3) * 4;
    unsigned tb2 = WS_T1 + s2a * TB + (kb * 16 + j1) * 128 + ch * 16;
    PIN(sb); PIN(tb2);
    VMWAIT(0);
    BAR();
    if (tid == 0) st32(rws, WS_FPQ + (rp * 8 + hg) * 4, ep);
    const u32x4 i0 = ld128(rws, tb2), i1 = ld128s(rws, tb2, 64 * TB);
    unsigned isp = 0;
    if (kb == 0 && tid < 128) isp = ld32(rws, WS_T1 + tid * TB + ((16 - j1) & 15) * 128);
    FENCE();
    Cx a1, bz, a2, bv;
    {
        const int x = g >> 1, col = 2 * w + x;
        const float th = (float)(16 * kb + col) * (1.f / 512), sn = __builtin_amdgcn_sinf(th), cs = __builtin_amdgcn_cosf(th);
        const unsigned wp = L_W + (c16 + 64 * (g & 1)) * 40 + col * 2, wq = wp + 10240 + 30 - col * 4;
        f32x4 pr, pi, qr, qi;
#pragma unroll
        for (int r = 0; r < 4; ++r) { pr[r] = lrbf(wp + r * 640); pi[r] = lrbf(wp + r * 640 + 5120); qr[r] = lrbf(wq + r * 640); qi[r] = lrbf(wq + r * 640 + 5120); }
        qi = -qi;
        const bool sx = special && x == 0;
        if (special) {
#pragma unroll
            for (int r = 0; r < 4; ++r) if (sx) {
                pr[r] = 0.5f * (pr[r] + sp[0][r]); pi[r] = 0.5f * (pi[r] - sp[1][r]); qr[r] = 0.5f * (qr[r] + sp[2][r]); qi[r] = 0.5f * (qi[r] + sp[3][r]); }
        }
        const float sc = 1.f / 1048576;
        const f32x4 sr = (pr + qr) * sc, si = (pi + qi) * sc, dr = (pr - qr) * sc, di = (pi - qi) * sc;
        a1.re = sr - dr * sn; a1.im = si - di * sn; a2.re = sr + dr * sn; a2.im = si + di * sn;
        bz.re = dr * cs; bz.im = di * cs; bv = bz;
        if (special) {
#pragma unroll
            for (int r = 0; r < 4; ++r) if (sx) { a2.re[r] = 2.f * sc * sp[4][r]; a2.im[r] = 2.f * sc * sp[5][r]; bv.re[r] = 0.f; bv.im[r] = 0.f; }
        }
    }
    const unsigned ca = L_C + lane * 16;
    Mat F1 = {lr128(ca), lr128(ca + 1024)}, F2 = {lr128(ca + 2048), lr128(ca + 3072)}, G = {lr128(ca + 4096), lr128(ca + 5120)};
    Cx twa = {lrf128(ca + 6144), lrf128(ca + 7168)}, twb = {lrf128(ca + 8192), lrf128(ca + 9216)};
    unsigned ta = L_U + ((w >> 1) * 2 + 8 * (w & 1)) * 1040 + lane * 16;
    unsigned tb3 = WS_T2 + (kb * 16 + j1) * TB + s2a * 128 + ch * 16;
    PIN(a1.re); PIN(a1.im); PIN(a2.re); PIN(a2.im); PIN(bz.re); PIN(bz.im); PIN(bv.re); PIN(bv.im);
    PIN(F1.re); PIN(F1.im); PIN(F2.re); PIN(F2.im); PIN(G.re); PIN(G.im); PIN(twa.re); PIN(twa.im); PIN(twb.re); PIN(twb.im); PIN(ta); PIN(tb3);
    lw32(sb, i0[0]); lw32(sb + 128, i0[1]); lw32(sb + 8320, i0[2]); lw32(sb + 8320 + 128, i0[3]);
    lw32(sb + 512, i1[0]); lw32(sb + 512 + 128, i1[1]); lw32(sb + 512 + 8320, i1[2]); lw32(sb + 512 + 8320 + 128, i1[3]);
    if (kb == 0 && tid < 128) lw32(L_SP + ((tid >> 5) * 8 + (tid & 7)) * 16 + ((tid >> 3) & 3) * 4, isp ^ 0x80000000u);
    BAR();
    STAMP(4);
    {
        const u32x4 zi = lr128(ta), vi = lr128(ta + 1040);
        const Cx Z = dftB(twpk(dftA(zi, F1), twa), G), V = dftB(twpk(dftA(vi, F1), twa), G);
        Cx Vz = V;
        if (special) {
            u32x4 v0 = lr128(L_SP + (g * 8 + (c16 & 7)) * 16);
            if (c16 >= 8) v0 = vi;
            Vz = dftB(twpk(dftA(v0, F1), twa), G);
        }
        const u32x4 zc = zip(a1.re * Z.re - a1.im * Z.im - bz.re * Vz.im - bz.im * Vz.re, bz.im * Vz.im - a1.re * Z.im - a1.im * Z.re - bz.re * Vz.re);
        const u32x4 vc = zip(a2.re * V.re - a2.im * V.im + bv.re * Z.im + bv.im * Z.re, bv.re * Z.re - a2.re * V.im - a2.im * V.re - bv.im * Z.im);
        lw128(ta, pk4(dftB(twpk(dftA(zc, G), twb), F2))); lw128(ta + 1040, pk4c(dftB(twpk(dftA(vc, G), twb), F2)));
        BAR();
        const u32x4 o0 = {lr32(sb), lr32(sb + 128), lr32(sb + 8320), lr32(sb + 8320 + 128)};
        const u32x4 o1 = {lr32(sb + 512), lr32(sb + 512 + 128), lr32(sb + 512 + 8320), lr32(sb + 512 + 8320 + 128)};
        st128(rws, tb3, o0); st128(rws, tb3 + 64 * 128, o1);
    }
    STAMP(5);
    VMWAIT(0);
    BAR();
    if (tid == 0) st32(rws, WS_FX2 + me * 4, ep);
    if (w >= 4) {
        const rsrc_t ro = mkrsrc(p.wo, 512 * 512 * 2);
        const unsigned vo = (lw * 128 + r8) * 1024 + ct * 128 + ((ch ^ (r8 & 6)) << 4);
#pragma unroll 1
        for (int q = 0; q < 16; ++q) dma(ro, vo, q * 8192, lw * 16384 + q * 1024);
    }
    unsigned ra[4];
#pragma unroll
    for (int i = 0; i < 4; ++i) {
        const int km = 64 * g + 16 * i + c16, kq = km < 128 ? km : (256 - km) & 127;
        ra[i] = L_U + ((km >> 7) * 16 + 2 * w) * 592 + (kq + 16 * (kq >> 6)) * 4;
        PIN(ra[i]);
    }
    STAMP(6);
    if (w < 4) {
        const int set = g & 1, s1 = set ? (16 - c16) & 15 : c16, k0 = 16 * (2 * w + (g >> 1));
        unsigned u2 = L_U + (set * 16 + s1) * 592 + (k0 + 16 * (k0 >> 6)) * 4;
        unsigned tb = WS_T2 + ((2 * w + (c16 >> 3)) * 16 + 4 * g) * TB + me * 128 + ch * 16;
        PIN(u2); PIN(tb);
        wait_flags(rws, WS_FX2, ep, lane);
        STAMP(10);
        const u32x4 l0 = ld128(rws, tb), l1 = ld128s(rws, tb, TB), l2 = ld128s(rws, tb, 2 * TB), l3 = ld128s(rws, tb, 3 * TB);
        u32x4 t[4];
#pragma unroll
        for (int e = 0; e < 4; ++e) t[e] = pk4(dftA(u32x4{l0[e], l1[e], l2[e], l3[e]}, F));
#pragma unroll
        for (int r = 0; r < 4; ++r) lw128(u2 + r * 16, u32x4{t[0][r], t[1][r], t[2][r], t[3][r]});
    }
    BAR();
    STAMP(11);
    {
        const u32x4 d0 = {lr32(ra[0]), lr32(ra[1]), lr32(ra[2]), lr32(ra[3])};
        const u32x4 d1 = {lr32(ra[0] + 592), lr32(ra[1] + 592), lr32(ra[2] + 592), lr32(ra[3] + 592)};
        const u32x4 y0 = pk4c(dftB(twpk(dftA(d0, F), tw1), F)), y1 = pk4c(dftB(twpk(dftA(d1, F), tw1), F));
        unsigned* o = (unsigned*)(p.out + (256 * w + me) * 1024 + 512) + 64 * g + c16;
#pragma unroll
        for (int r = 0; r < 4; ++r) { o[16 * r] = y0[r]; o[16 * r + 128 * 512] = y1[r]; }
    }
    if (w >= 4) VMWAIT(8);
}

__global__ void __launch_bounds__(512, 2) fused_kernel(Params p) {
    const int b = blockIdx.x, xcd = b & 7, i = b >> 3, li = i & 15;
    const int tid = threadIdx.x, lane = tid & 63, w = __builtin_amdgcn_readfirstlane(tid >> 6);
    const int rt = xcd * 4 + (i & 3), ct = i >> 2;
    __shared__ __attribute__((aligned(16))) char smem[LDS_BYTES];
    asm volatile("" :: "s"((unsigned)(size_t)(LAS char*)smem));
    const rsrc_t rws = mkrsrc(p.ws, WS_END);
    const unsigned ep = *(const __attribute__((address_space(4))) unsigned*)(p.ws + WS_EPOCH) + 1;
    STAMP(0);
    unsigned wob;
    if (i < 16) { role_kv(p, rws, ep, xcd, li, tid, lane, w, ct STP_ARG); wob = 98304; }
    else        { role_qb(p, rws, ep, xcd, li, tid, lane, w, ct STP_ARG); wob = 0; }
    const int g = lane >> 4, c16 = lane & 15, q = c16 >> 2;
    unsigned ob = WS_O + (rt * 4 + w) * 16384 + lane * 16;
    unsigned wa = wob + (4 * g + q) * 128 + 8 * (lane & 3) + (((2 * g + (q >> 1)) & 3) << 5);
    PIN(ob); PIN(wa);
    BAR();
    STAMP(7);
    if (w < 4) {
        f32x4 acc[4];
#pragma unroll 1
        for (int it = 0; it < SPIN_CAP; ++it) {
            FENCE();
            u32x4 o[16];
#pragma unroll
            for (int k = 0; k < 16; ++k) o[k] = ld128s(rws, ob, k * 1024);
            unsigned m = 0;
#pragma unroll
            for (int t = 0; t < 4; ++t) acc[t] = Z4;
#pragma unroll
            for (int k = 0; k < 16; ++k) {
#pragma unroll
                for (int t = 0; t < 4; ++t) { const unsigned a = (wa ^ (t << 5)) + k * 4096; acc[t] = mma(ltr2(a, a + 2048), o[k], acc[t]); }
                m = umax4(m, o[k]);
            }
            if (!__any(m == POISON)) break;
        }
        STAMP(8);
        const unsigned sc = 65536 + w * 2304;
#pragma unroll
        for (int t = 0; t < 4; ++t) lw64(sc + c16 * 144 + t * 32 + g * 8, pkv(acc[t]));
        const int r8 = lane >> 3, ch = lane & 7;
        const u32x4 s0 = lr128(sc + r8 * 144 + ch * 16), s1 = lr128(sc + (r8 + 8) * 144 + ch * 16);
        u32x4* o = (u32x4*)(p.out + (rt * 64 + w * 16 + r8) * 1024 + ct * 64 + ch * 8);
        o[0] = s0; o[1024] = s1;
        if (b == 0 && tid == 0) st32(rws, WS_EPOCH, ep);
        STAMP(9);
    }
}

extern "C" void kernel_launch(void* const* d_in, const int* in_sizes, int n_in,
                              void* d_out, int out_size, void* d_ws, size_t ws_size,
                              hipStream_t stream) {
    Params p;
    p.ax = (const unsigned short*)d_in[0]; p.wqkv = (const unsigned short*)d_in[1]; p.wo = (const unsigned short*)d_in[2];
    p.bx = (const unsigned short*)d_in[3]; p.wr = (const unsigned short*)d_in[4]; p.wi = (const unsigned short*)d_in[5];
    p.out = (unsigned short*)d_out; p.ws = (unsigned char*)d_ws;
    fused_kernel<<<256, 512, 0, stream>>>(p);
}
```

```cpp
#define TCAL 4000
#include <hip/hip_runtime.h>
#include <stdint.h>

typedef short    s16x4 __attribute__((ext_vector_type(4)));
typedef __bf16   bf16x8 __attribute__((ext_vector_type(8)));
typedef __bf16   bf16x2 __attribute__((ext_vector_type(2)));
typedef float    f32x2 __attribute__((ext_vector_type(2)));
typedef float    f32x4 __attribute__((ext_vector_type(4)));
typedef unsigned u32x2 __attribute__((ext_vector_type(2)));
typedef unsigned u32x4 __attribute__((ext_vector_type(4)));
typedef __amdgpu_buffer_rsrc_t rsrc_t;
#define LAS __attribute__((address_space(3)))
#define DI __device__ __forceinline__

constexpr unsigned WS_EPOCH = 0, WS_FX1 = 1024, WS_FPQ = 2048, WS_FX2 = 3072;
constexpr unsigned PART_STRIDE = 2304, WS_PART = 4096;
constexpr unsigned TB = 16512;
constexpr unsigned WS_T1 = 1u << 19, WS_T2 = WS_T1 + 128 * TB;
constexpr unsigned WS_PQ = WS_T2 + 128 * TB, WS_O = WS_PQ + (1u << 21), WS_TS = WS_O + (1u << 21), WS_END = WS_TS + 4096;
constexpr unsigned POISON = 0xFFFFFFFFu;
constexpr int SPIN_CAP = 1 << 15;
constexpr unsigned LDS_BYTES = 163840;
#define STAMP(i) do {} while (0)
#define STP_PARAM
#define STP_ARG

struct Params {
    const unsigned short *ax, *wqkv, *wo, *bx, *wr, *wi;
    unsigned short* out;
    unsigned char* ws;
};

DI rsrc_t mkrsrc(const void* ptr, unsigned bytes) { return __builtin_amdgcn_make_buffer_rsrc((void*)ptr, 0, bytes, 0x00020000); }
DI u32x4 ld128(rsrc_t r, unsigned off)  { return __builtin_amdgcn_raw_buffer_load_b128(r, off, 0, 16); }
DI u32x2 ld64(rsrc_t r, unsigned off)   { return __builtin_amdgcn_raw_buffer_load_b64(r, off, 0, 16); }
DI unsigned ld32(rsrc_t r, unsigned off){ return __builtin_amdgcn_raw_buffer_load_b32(r, off, 0, 16); }
DI u32x4 ld128s(rsrc_t r, unsigned off, unsigned so)  { return __builtin_amdgcn_raw_buffer_load_b128(r, off, so, 16); }
DI unsigned ld32s(rsrc_t r, unsigned off, unsigned so){ return __builtin_amdgcn_raw_buffer_load_b32(r, off, so, 16); }
DI void st128(rsrc_t r, unsigned off, u32x4 v) { __builtin_amdgcn_raw_buffer_store_b128(v, r, off, 0, 16); }
DI void st64(rsrc_t r, unsigned off, u32x2 v) { __builtin_amdgcn_raw_buffer_store_b64(v, r, off, 0, 16); }
DI void st32(rsrc_t r, unsigned off, unsigned v) { __builtin_amdgcn_raw_buffer_store_b32(v, r, off, 0, 16); }
#define FENCE() asm volatile("" ::: "memory")
#define BAR() asm volatile("s_waitcnt lgkmcnt(0)\n\ts_barrier" ::: "memory")
#define VMWAIT(n) asm volatile("s_waitcnt vmcnt(" #n ")" ::: "memory")
#define PIN(x) asm volatile("" : "+v"(x))
#define SCHED_FENCE() __builtin_amdgcn_sched_barrier(0)
DI void dma(rsrc_t r, unsigned voff, unsigned soff, unsigned lds) {
    asm volatile("s_mov_b32 m0, %0\n\ts_nop 0\n\tbuffer_load_dwordx4 %1, %2, %3 offen lds"
                 :: "s"(lds), "v"(voff), "s"(r), "s"(soff) : "memory");
}
DI unsigned pk(float lo, float hi) { f32x2 v = {lo, hi}; return __builtin_bit_cast(unsigned, __builtin_convertvector(v, bf16x2)); }
DI u32x2 pkv(f32x4 v) { return u32x2{pk(v[0], v[1]), pk(v[2], v[3])}; }
DI u32x4 cat(u32x2 a, u32x2 b) { return u32x4{a[0], a[1], b[0], b[1]}; }
DI f32x4 mma(u32x4 a, u32x4 b, f32x4 c) {
    return __builtin_amdgcn_mfma_f32_16x16x32_bf16(__builtin_bit_cast(bf16x8, a), __builtin_bit_cast(bf16x8, b), c, 0, 0, 0); }
DI f32x4 mma16(u32x2 a, u32x2 b, f32x4 c) {
    return __builtin_amdgcn_mfma_f32_16x16x16bf16_1k(__builtin_bit_cast(s16x4, a), __builtin_bit_cast(s16x4, b), c, 0, 0, 0); }
#define LP(T, a) ((LAS T*)(size_t)(a))
DI u32x4 lr128(unsigned a) { return *LP(const u32x4, a); }
DI f32x4 lrf128(unsigned a) { return *LP(const f32x4, a); }
DI unsigned lr32(unsigned a) { return *LP(const unsigned, a); }
DI float bf(unsigned short v) { return __builtin_bit_cast(float, (unsigned)v << 16); }
DI float lrbf(unsigned a) { return bf(*LP(const unsigned short, a)); }
DI void lw128(unsigned a, u32x4 v) { *LP(u32x4, a) = v; }
DI void lwf128(unsigned a, f32x4 v) { *LP(f32x4, a) = v; }
DI void lw64(unsigned a, u32x2 v) { *LP(u32x2, a) = v; }
DI void lw32(unsigned a, unsigned v) { *LP(unsigned, a) = v; }
DI u32x2 ltr(unsigned a) { return __builtin_bit_cast(u32x2, __builtin_amdgcn_ds_read_tr16_b64_v4i16(LP(s16x4, a))); }
DI u32x4 ltr2(unsigned a, unsigned b) { return cat(ltr(a), ltr(b)); }
DI unsigned umax3(unsigned a, unsigned b, unsigned c) { return max(max(a, b), c); }
DI unsigned umax4(unsigned m, u32x4 v) { return umax3(umax3(m, v[0], v[1]), v[2], v[3]); }
DI u32x2 phi4(f32x4 t) {
    f32x4 e, m;
#pragma unroll
    for (int r = 0; r < 4; ++r) {
        e[r] = __builtin_amdgcn_fmed3f(__builtin_amdgcn_exp2f(t[r] * 1.44269504f), 0.f, 1.f);
        m[r] = __builtin_elementwise_maximum(t[r], 0.f);
    }
    return pkv((m + e) + 1e-6f);
}
constexpr f32x4 Z4 = {0.f, 0.f, 0.f, 0.f};

struct Mat { u32x4 re, im; };
struct Cx  { f32x4 re, im; };
DI Cx angles(int n0, int dn, int mask, float inv) {
    Cx t;
#pragma unroll
    for (int r = 0; r < 4; ++r) { float a = (float)((n0 + r * dn) & mask) * inv;
        t.re[r] = __builtin_amdgcn_cosf(a); t.im[r] = __builtin_amdgcn_sinf(a); }
    return t;
}
DI u32x4 zip(f32x4 a, f32x4 b) { return u32x4{pk(a[0], b[0]), pk(a[1], b[1]), pk(a[2], b[2]), pk(a[3], b[3])}; }
DI Mat tomat(Cx t) { return Mat{zip(t.re, t.im), zip(-t.im, t.re)}; }
DI Cx dftA(u32x4 d, const Mat& f) { return Cx{mma(d, f.re, Z4), mma(d, f.im, Z4)}; }
DI Cx dftB(u32x4 d, const Mat& f) { return Cx{mma(f.re, d, Z4), mma(f.im, d, Z4)}; }
DI u32x4 twpk(Cx v, const Cx& w) { return zip(v.re * w.re + v.im * w.im, v.im * w.re - v.re * w.im); }
DI u32x4 pk4(Cx v)  { return zip(v.re, v.im); }
DI u32x4 pk4c(Cx v) { return zip(v.re, v.im) ^ 0x80000000u; }

DI void wait_flags(rsrc_t rws, unsigned base, unsigned ep, int lane) {
#pragma unroll 1
    for (int it = 0; it < SPIN_CAP; ++it) {
        FENCE();
        u32x2 f = ld64(rws, base + lane * 8);
        if (__all(f[0] == ep && f[1] == ep)) break;
        __builtin_amdgcn_s_sleep(2);
    }
    FENCE();
}
DI void dma_x(rsrc_t rx, unsigned v0, unsigned v1, unsigned soff, unsigned lds) {
    dma(rx, v0, soff, lds); dma(rx, v1, soff + 8192, lds + 1024); dma(rx, v0, soff + 16384, lds + 2048); dma(rx, v1, soff + 24576, lds + 3072);
}
template <int NB> struct Frag { u32x4 a[4], b[NB]; };
template <int NB> DI void frag_read(Frag<NB>& f, unsigned xa, unsigned w0) {
    const unsigned w1 = w0 ^ 32;
    f.a[0] = lr128(xa); f.b[0] = ltr2(w0, w0 + 512);
    f.a[1] = lr128(xa + 2048); f.a[2] = lr128(xa + 4096); f.a[3] = lr128(xa + 6144);
    f.b[1] = ltr2(w1, w1 + 512);
    if constexpr (NB == 4) { f.b[2] = ltr2(w0 + 8192, w0 + 8704); f.b[3] = ltr2(w1 + 8192, w1 + 8704); }
}
template <int NB> DI void frag_mma(const Frag<NB>& f, f32x4 (&acc)[4][NB]) {
#pragma unroll
    for (int j = 0; j < NB; ++j)
#pragma unroll
        for (int i = 0; i < 4; ++i) acc[i][j] = (NB == 4) ? mma(f.a[i], f.b[j], acc[i][j]) : mma(f.b[j], f.a[i], acc[i][j]);
}
template <int NB> DI void interleave() {
#pragma unroll
    for (int i = 0; i < 2 + NB; ++i) { __builtin_amdgcn_sched_group_barrier(0x8, 1, 0); __builtin_amdgcn_sched_group_barrier(0x100, 2, 0); }
    __builtin_amdgcn_sched_group_barrier(0x8, 4 * NB, 0);
}

DI void role_kv(const Params& p, rsrc_t rws, unsigned ep, int xcd, int li, int tid, int lane, int w, int ct STP_PARAM) {
    const int rp = (xcd >> 1) * 4 + (li & 3), hg = (xcd & 1) * 4 + (li >> 2);
    const int rot = 2 * (li >> 2) + 2 * (li & 3);
    const int g = lane >> 4, c16 = lane & 15;
    if (w >= 4) {
        const int lw = w - 4, r8 = lane >> 3, ch = lane & 7;
        const rsrc_t rx = mkrsrc(p.ax, 2048 * 512 * 2), rw = mkrsrc(p.wqkv, 512 * 1536 * 2), ro = mkrsrc(p.wo, 512 * 512 * 2);
        const unsigned xrow = (rp * 128 + lw * 32 + r8) * 1024;
        const unsigned vx0 = xrow + ((ch ^ (r8 >> 1)) << 4), vx1 = vx0 ^ 64;
        const unsigned vw0 = (lw * 16 + r8) * 3072 + hg * 128 + 1024 + ((ch ^ (r8 & 2)) << 4), vw1 = (vw0 ^ 64) + 8 * 3072;
        const unsigned vo = (lw * 64 + r8) * 1024 + ct * 128 + ((ch ^ (r8 & 6)) << 4);
#pragma unroll 1
        for (int s = -4; s < 8; ++s) {
            if (s >= 0) { if (s < 7) VMWAIT(24); else VMWAIT(16); BAR(); }
            if (s < 4) {
                const unsigned kb = (s + 4 + rot) & 7, slot = (s > 0 ? s - 1 : s + 4) * 32768, so = kb * (64 * 3072);
                dma_x(rx, vx0, vx1, kb * 128, slot + lw * 4096);
                dma(rw, vw0, so, slot + 16384 + lw * 2048); dma(rw, vw1, so, slot + 16384 + lw * 2048 + 1024);
                dma(rw, vw0, so + 1024, slot + 24576 + lw * 2048); dma(rw, vw1, so + 1024, slot + 24576 + lw * 2048 + 1024);
            } else if (s < 6) {
#pragma unroll 1
                for (int q = 0; q < 8; ++q) dma(ro, vo, (s - 4) * 262144 + q * 8192, 98304 + (s - 4) * 32768 + lw * 8192 + q * 1024);
            }
        }
        STAMP(1);
        BAR();
    } else {
        const int rh = w >> 1, hp = w & 1;
        f32x4 acc[4][4];
#pragma unroll
        for (int i = 0; i < 4; ++i)
#pragma unroll
            for (int j = 0; j < 4; ++j) { acc[i][j] = Z4; PIN(acc[i][j]); }
        const unsigned xa = (rh * 64 + c16) * 128 + ((g ^ (c16 >> 1)) << 4);
        const int q = c16 >> 2, fk = (q >> 1) | ((g & 1) << 1);
        const unsigned wa = 16384 + (8 * g + q) * 128 + (((2 * hp) ^ fk) << 5) + 8 * (lane & 3);
        Frag<4> f0, f1;
        unsigned slot = 0;
        BAR();
        frag_read(f0, xa, wa);
#pragma unroll 1
        for (int s = 0; s < 7; ++s) {
            SCHED_FENCE();
            frag_read(f1, slot + (xa ^ 64), slot + wa + 4096); frag_mma(f0, acc); interleave<4>();
            slot = (slot == 4 * 32768) ? 0 : slot + 32768;
            SCHED_FENCE(); BAR();
            frag_read(f0, slot + xa, slot + wa); frag_mma(f1, acc); interleave<4>();
        }
        SCHED_FENCE();
        u32x2 fx = {ep, ep};
        if (w == 2) fx = ld64(rws, WS_FX1 + lane * 8);
        frag_read(f1, slot + (xa ^ 64), slot + wa + 4096); frag_mma(f0, acc); interleave<4>();
        SCHED_FENCE();
        frag_mma(f1, acc);
        STAMP(1);
        f32x4 kv[2] = {Z4, Z4}, zz[2] = {Z4, Z4};
        const u32x4 ones = {0x3F803F80u, 0x3F803F80u, 0x3F803F80u, 0x3F803F80u};
#pragma unroll
        for (int b = 0; b < 2; ++b)
#pragma unroll
            for (int u = 0; u < 2; ++u) {
                const u32x4 A = cat(phi4(acc[2 * u][b]), phi4(acc[2 * u + 1][b])), B = cat(pkv(acc[2 * u][2 + b]), pkv(acc[2 * u + 1][2 + b]));
                kv[b] = mma(A, B, kv[b]); zz[b] = mma(A, ones, zz[b]);
            }
        const unsigned red = hp * 4096 + lane * 16;
        if (rh) { lwf128(red, kv[0]); lwf128(red + 1024, kv[1]); lwf128(red + 2048, zz[0]); lwf128(red + 3072, zz[1]); }
        if (!__all(fx[0] == ep && fx[1] == ep)) wait_flags(rws, WS_FX1, ep, lane);
        BAR();
        if (!rh) {
            kv[0] += lrf128(red); kv[1] += lrf128(red + 1024); zz[0] += lrf128(red + 2048); zz[1] += lrf128(red + 3072);
            const unsigned pb = WS_PART + (rp * 8 + hg) * PART_STRIDE;
            st128(rws, pb + hp * 1024 + lane * 16, cat(pkv(kv[0]), pkv(kv[1])));
            if (c16 == 0) { const unsigned zb = pb + 2048 + hp * 64 + g * 8; st64(rws, zb, pkv(zz[0])); st64(rws, zb + 32, pkv(zz[1])); }
        }
    }
    STAMP(2);
    if (w == 3) {
#pragma unroll 1
        for (int it = 0; it < SPIN_CAP; ++it) {
            FENCE();
            if (__builtin_amdgcn_readfirstlane(ld32(rws, WS_FPQ + (rp * 8 + hg) * 4)) == ep) break;
            __builtin_amdgcn_s_sleep(2);
        }
    }
    const int rg = w >> 1, hp = w & 1;
    unsigned pqb = WS_PQ + (((rp * 8 + hg) * 8 + 2 * rg) * 2 + hp) * 1024 + lane * 16;
    unsigned pb = WS_PART + hg * PART_STRIDE + tid * 4, pz = WS_PART + hg * PART_STRIDE + (tid >> 5) * 8 * PART_STRIDE + 2048 + (tid & 31) * 4;
    unsigned za = 32768 + 2048 + (4 * g + (c16 >> 2)) * 128 + hp * 64 + 8 * (lane & 3);
    unsigned ob = WS_O + ((rp * 8 + 2 * rg) * 16 + hg * 2 + hp) * 1024 + lane * 16;
    PIN(pqb); PIN(pb); PIN(pz); PIN(za); PIN(ob);
    if (w < 2) VMWAIT(0);
    BAR();
    STAMP(3);
    unsigned d[16], zr;
    u32x4 pq0, pq1;
#pragma unroll 1
    for (int it = 0; it < SPIN_CAP; ++it) {
        FENCE();
#pragma unroll
        for (int j = 0; j < 16; ++j) d[j] = ld32s(rws, pb, j * 8 * PART_STRIDE);
        zr = ld32(rws, pz);
        pq0 = ld128(rws, pqb); pq1 = ld128(rws, pqb + 2048);
        unsigned m = zr;
#pragma unroll
        for (int j = 0; j < 16; j += 2) m = umax3(m, d[j], d[j + 1]);
        if (!__any(m == POISON)) break;
    }
    f32x2 sm = {0.f, 0.f};
#pragma unroll
    for (int j = 0; j < 16; ++j) sm += f32x2{__builtin_bit_cast(float, d[j] << 16), __builtin_bit_cast(float, d[j] & 0xFFFF0000u)};
    lw32(32768 + tid * 4, pk(sm[0], sm[1])); lw32(32768 + 2048 + tid * 4, zr);
    BAR();
    STAMP(4);
    {
        const u32x4 kvA = lr128(32768 + hp * 1024 + lane * 16);
        const u32x2 one2 = {0x3F803F80u, 0x3F803F80u};
        const u32x2 zA[2] = {pkv(mma16(ltr(za), one2, Z4)), pkv(mma16(ltr(za + 32), one2, Z4))};
#pragma unroll
        for (int a = 0; a < 2; ++a) {
            const u32x4 pq = a ? pq1 : pq0;
            u32x2 o[2];
#pragma unroll
            for (int b = 0; b < 2; ++b) {
                const u32x2 B = {pq[2 * b], pq[2 * b + 1]};
                const f32x4 num = mma16(u32x2{kvA[2 * b], kvA[2 * b + 1]}, B, Z4), den = mma16(zA[b], B, Z4);
                o[b] = pkv(num * __builtin_amdgcn_rcpf(den[0] + 1e-6f));
            }
            st128(rws, ob + a * 16384, cat(o[0], o[1]));
        }
    }
    STAMP(5);
    VMWAIT(0);
}

constexpr unsigned L_U = 98304;
constexpr unsigned L_W = L_U + 19200;
constexpr unsigned L_SP = L_W + 20480;
constexpr unsigned L_C = L_SP + 512;
DI void role_qb(const Params& p, rsrc_t rws, unsigned ep, int xcd, int li, int tid, int lane, int w, int ct STP_PARAM) {
    const int rp = (xcd >> 1) * 4 + (li & 3), hg = (xcd & 1) * 4 + (li >> 2);
    const int rot = 2 * (li >> 2) + 2 * (li & 3) + 1;
    const int g = lane >> 4, c16 = lane & 15;
    const int me = xcd * 16 + li;
    const int jh = li >> 3, j1 = jh ? (xcd ? 16 - xcd : 8) : xcd, kb = li & 7;
    const int lw = w - 4, r8 = lane >> 3, ch = lane & 7;
    u32x4 x0, x1;
    {
        const rsrc_t rb = mkrsrc(p.bx, 2048 * 512 * 2);
        const unsigned vo = (256 * w + me) * 1024 + g * 256 + c16 * 4;
        asm volatile("buffer_load_dword %0, %8, %9, 0 offen\n\tbuffer_load_dword %1, %8, %9, 0 offen offset:64\n\t"
                     "buffer_load_dword %2, %8, %9, 0 offen offset:128\n\tbuffer_load_dword %3, %8, %9, 0 offen offset:192\n\t"
                     "buffer_load_dword %4, %8, %9, %10 offen\n\tbuffer_load_dword %5, %8, %9, %10 offen offset:64\n\t"
                     "buffer_load_dword %6, %8, %9, %10 offen offset:128\n\tbuffer_load_dword %7, %8, %9, %10 offen offset:192"
                     : "=&v"(x0[0]), "=&v"(x0[1]), "=&v"(x0[2]), "=&v"(x0[3]), "=&v"(x1[0]), "=&v"(x1[1]), "=&v"(x1[2]), "=&v"(x1[3])
                     : "v"(vo), "s"(rb), "s"(131072u) : "memory");
    }
    const rsrc_t rx = mkrsrc(p.ax, 2048 * 512 * 2), rw = mkrsrc(p.wqkv, 512 * 1536 * 2);
    const unsigned vx0 = (rp * 128 + lw * 32 + r8) * 1024 + ((ch ^ (r8 >> 1)) << 4), vx1 = vx0 ^ 64;
    const unsigned vw0 = (lw * 16 + r8) * 3072 + hg * 128 + ((ch ^ (r8 & 2)) << 4), vw1 = (vw0 ^ 64) + 8 * 3072;
    auto stage = [&](int s) {
        const unsigned kq = (s + rot) & 7, slot = (s & 3) * 24576;
        dma_x(rx, vx0, vx1, kq * 128, slot + lw * 4096);
        dma(rw, vw0, kq * (64 * 3072), slot + 16384 + lw * 2048); dma(rw, vw1, kq * (64 * 3072), slot + 16384 + lw * 2048 + 1024);
    };
    if (w >= 4) {
#pragma unroll 1
        for (int s = 0; s < 2; ++s) stage(s);
    }
    if (w < 5) {
        const int k = w, k4 = 4 * k, sU = (0x04004 >> k4) & 15, sQ = (0x40040 >> k4) & 15;
        const int P = (((c16 & ((0x7F7FF >> k4) & 15)) << sU) + (sU ? j1 : 0)) << ((0x00833 >> k4) & 15);
        const int n0 = P * (((4 * (g & ((0x31133 >> k4) & 15))) << sQ) + (sQ ? j1 : 0)), dn = P << sQ;
        const Cx t = angles(n0, dn, 2047, 1.f / 2048);
        Mat m = tomat(t);
        if (k > 2) m = Mat{__builtin_bit_cast(u32x4, t.re), __builtin_bit_cast(u32x4, t.im)};
        if (k == 2 && (g >> 1) != (c16 >> 3)) m = Mat{u32x4{0, 0, 0, 0}, u32x4{0, 0, 0, 0}};
        lw128(L_C + k * 2048 + lane * 16, m.re); lw128(L_C + k * 2048 + 1024 + lane * 16, m.im);
    }
    Mat F = tomat(angles(4 * g * c16, c16, 15, 1.f / 16));
    Cx tw1 = angles(4 * g * c16, c16, 255, 1.f / 256);
    const int km0 = 16 * w + c16, km1 = km0 ? 256 - km0 : 128;
    unsigned ua = L_U + (2 * w) * 1072 + g * 272 + c16 * 16;
    unsigned a0 = L_U + 4 * g * 1072 + ((km0 >> 2) & 3) * 272 + (km0 >> 4) * 16 + (km0 & 3) * 4;
    unsigned a1s = L_U + 4 * g * 1072 + ((km1 >> 2) & 3) * 272 + (km1 >> 4) * 16 + (km1 & 3) * 4;
    unsigned sw0 = L_W + w * 2304 + c16 * 144 + g * 16, sw1 = L_W + w * 2304 + ((16 - c16) & 15) * 144 + 64 + g * 16;
    unsigned sr0 = L_W + w * 2304 + r8 * 144 + ch * 16;
    unsigned tb1 = WS_T1 + me * TB + w * 2048 + lane * 16, obp = WS_O + ((rp * 8 + w) * 16 + hg * 2) * 1024 + lane * 16;
    PIN(F.re); PIN(F.im); PIN(tw1.re); PIN(tw1.im); PIN(ua); PIN(a0); PIN(a1s); PIN(sw0); PIN(sw1); PIN(sr0); PIN(tb1); PIN(obp);
    asm volatile("s_cmp_lt_u32 %2, 4\n\ts_cbranch_scc1 1f\n\ts_waitcnt vmcnt(12)\n\ts_branch 2f\n1:\n\ts_waitcnt vmcnt(0)\n2:"
                 : "+v"(x0), "+v"(x1) : "s"(w) : "memory", "scc");
    STAMP(1);
    {
        lw128(ua, pk4(dftA(twpk(dftA(x0, F), tw1), F))); lw128(ua + 1072, pk4(dftA(twpk(dftA(x1, F), tw1), F)));
        BAR();
        const u32x4 d0 = {lr32(a0), lr32(a0 + 1072), lr32(a0 + 2144), lr32(a0 + 3216)};
        const u32x4 d1 = {lr32(a1s), lr32(a1s + 1072), lr32(a1s + 2144), lr32(a1s + 3216)};
        lw128(sw0, pk4(dftA(d0, F))); lw128(sw1, pk4c(dftA(d1, F)));
        const u32x4 s0 = lr128(sr0), s1 = lr128(sr0 + 8 * 144);
        st128(rws, tb1, s0); st128(rws, tb1 + 1024, s1);
        const u32x4 P4 = {POISON, POISON, POISON, POISON};
        st128(rws, obp, P4); st128(rws, obp + 1024, P4);
        if (tid < 136) st128(rws, WS_PART + (rp * 8 + hg) * PART_STRIDE + tid * 16, P4);
    }
    STAMP(2);
    const bool special = (kb == 0 && w == 0);
    float sp[6][4];
    if (w >= 4) {
#pragma unroll 1
        for (int s = -2; s < 5; ++s) {
            if (s >= 0) { if (s == 0) VMWAIT(22); else if (s == 1) VMWAIT(16); else VMWAIT(12); BAR(); }
            if (s != 0) stage(s < 0 ? s + 4 : s + 3);
        }
        struct __attribute__((packed, aligned(2))) U4 { u32x4 v; };
        const int j2 = (tid >> 1) & 127, ks = j1 + 16 * j2, hf = (tid & 1) * 8;
        const int op = ks * 257 + 16 * kb + hf, oq = ((2048 - ks) & 2047) * 257 + 241 - 16 * kb + hf;
        const u32x4 l0 = ((const U4*)(p.wr + op))->v, l1 = ((const U4*)(p.wi + op))->v, l2 = ((const U4*)(p.wr + oq))->v, l3 = ((const U4*)(p.wi + oq))->v;
        FENCE();
        VMWAIT(16); BAR(); VMWAIT(10); BAR(); VMWAIT(4); BAR();
        const unsigned wa = L_W + j2 * 40 + (tid & 1) * 16;
        lw64(wa, u32x2{l0[0], l0[1]}); lw64(wa + 8, u32x2{l0[2], l0[3]});
        lw64(wa + 5120, u32x2{l1[0], l1[1]}); lw64(wa + 5128, u32x2{l1[2], l1[3]});
        lw64(wa + 10240, u32x2{l2[0], l2[1]}); lw64(wa + 10248, u32x2{l2[2], l2[3]});
        lw64(wa + 15360, u32x2{l3[0], l3[1]}); lw64(wa + 15368, u32x2{l3[2], l3[3]});
        if (w == 7) wait_flags(rws, WS_FX1, ep, lane);
    } else {
        const int rh = w >> 1, hp = w & 1;
        f32x4 acc[4][2];
#pragma unroll
        for (int i = 0; i < 4; ++i) { acc[i][0] = Z4; acc[i][1] = Z4; PIN(acc[i][0]); PIN(acc[i][1]); }
        const unsigned xa = (rh * 64 + c16) * 128 + ((g ^ (c16 >> 1)) << 4);
        const int q = c16 >> 2, fk = (q >> 1) | ((g & 1) << 1);
        const unsigned wa = 16384 + (8 * g + q) * 128 + (((2 * hp) ^ fk) << 5) + 8 * (lane & 3);
        Frag<2> f0, f1;
        BAR();
        frag_read(f0, xa, wa);
#pragma unroll 1
        for (int s = 0; s < 7; ++s) {
            const unsigned slot = (s & 3) * 24576, nslot = ((s + 1) & 3) * 24576;
            SCHED_FENCE();
            frag_read(f1, slot + (xa ^ 64), slot + wa + 4096); frag_mma(f0, acc); interleave<2>();
            SCHED_FENCE();
            if (s == 1) VMWAIT(0);
            BAR();
            if (s == 1 && tid == 0) st32(rws, WS_FX1 + me * 4, ep);
            frag_read(f0, nslot + xa, nslot + wa); frag_mma(f1, acc); interleave<2>();
        }
        SCHED_FENCE();
        if (special) {
#pragma unroll
            for (int r = 0; r < 4; ++r) {
                const int ks = j1 + 16 * (c16 + 64 * (g & 1) + 16 * r), kn = (2048 - ks) & 2047;
                sp[0][r] = bf(p.wr[kn * 257]); sp[1][r] = bf(p.wi[kn * 257]);
                sp[2][r] = bf(p.wr[ks * 257 + 256]); sp[3][r] = bf(p.wi[ks * 257 + 256]);
                sp[4][r] = bf(p.wr[ks * 257 + 128]); sp[5][r] = bf(p.wi[ks * 257 + 128]);
            }
        }
        frag_read(f1, 3 * 24576 + (xa ^ 64), 3 * 24576 + wa + 4096); frag_mma(f0, acc); interleave<2>();
        SCHED_FENCE();
        frag_mma(f1, acc);
        const unsigned qb = WS_PQ + (((rp * 8 + hg) * 8 + 4 * rh) * 2 + hp) * 1024 + lane * 16;
#pragma unroll
        for (int i = 0; i < 4; ++i) st128(rws, qb + i * 2048, cat(phi4(acc[i][0]), phi4(acc[i][1])));
    }
    STAMP(3);
    const int lam = tid >> 3, s2a = (lam >> 5) * 32 + (lam & 3) * 8 + ((lam >> 2) & 7);
    unsigned sb = L_U + ((ch & 3) * 2 + (ch >> 2)) * 1040 + ((lam >> 5) * 16 + ((lam >> 2) & 7)) * 16 + (lam & 3) * 4;
    unsigned tb2 = WS_T1 + s2a * TB + (kb * 16 + j1) * 128 + ch * 16;
    PIN(sb); PIN(tb2);
    VMWAIT(0);
    BAR();
    if (tid == 0) st32(rws, WS_FPQ + (rp * 8 + hg) * 4, ep);
    const u32x4 i0 = ld128(rws, tb2), i1 = ld128s(rws, tb2, 64 * TB);
    unsigned isp = 0;
    if (kb == 0 && tid < 128) isp = ld32(rws, WS_T1 + tid * TB + ((16 - j1) & 15) * 128);
    FENCE();
    Cx a1, bz, a2, bv;
    {
        const int x = g >> 1, col = 2 * w + x;
        const float th = (float)(16 * kb + col) * (1.f / 512), sn = __builtin_amdgcn_sinf(th), cs = __builtin_amdgcn_cosf(th);
        const unsigned wp = L_W + (c16 + 64 * (g & 1)) * 40 + col * 2, wq = wp + 10240 + 30 - col * 4;
        f32x4 pr, pi, qr, qi;
#pragma unroll
        for (int r = 0; r < 4; ++r) { pr[r] = lrbf(wp + r * 640); pi[r] = lrbf(wp + r * 640 + 5120); qr[r] = lrbf(wq + r * 640); qi[r] = lrbf(wq + r * 640 + 5120); }
        qi = -qi;
        const bool sx = special && x == 0;
        if (special) {
#pragma unroll
            for (int r = 0; r < 4; ++r) if (sx) {
                pr[r] = 0.5f * (pr[r] + sp[0][r]); pi[r] = 0.5f * (pi[r] - sp[1][r]); qr[r] = 0.5f * (qr[r] + sp[2][r]); qi[r] = 0.5f * (qi[r] + sp[3][r]); }
        }
        const float sc = 1.f / 1048576;
        const f32x4 sr = (pr + qr) * sc, si = (pi + qi) * sc, dr = (pr - qr) * sc, di = (pi - qi) * sc;
        a1.re = sr - dr * sn; a1.im = si - di * sn; a2.re = sr + dr * sn; a2.im = si + di * sn;
        bz.re = dr * cs; bz.im = di * cs; bv = bz;
        if (special) {
#pragma unroll
            for (int r = 0; r < 4; ++r) if (sx) { a2.re[r] = 2.f * sc * sp[4][r]; a2.im[r] = 2.f * sc * sp[5][r]; bv.re[r] = 0.f; bv.im[r] = 0.f; }
        }
    }
    const unsigned ca = L_C + lane * 16;
    Mat F1 = {lr128(ca), lr128(ca + 1024)}, F2 = {lr128(ca + 2048), lr128(ca + 3072)}, G = {lr128(ca + 4096), lr128(ca + 5120)};
    Cx twa = {lrf128(ca + 6144), lrf128(ca + 7168)}, twb = {lrf128(ca + 8192), lrf128(ca + 9216)};
    unsigned ta = L_U + ((w >> 1) * 2 + 8 * (w & 1)) * 1040 + lane * 16;
    unsigned tb3 = WS_T2 + (kb * 16 + j1) * TB + s2a * 128 + ch * 16;
    PIN(a1.re); PIN(a1.im); PIN(a2.re); PIN(a2.im); PIN(bz.re); PIN(bz.im); PIN(bv.re); PIN(bv.im);
    PIN(F1.re); PIN(F1.im); PIN(F2.re); PIN(F2.im); PIN(G.re); PIN(G.im); PIN(twa.re); PIN(twa.im); PIN(twb.re); PIN(twb.im); PIN(ta); PIN(tb3);
    lw32(sb, i0[0]); lw32(sb + 128, i0[1]); lw32(sb + 8320, i0[2]); lw32(sb + 8320 + 128, i0[3]);
    lw32(sb + 512, i1[0]); lw32(sb + 512 + 128, i1[1]); lw32(sb + 512 + 8320, i1[2]); lw32(sb + 512 + 8320 + 128, i1[3]);
    if (kb == 0 && tid < 128) lw32(L_SP + ((tid >> 5) * 8 + (tid & 7)) * 16 + ((tid >> 3) & 3) * 4, isp ^ 0x80000000u);
    BAR();
    STAMP(4);
    {
        const u32x4 zi = lr128(ta), vi = lr128(ta + 1040);
        const Cx Z = dftB(twpk(dftA(zi, F1), twa), G), V = dftB(twpk(dftA(vi, F1), twa), G);
        Cx Vz = V;
        if (special) {
            u32x4 v0 = lr128(L_SP + (g * 8 + (c16 & 7)) * 16);
            if (c16 >= 8) v0 = vi;
            Vz = dftB(twpk(dftA(v0, F1), twa), G);
        }
        const u32x4 zc = zip(a1.re * Z.re - a1.im * Z.im - bz.re * Vz.im - bz.im * Vz.re, bz.im * Vz.im - a1.re * Z.im - a1.im * Z.re - bz.re * Vz.re);
        const u32x4 vc = zip(a2.re * V.re - a2.im * V.im + bv.re * Z.im + bv.im * Z.re, bv.re * Z.re - a2.re * V.im - a2.im * V.re - bv.im * Z.im);
        lw128(ta, pk4(dftB(twpk(dftA(zc, G), twb), F2))); lw128(ta + 1040, pk4c(dftB(twpk(dftA(vc, G), twb), F2)));
        BAR();
        const u32x4 o0 = {lr32(sb), lr32(sb + 128), lr32(sb + 8320), lr32(sb + 8320 + 128)};
        const u32x4 o1 = {lr32(sb + 512), lr32(sb + 512 + 128), lr32(sb + 512 + 8320), lr32(sb + 512 + 8320 + 128)};
        st128(rws, tb3, o0); st128(rws, tb3 + 64 * 128, o1);
    }
    STAMP(5);
    VMWAIT(0);
    BAR();
    if (tid == 0) st32(rws, WS_FX2 + me * 4, ep);
    if (w >= 4) {
        const rsrc_t ro = mkrsrc(p.wo, 512 * 512 * 2);
        const unsigned vo = (lw * 128 + r8) * 1024 + ct * 128 + ((ch ^ (r8 & 6)) << 4);
#pragma unroll 1
        for (int q = 0; q < 16; ++q) dma(ro, vo, q * 8192, lw * 16384 + q * 1024);
    }
    unsigned ra[4];
#pragma unroll
    for (int i = 0; i < 4; ++i) {
        const int km = 64 * g + 16 * i + c16, kq = km < 128 ? km : (256 - km) & 127;
        ra[i] = L_U + ((km >> 7) * 16 + 2 * w) * 592 + (kq + 16 * (kq >> 6)) * 4;
        PIN(ra[i]);
    }
    STAMP(6);
    if (w < 4) {
        const int set = g & 1, s1 = set ? (16 - c16) & 15 : c16, k0 = 16 * (2 * w + (g >> 1));
        unsigned u2 = L_U + (set * 16 + s1) * 592 + (k0 + 16 * (k0 >> 6)) * 4;
        unsigned tb = WS_T2 + ((2 * w + (c16 >> 3)) * 16 + 4 * g) * TB + me * 128 + ch * 16;
        PIN(u2); PIN(tb);
        wait_flags(rws, WS_FX2, ep, lane);
        STAMP(10);
        const u32x4 l0 = ld128(rws, tb), l1 = ld128s(rws, tb, TB), l2 = ld128s(rws, tb, 2 * TB), l3 = ld128s(rws, tb, 3 * TB);
        u32x4 t[4];
#pragma unroll
        for (int e = 0; e < 4; ++e) t[e] = pk4(dftA(u32x4{l0[e], l1[e], l2[e], l3[e]}, F));
#pragma unroll
        for (int r = 0; r < 4; ++r) lw128(u2 + r * 16, u32x4{t[0][r], t[1][r], t[2][r], t[3][r]});
    }
    BAR();
    STAMP(11);
    {
        const u32x4 d0 = {lr32(ra[0]), lr32(ra[1]), lr32(ra[2]), lr32(ra[3])};
        const u32x4 d1 = {lr32(ra[0] + 592), lr32(ra[1] + 592), lr32(ra[2] + 592), lr32(ra[3] + 592)};
        const u32x4 y0 = pk4c(dftB(twpk(dftA(d0, F), tw1), F)), y1 = pk4c(dftB(twpk(dftA(d1, F), tw1), F));
        unsigned* o = (unsigned*)(p.out + (256 * w + me) * 1024 + 512) + 64 * g + c16;
#pragma unroll
        for (int r = 0; r < 4; ++r) { __hip_atomic_store(o + 16 * r, y0[r], __ATOMIC_RELAXED, __HIP_MEMORY_SCOPE_AGENT); __hip_atomic_store(o + 16 * r + 128 * 512, y1[r], __ATOMIC_RELAXED, __HIP_MEMORY_SCOPE_AGENT); }
    }
    if (w >= 4) VMWAIT(8);
}

__global__ void __launch_bounds__(512, 2) fused_kernel(Params p) {
    const int b = blockIdx.x, xcd = b & 7, i = b >> 3, li = i & 15;
    const int tid = threadIdx.x, lane = tid & 63, w = __builtin_amdgcn_readfirstlane(tid >> 6);
    const int rt = xcd * 4 + (i & 3), ct = i >> 2;
    __shared__ __attribute__((aligned(16))) char smem[LDS_BYTES];
    asm volatile("" :: "s"((unsigned)(size_t)(LAS char*)smem));
    const rsrc_t rws = mkrsrc(p.ws, WS_END);
    const unsigned ep = *(const __attribute__((address_space(4))) unsigned*)(p.ws + WS_EPOCH) + 1;
    { const unsigned t0 = (unsigned)__builtin_amdgcn_s_memrealtime(); if (tid == 0) st32(rws, WS_TS + b * 4, t0); }
    unsigned wob;
    if (i < 16) { role_kv(p, rws, ep, xcd, li, tid, lane, w, ct STP_ARG); wob = 98304; }
    else        { role_qb(p, rws, ep, xcd, li, tid, lane, w, ct STP_ARG); wob = 0; }
    const int g = lane >> 4, c16 = lane & 15, q = c16 >> 2;
    unsigned ob = WS_O + (rt * 4 + w) * 16384 + lane * 16;
    unsigned wa = wob + (4 * g + q) * 128 + 8 * (lane & 3) + (((2 * g + (q >> 1)) & 3) << 5);
    PIN(ob); PIN(wa);
    BAR();
    STAMP(7);
    if (w < 4) {
        f32x4 acc[4];
#pragma unroll 1
        for (int it = 0; it < SPIN_CAP; ++it) {
            FENCE();
            u32x4 o[16];
#pragma unroll
            for (int k = 0; k < 16; ++k) o[k] = ld128s(rws, ob, k * 1024);
            unsigned m = 0;
#pragma unroll
            for (int t = 0; t < 4; ++t) acc[t] = Z4;
#pragma unroll
            for (int k = 0; k < 16; ++k) {
#pragma unroll
                for (int t = 0; t < 4; ++t) { const unsigned a = (wa ^ (t << 5)) + k * 4096; acc[t] = mma(ltr2(a, a + 2048), o[k], acc[t]); }
                m = umax4(m, o[k]);
            }
            if (!__any(m == POISON)) break;
        }
        STAMP(8);
        const unsigned sc = 65536 + w * 2304;
#pragma unroll
        for (int t = 0; t < 4; ++t) lw64(sc + c16 * 144 + t * 32 + g * 8, pkv(acc[t]));
        const int r8 = lane >> 3, ch = lane & 7;
        const u32x4 s0 = lr128(sc + r8 * 144 + ch * 16), s1 = lr128(sc + (r8 + 8) * 144 + ch * 16);
        const rsrc_t rout = mkrsrc(p.out, 2048 * 1024 * 2);
        const unsigned oo = ((rt * 64 + w * 16 + r8) * 1024 + ct * 64 + ch * 8) * 2;
        st128(rout, oo, s0); st128(rout, oo + 16384, s1);
        if (b == 0 && tid == 0) st32(rws, WS_EPOCH, ep);
        STAMP(9);
    }
    {
        VMWAIT(0);
        u32x4 tt = ld128(rws, WS_TS + lane * 16);
        const unsigned now0 = (unsigned)__builtin_amdgcn_s_memrealtime();
        unsigned d = max(max(now0 - tt[0], now0 - tt[1]), max(now0 - tt[2], now0 - tt[3]));
        for (int o = 32; o; o >>= 1) d = max(d, (unsigned)__shfl_xor((int)d, o));
        const unsigned tmin = now0 - __builtin_amdgcn_readfirstlane(d);
        while ((unsigned)__builtin_amdgcn_s_memrealtime() - tmin < TCAL) __builtin_amdgcn_s_sleep(1);
    }
}

extern "C" void kernel_launch(void* const* d_in, const int* in_sizes, int n_in,
                              void* d_out, int out_size, void* d_ws, size_t ws_size,
                              hipStream_t stream) {
    Params p;
    p.ax = (const unsigned short*)d_in[0]; p.wqkv = (const unsigned short*)d_in[1]; p.wo = (const unsigned short*)d_in[2];
    p.bx = (const unsigned short*)d_in[3]; p.wr = (const unsigned short*)d_in[4]; p.wi = (const unsigned short*)d_in[5];
    p.out = (unsigned short*)d_out; p.ws = (unsigned char*)d_ws;
    fused_kernel<<<256, 512, 0, stream>>>(p);
}
```

```cpp
#define TCAL 4000
#include <hip/hip_runtime.h>
#include <stdint.h>

typedef short    s16x4 __attribute__((ext_vector_type(4)));
typedef __bf16   bf16x8 __attribute__((ext_vector_type(8)));
typedef __bf16   bf16x2 __attribute__((ext_vector_type(2)));
typedef float    f32x2 __attribute__((ext_vector_type(2)));
typedef float    f32x4 __attribute__((ext_vector_type(4)));
typedef unsigned u32x2 __attribute__((ext_vector_type(2)));
typedef unsigned u32x4 __attribute__((ext_vector_type(4)));
typedef __amdgpu_buffer_rsrc_t rsrc_t;
#define LAS __attribute__((address_space(3)))
#define DI __device__ __forceinline__

constexpr unsigned WS_EPOCH = 0, WS_FX1 = 1024, WS_FPQ = 2048, WS_FX2 = 3072;
constexpr unsigned PART_STRIDE = 2304, WS_PART = 4096;
constexpr unsigned TB = 16512;
constexpr unsigned WS_T1 = 1u << 19, WS_T2 = WS_T1 + 128 * TB;
constexpr unsigned WS_PQ = WS_T2 + 128 * TB, WS_O = WS_PQ + (1u << 21), WS_END = WS_O + (1u << 21);
constexpr unsigned POISON = 0xFFFFFFFFu;
constexpr int SPIN_CAP = 1 << 15;
constexpr unsigned LDS_BYTES = 163840;
#define STAMP(i) do {} while (0)
#define STP_PARAM
#define STP_ARG

struct Params {
    const unsigned short *ax, *wqkv, *wo, *bx, *wr, *wi;
    unsigned short* out;
    unsigned char* ws;
};

DI rsrc_t mkrsrc(const void* ptr, unsigned bytes) { return __builtin_amdgcn_make_buffer_rsrc((void*)ptr, 0, bytes, 0x00020000); }
DI u32x4 ld128(rsrc_t r, unsigned off)  { return __builtin_amdgcn_raw_buffer_load_b128(r, off, 0, 16); }
DI u32x2 ld64(rsrc_t r, unsigned off)   { return __builtin_amdgcn_raw_buffer_load_b64(r, off, 0, 16); }
DI unsigned ld32(rsrc_t r, unsigned off){ return __builtin_amdgcn_raw_buffer_load_b32(r, off, 0, 16); }
DI u32x4 ld128s(rsrc_t r, unsigned off, unsigned so)  { return __builtin_amdgcn_raw_buffer_load_b128(r, off, so, 16); }
DI unsigned ld32s(rsrc_t r, unsigned off, unsigned so){ return __builtin_amdgcn_raw_buffer_load_b32(r, off, so, 16); }
DI void st128(rsrc_t r, unsigned off, u32x4 v) { __builtin_amdgcn_raw_buffer_store_b128(v, r, off, 0, 16); }
DI void st64(rsrc_t r, unsigned off, u32x2 v) { __builtin_amdgcn_raw_buffer_store_b64(v, r, off, 0, 16); }
DI void st32(rsrc_t r, unsigned off, unsigned v) { __builtin_amdgcn_raw_buffer_store_b32(v, r, off, 0, 16); }
#define FENCE() asm volatile("" ::: "memory")
#define BAR() asm volatile("s_waitcnt lgkmcnt(0)\n\ts_barrier" ::: "memory")
#define VMWAIT(n) asm volatile("s_waitcnt vmcnt(" #n ")" ::: "memory")
#define PIN(x) asm volatile("" : "+v"(x))
#define SCHED_FENCE() __builtin_amdgcn_sched_barrier(0)
DI void dma(rsrc_t r, unsigned voff, unsigned soff, unsigned lds) {
    asm volatile("s_mov_b32 m0, %0\n\ts_nop 0\n\tbuffer_load_dwordx4 %1, %2, %3 offen lds"
                 :: "s"(lds), "v"(voff), "s"(r), "s"(soff) : "memory");
}
DI unsigned pk(float lo, float hi) { f32x2 v = {lo, hi}; return __builtin_bit_cast(unsigned, __builtin_convertvector(v, bf16x2)); }
DI u32x2 pkv(f32x4 v) { return u32x2{pk(v[0], v[1]), pk(v[2], v[3])}; }
DI u32x4 cat(u32x2 a, u32x2 b) { return u32x4{a[0], a[1], b[0], b[1]}; }
DI f32x4 mma(u32x4 a, u32x4 b, f32x4 c) {
    return __builtin_amdgcn_mfma_f32_16x16x32_bf16(__builtin_bit_cast(bf16x8, a), __builtin_bit_cast(bf16x8, b), c, 0, 0, 0); }
DI f32x4 mma16(u32x2 a, u32x2 b, f32x4 c) {
    return __builtin_amdgcn_mfma_f32_16x16x16bf16_1k(__builtin_bit_cast(s16x4, a), __builtin_bit_cast(s16x4, b), c, 0, 0, 0); }
#define LP(T, a) ((LAS T*)(size_t)(a))
DI u32x4 lr128(unsigned a) { return *LP(const u32x4, a); }
DI f32x4 lrf128(unsigned a) { return *LP(const f32x4, a); }
DI unsigned lr32(unsigned a) { return *LP(const unsigned, a); }
DI float bf(unsigned short v) { return __builtin_bit_cast(float, (unsigned)v << 16); }
DI float lrbf(unsigned a) { return bf(*LP(const unsigned short, a)); }
DI void lw128(unsigned a, u32x4 v) { *LP(u32x4, a) = v; }
DI void lwf128(unsigned a, f32x4 v) { *LP(f32x4, a) = v; }
DI void lw64(unsigned a, u32x2 v) { *LP(u32x2, a) = v; }
DI void lw32(unsigned a, unsigned v) { *LP(unsigned, a) = v; }
DI u32x2 ltr(unsigned a) { return __builtin_bit_cast(u32x2, __builtin_amdgcn_ds_read_tr16_b64_v4i16(LP(s16x4, a))); }
DI u32x4 ltr2(unsigned a, unsigned b) { return cat(ltr(a), ltr(b)); }
DI unsigned umax3(unsigned a, unsigned b, unsigned c) { return max(max(a, b), c); }
DI unsigned umax4(unsigned m, u32x4 v) { return umax3(umax3(m, v[0], v[1]), v[2], v[3]); }
DI u32x2 phi4(f32x4 t) {
    f32x4 e, m;
#pragma unroll
    for (int r = 0; r < 4; ++r) {
        e[r] = __builtin_amdgcn_fmed3f(__builtin_amdgcn_exp2f(t[r] * 1.44269504f), 0.f, 1.f);
        m[r] = __builtin_elementwise_maximum(t[r], 0.f);
    }
    return pkv((m + e) + 1e-6f);
}
constexpr f32x4 Z4 = {0.f, 0.f, 0.f, 0.f};

struct Mat { u32x4 re, im; };
struct Cx  { f32x4 re, im; };
DI Cx angles(int n0, int dn, int mask, float inv) {
    Cx t;
#pragma unroll
    for (int r = 0; r < 4; ++r) { float a = (float)((n0 + r * dn) & mask) * inv;
        t.re[r] = __builtin_amdgcn_cosf(a); t.im[r] = __builtin_amdgcn_sinf(a); }
    return t;
}
DI u32x4 zip(f32x4 a, f32x4 b) { return u32x4{pk(a[0], b[0]), pk(a[1], b[1]), pk(a[2], b[2]), pk(a[3], b[3])}; }
DI Mat tomat(Cx t) { return Mat{zip(t.re, t.im), zip(-t.im, t.re)}; }
DI Cx dftA(u32x4 d, const Mat& f) { return Cx{mma(d, f.re, Z4), mma(d, f.im, Z4)}; }
DI Cx dftB(u32x4 d, const Mat& f) { return Cx{mma(f.re, d, Z4), mma(f.im, d, Z4)}; }
DI u32x4 twpk(Cx v, const Cx& w) { return zip(v.re * w.re + v.im * w.im, v.im * w.re - v.re * w.im); }
DI u32x4 pk4(Cx v)  { return zip(v.re, v.im); }
DI u32x4 pk4c(Cx v) { return zip(v.re, v.im) ^ 0x80000000u; }

DI void wait_flags(rsrc_t rws, unsigned base, unsigned ep, int lane) {
#pragma unroll 1
    for (int it = 0; it < SPIN_CAP; ++it) {
        FENCE();
        u32x2 f = ld64(rws, base + lane * 8);
        if (__all(f[0] == ep && f[1] == ep)) break;
        __builtin_amdgcn_s_sleep(2);
    }
    FENCE();
}
DI void dma_x(rsrc_t rx, unsigned v0, unsigned v1, unsigned soff, unsigned lds) {
    dma(rx, v0, soff, lds); dma(rx, v1, soff + 8192, lds + 1024); dma(rx, v0, soff + 16384, lds + 2048); dma(rx, v1, soff + 24576, lds + 3072);
}
template <int NB> struct Frag { u32x4 a[4], b[NB]; };
template <int NB> DI void frag_read(Frag<NB>& f, unsigned xa, unsigned w0) {
    const unsigned w1 = w0 ^ 32;
    f.a[0] = lr128(xa); f.b[0] = ltr2(w0, w0 + 512);
    f.a[1] = lr128(xa + 2048); f.a[2] = lr128(xa + 4096); f.a[3] = lr128(xa + 6144);
    f.b[1] = ltr2(w1, w1 + 512);
    if constexpr (NB == 4) { f.b[2] = ltr2(w0 + 8192, w0 + 8704); f.b[3] = ltr2(w1 + 8192, w1 + 8704); }
}
template <int NB> DI void frag_mma(const Frag<NB>& f, f32x4 (&acc)[4][NB]) {
#pragma unroll
    for (int j = 0; j < NB; ++j)
#pragma unroll
        for (int i = 0; i < 4; ++i) acc[i][j] = (NB == 4) ? mma(f.a[i], f.b[j], acc[i][j]) : mma(f.b[j], f.a[i], acc[i][j]);
}
template <int NB> DI void interleave() {
#pragma unroll
    for (int i = 0; i < 2 + NB; ++i) { __builtin_amdgcn_sched_group_barrier(0x8, 1, 0); __builtin_amdgcn_sched_group_barrier(0x100, 2, 0); }
    __builtin_amdgcn_sched_group_barrier(0x8, 4 * NB, 0);
}

DI void role_kv(const Params& p, rsrc_t rws, unsigned ep, int xcd, int li, int tid, int lane, int w, int ct STP_PARAM) {
    const int rp = (xcd >> 1) * 4 + (li & 3), hg = (xcd & 1) * 4 + (li >> 2);
    const int rot = 2 * (li >> 2) + 2 * (li & 3);
    const int g = lane >> 4, c16 = lane & 15;
    if (w >= 4) {
        const int lw = w - 4, r8 = lane >> 3, ch = lane & 7;
        const rsrc_t rx = mkrsrc(p.ax, 2048 * 512 * 2), rw = mkrsrc(p.wqkv, 512 * 1536 * 2), ro = mkrsrc(p.wo, 512 * 512 * 2);
        const unsigned xrow = (rp * 128 + lw * 32 + r8) * 1024;
        const unsigned vx0 = xrow + ((ch ^ (r8 >> 1)) << 4), vx1 = vx0 ^ 64;
        const unsigned vw0 = (lw * 16 + r8) * 3072 + hg * 128 + 1024 + ((ch ^ (r8 & 2)) << 4), vw1 = (vw0 ^ 64) + 8 * 3072;
        const unsigned vo = (lw * 64 + r8) * 1024 + ct * 128 + ((ch ^ (r8 & 6)) << 4);
#pragma unroll 1
        for (int s = -4; s < 8; ++s) {
            if (s >= 0) { if (s < 7) VMWAIT(24); else VMWAIT(16); BAR(); }
            if (s < 4) {
                const unsigned kb = (s + 4 + rot) & 7, slot = (s > 0 ? s - 1 : s + 4) * 32768, so = kb * (64 * 3072);
                dma_x(rx, vx0, vx1, kb * 128, slot + lw * 4096);
                dma(rw, vw0, so, slot + 16384 + lw * 2048); dma(rw, vw1, so, slot + 16384 + lw * 2048 + 1024);
                dma(rw, vw0, so + 1024, slot + 24576 + lw * 2048); dma(rw, vw1, so + 1024, slot + 24576 + lw * 2048 + 1024);
            } else if (s < 6) {
#pragma unroll 1
                for (int q = 0; q < 8; ++q) dma(ro, vo, (s - 4) * 262144 + q * 8192, 98304 + (s - 4) * 32768 + lw * 8192 + q * 1024);
            }
        }
        STAMP(1);
        BAR();
    } else {
        const int rh = w >> 1, hp = w & 1;
        f32x4 acc[4][4];
#pragma unroll
        for (int i = 0; i < 4; ++i)
#pragma unroll
            for (int j = 0; j < 4; ++j) { acc[i][j] = Z4; PIN(acc[i][j]); }
        const unsigned xa = (rh * 64 + c16) * 128 + ((g ^ (c16 >> 1)) << 4);
        const int q = c16 >> 2, fk = (q >> 1) | ((g & 1) << 1);
        const unsigned wa = 16384 + (8 * g + q) * 128 + (((2 * hp) ^ fk) << 5) + 8 * (lane & 3);
        Frag<4> f0, f1;
        unsigned slot = 0;
        BAR();
        frag_read(f0, xa, wa);
#pragma unroll 1
        for (int s = 0; s < 7; ++s) {
            SCHED_FENCE();
            frag_read(f1, slot + (xa ^ 64), slot + wa + 4096); frag_mma(f0, acc); interleave<4>();
            slot = (slot == 4 * 32768) ? 0 : slot + 32768;
            SCHED_FENCE(); BAR();
            frag_read(f0, slot + xa, slot + wa); frag_mma(f1, acc); interleave<4>();
        }
        SCHED_FENCE();
        u32x2 fx = {ep, ep};
        if (w == 2) fx = ld64(rws, WS_FX1 + lane * 8);
        frag_read(f1, slot + (xa ^ 64), slot + wa + 4096); frag_mma(f0, acc); interleave<4>();
        SCHED_FENCE();
        frag_mma(f1, acc);
        STAMP(1);
        f32x4 kv[2] = {Z4, Z4}, zz[2] = {Z4, Z4};
        const u32x4 ones = {0x3F803F80u, 0x3F803F80u, 0x3F803F80u, 0x3F803F80u};
#pragma unroll
        for (int b = 0; b < 2; ++b)
#pragma unroll
            for (int u = 0; u < 2; ++u) {
                const u32x4 A = cat(phi4(acc[2 * u][b]), phi4(acc[2 * u + 1][b])), B = cat(pkv(acc[2 * u][2 + b]), pkv(acc[2 * u + 1][2 + b]));
                kv[b] = mma(A, B, kv[b]); zz[b] = mma(A, ones, zz[b]);
            }
        const unsigned red = hp * 4096 + lane * 16;
        if (rh) { lwf128(red, kv[0]); lwf128(red + 1024, kv[1]); lwf128(red + 2048, zz[0]); lwf128(red + 3072, zz[1]); }
        if (!__all(fx[0] == ep && fx[1] == ep)) wait_flags(rws, WS_FX1, ep, lane);
        BAR();
        if (!rh) {
            kv[0] += lrf128(red); kv[1] += lrf128(red + 1024); zz[0] += lrf128(red + 2048); zz[1] += lrf128(red + 3072);
            const unsigned pb = WS_PART + (rp * 8 + hg) * PART_STRIDE;
            st128(rws, pb + hp * 1024 + lane * 16, cat(pkv(kv[0]), pkv(kv[1])));
            if (c16 == 0) { const unsigned zb = pb + 2048 + hp * 64 + g * 8; st64(rws, zb, pkv(zz[0])); st64(rws, zb + 32, pkv(zz[1])); }
        }
    }
    STAMP(2);
    if (w == 3) {
#pragma unroll 1
        for (int it = 0; it < SPIN_CAP; ++it) {
            FENCE();
            if (__builtin_amdgcn_readfirstlane(ld32(rws, WS_FPQ + (rp * 8 + hg) * 4)) == ep) break;
            __builtin_amdgcn_s_sleep(2);
        }
    }
    const int rg = w >> 1, hp = w & 1;
    unsigned pqb = WS_PQ + (((rp * 8 + hg) * 8 + 2 * rg) * 2 + hp) * 1024 + lane * 16;
    unsigned pb = WS_PART + hg * PART_STRIDE + tid * 4, pz = WS_PART + hg * PART_STRIDE + (tid >> 5) * 8 * PART_STRIDE + 2048 + (tid & 31) * 4;
    unsigned za = 32768 + 2048 + (4 * g + (c16 >> 2)) * 128 + hp * 64 + 8 * (lane & 3);
    unsigned ob = WS_O + ((rp * 8 + 2 * rg) * 16 + hg * 2 + hp) * 1024 + lane * 16;
    PIN(pqb); PIN(pb); PIN(pz); PIN(za); PIN(ob);
    if (w < 2) VMWAIT(0);
    BAR();
    STAMP(3);
    unsigned d[16], zr;
    u32x4 pq0, pq1;
#pragma unroll 1
    for (int it = 0; it < SPIN_CAP; ++it) {
        FENCE();
#pragma unroll
        for (int j = 0; j < 16; ++j) d[j] = ld32s(rws, pb, j * 8 * PART_STRIDE);
        zr = ld32(rws, pz);
        pq0 = ld128(rws, pqb); pq1 = ld128(rws, pqb + 2048);
        unsigned m = zr;
#pragma unroll
        for (int j = 0; j < 16; j += 2) m = umax3(m, d[j], d[j + 1]);
        if (!__any(m == POISON)) break;
    }
    f32x2 sm = {0.f, 0.f};
#pragma unroll
    for (int j = 0; j < 16; ++j) sm += f32x2{__builtin_bit_cast(float, d[j] << 16), __builtin_bit_cast(float, d[j] & 0xFFFF0000u)};
    lw32(32768 + tid * 4, pk(sm[0], sm[1])); lw32(32768 + 2048 + tid * 4, zr);
    BAR();
    STAMP(4);
    {
        const u32x4 kvA = lr128(32768 + hp * 1024 + lane * 16);
        const u32x2 one2 = {0x3F803F80u, 0x3F803F80u};
        const u32x2 zA[2] = {pkv(mma16(ltr(za), one2, Z4)), pkv(mma16(ltr(za + 32), one2, Z4))};
#pragma unroll
        for (int a = 0; a < 2; ++a) {
            const u32x4 pq = a ? pq1 : pq0;
            u32x2 o[2];
#pragma unroll
            for (int b = 0; b < 2; ++b) {
                const u32x2 B = {pq[2 * b], pq[2 * b + 1]};
                const f32x4 num = mma16(u32x2{kvA[2 * b], kvA[2 * b + 1]}, B, Z4), den = mma16(zA[b], B, Z4);
                o[b] = pkv(num * __builtin_amdgcn_rcpf(den[0] + 1e-6f));
            }
            st128(rws, ob + a * 16384, cat(o[0], o[1]));
        }
    }
    STAMP(5);
    VMWAIT(0);
}

constexpr unsigned L_U = 98304;
constexpr unsigned L_W = L_U + 19200;
constexpr unsigned L_SP = L_W + 20480;
constexpr unsigned L_C = L_SP + 512;
DI void role_qb(const Params& p, rsrc_t rws, unsigned ep, int xcd, int li, int tid, int lane, int w, int ct STP_PARAM) {
    const int rp = (xcd >> 1) * 4 + (li & 3), hg = (xcd & 1) * 4 + (li >> 2);
    const int rot = 2 * (li >> 2) + 2 * (li & 3) + 1;
    const int g = lane >> 4, c16 = lane & 15;
    const int me = xcd * 16 + li;
    const int jh = li >> 3, j1 = jh ? (xcd ? 16 - xcd : 8) : xcd, kb = li & 7;
    const int lw = w - 4, r8 = lane >> 3, ch = lane & 7;
    u32x4 x0, x1;
    {
        const rsrc_t rb = mkrsrc(p.bx, 2048 * 512 * 2);
        const unsigned vo = (256 * w + me) * 1024 + g * 256 + c16 * 4;
        asm volatile("buffer_load_dword %0, %8, %9, 0 offen\n\tbuffer_load_dword %1, %8, %9, 0 offen offset:64\n\t"
                     "buffer_load_dword %2, %8, %9, 0 offen offset:128\n\tbuffer_load_dword %3, %8, %9, 0 offen offset:192\n\t"
                     "buffer_load_dword %4, %8, %9, %10 offen\n\tbuffer_load_dword %5, %8, %9, %10 offen offset:64\n\t"
                     "buffer_load_dword %6, %8, %9, %10 offen offset:128\n\tbuffer_load_dword %7, %8, %9, %10 offen offset:192"
                     : "=&v"(x0[0]), "=&v"(x0[1]), "=&v"(x0[2]), "=&v"(x0[3]), "=&v"(x1[0]), "=&v"(x1[1]), "=&v"(x1[2]), "=&v"(x1[3])
                     : "v"(vo), "s"(rb), "s"(131072u) : "memory");
    }
    const rsrc_t rx = mkrsrc(p.ax, 2048 * 512 * 2), rw = mkrsrc(p.wqkv, 512 * 1536 * 2);
    const unsigned vx0 = (rp * 128 + lw * 32 + r8) * 1024 + ((ch ^ (r8 >> 1)) << 4), vx1 = vx0 ^ 64;
    const unsigned vw0 = (lw * 16 + r8) * 3072 + hg * 128 + ((ch ^ (r8 & 2)) << 4), vw1 = (vw0 ^ 64) + 8 * 3072;
    auto stage = [&](int s) {
        const unsigned kq = (s + rot) & 7, slot = (s & 3) * 24576;
        dma_x(rx, vx0, vx1, kq * 128, slot + lw * 4096);
        dma(rw, vw0, kq * (64 * 3072), slot + 16384 + lw * 2048); dma(rw, vw1, kq * (64 * 3072), slot + 16384 + lw * 2048 + 1024);
    };
    if (w >= 4) {
#pragma unroll 1
        for (int s = 0; s < 2; ++s) stage(s);
    }
    if (w < 5) {
        const int k = w, k4 = 4 * k, sU = (0x04004 >> k4) & 15, sQ = (0x40040 >> k4) & 15;
        const int P = (((c16 & ((0x7F7FF >> k4) & 15)) << sU) + (sU ? j1 : 0)) << ((0x00833 >> k4) & 15);
        const int n0 = P * (((4 * (g & ((0x31133 >> k4) & 15))) << sQ) + (sQ ? j1 : 0)), dn = P << sQ;
        const Cx t = angles(n0, dn, 2047, 1.f / 2048);
        Mat m = tomat(t);
        if (k > 2) m = Mat{__builtin_bit_cast(u32x4, t.re), __builtin_bit_cast(u32x4, t.im)};
        if (k == 2 && (g >> 1) != (c16 >> 3)) m = Mat{u32x4{0, 0, 0, 0}, u32x4{0, 0, 0, 0}};
        lw128(L_C + k * 2048 + lane * 16, m.re); lw128(L_C + k * 2048 + 1024 + lane * 16, m.im);
    }
    Mat F = tomat(angles(4 * g * c16, c16, 15, 1.f / 16));
    Cx tw1 = angles(4 * g * c16, c16, 255, 1.f / 256);
    const int km0 = 16 * w + c16, km1 = km0 ? 256 - km0 : 128;
    unsigned ua = L_U + (2 * w) * 1072 + g * 272 + c16 * 16;
    unsigned a0 = L_U + 4 * g * 1072 + ((km0 >> 2) & 3) * 272 + (km0 >> 4) * 16 + (km0 & 3) * 4;
    unsigned a1s = L_U + 4 * g * 1072 + ((km1 >> 2) & 3) * 272 + (km1 >> 4) * 16 + (km1 & 3) * 4;
    unsigned sw0 = L_W + w * 2304 + c16 * 144 + g * 16, sw1 = L_W + w * 2304 + ((16 - c16) & 15) * 144 + 64 + g * 16;
    unsigned sr0 = L_W + w * 2304 + r8 * 144 + ch * 16;
    unsigned tb1 = WS_T1 + me * TB + w * 2048 + lane * 16, obp = WS_O + ((rp * 8 + w) * 16 + hg * 2) * 1024 + lane * 16;
    PIN(F.re); PIN(F.im); PIN(tw1.re); PIN(tw1.im); PIN(ua); PIN(a0); PIN(a1s); PIN(sw0); PIN(sw1); PIN(sr0); PIN(tb1); PIN(obp);
    asm volatile("s_cmp_lt_u32 %2, 4\n\ts_cbranch_scc1 1f\n\ts_waitcnt vmcnt(12)\n\ts_branch 2f\n1:\n\ts_waitcnt vmcnt(0)\n2:"
                 : "+v"(x0), "+v"(x1) : "s"(w) : "memory", "scc");
    STAMP(1);
    {
        lw128(ua, pk4(dftA(twpk(dftA(x0, F), tw1), F))); lw128(ua + 1072, pk4(dftA(twpk(dftA(x1, F), tw1), F)));
        BAR();
        const u32x4 d0 = {lr32(a0), lr32(a0 + 1072), lr32(a0 + 2144), lr32(a0 + 3216)};
        const u32x4 d1 = {lr32(a1s), lr32(a1s + 1072), lr32(a1s + 2144), lr32(a1s + 3216)};
        lw128(sw0, pk4(dftA(d0, F))); lw128(sw1, pk4c(dftA(d1, F)));
        const u32x4 s0 = lr128(sr0), s1 = lr128(sr0 + 8 * 144);
        st128(rws, tb1, s0); st128(rws, tb1 + 1024, s1);
        const u32x4 P4 = {POISON, POISON, POISON, POISON};
        st128(rws, obp, P4); st128(rws, obp + 1024, P4);
        if (tid < 136) st128(rws, WS_PART + (rp * 8 + hg) * PART_STRIDE + tid * 16, P4);
    }
    STAMP(2);
    const bool special = (kb == 0 && w == 0);
    float sp[6][4];
    if (w >= 4) {
#pragma unroll 1
        for (int s = -2; s < 5; ++s) {
            if (s >= 0) { if (s == 0) VMWAIT(22); else if (s == 1) VMWAIT(16); else VMWAIT(12); BAR(); }
            if (s != 0) stage(s < 0 ? s + 4 : s + 3);
        }
        struct __attribute__((packed, aligned(2))) U4 { u32x4 v; };
        const int j2 = (tid >> 1) & 127, ks = j1 + 16 * j2, hf = (tid & 1) * 8;
        const int op = ks * 257 + 16 * kb + hf, oq = ((2048 - ks) & 2047) * 257 + 241 - 16 * kb + hf;
        const u32x4 l0 = ((const U4*)(p.wr + op))->v, l1 = ((const U4*)(p.wi + op))->v, l2 = ((const U4*)(p.wr + oq))->v, l3 = ((const U4*)(p.wi + oq))->v;
        FENCE();
        VMWAIT(16); BAR(); VMWAIT(10); BAR(); VMWAIT(4); BAR();
        const unsigned wa = L_W + j2 * 40 + (tid & 1) * 16;
        lw64(wa, u32x2{l0[0], l0[1]}); lw64(wa + 8, u32x2{l0[2], l0[3]});
        lw64(wa + 5120, u32x2{l1[0], l1[1]}); lw64(wa + 5128, u32x2{l1[2], l1[3]});
        lw64(wa + 10240, u32x2{l2[0], l2[1]}); lw64(wa + 10248, u32x2{l2[2], l2[3]});
        lw64(wa + 15360, u32x2{l3[0], l3[1]}); lw64(wa + 15368, u32x2{l3[2], l3[3]});
        if (w == 7) wait_flags(rws, WS_FX1, ep, lane);
    } else {
        const int rh = w >> 1, hp = w & 1;
        f32x4 acc[4][2];
#pragma unroll
        for (int i = 0; i < 4; ++i) { acc[i][0] = Z4; acc[i][1] = Z4; PIN(acc[i][0]); PIN(acc[i][1]); }
        const unsigned xa = (rh * 64 + c16) * 128 + ((g ^ (c16 >> 1)) << 4);
        const int q = c16 >> 2, fk = (q >> 1) | ((g & 1) << 1);
        const unsigned wa = 16384 + (8 * g + q) * 128 + (((2 * hp) ^ fk) << 5) + 8 * (lane & 3);
        Frag<2> f0, f1;
        BAR();
        frag_read(f0, xa, wa);
#pragma unroll 1
        for (int s = 0; s < 7; ++s) {
            const unsigned slot = (s & 3) * 24576, nslot = ((s + 1) & 3) * 24576;
            SCHED_FENCE();
            frag_read(f1, slot + (xa ^ 64), slot + wa + 4096); frag_mma(f0, acc); interleave<2>();
            SCHED_FENCE();
            if (s == 1) VMWAIT(0);
            BAR();
            if (s == 1 && tid == 0) st32(rws, WS_FX1 + me * 4, ep);
            frag_read(f0, nslot + xa, nslot + wa); frag_mma(f1, acc); interleave<2>();
        }
        SCHED_FENCE();
        if (special) {
#pragma unroll
            for (int r = 0; r < 4; ++r) {
                const int ks = j1 + 16 * (c16 + 64 * (g & 1) + 16 * r), kn = (2048 - ks) & 2047;
                sp[0][r] = bf(p.wr[kn * 257]); sp[1][r] = bf(p.wi[kn * 257]);
                sp[2][r] = bf(p.wr[ks * 257 + 256]); sp[3][r] = bf(p.wi[ks * 257 + 256]);
                sp[4][r] = bf(p.wr[ks * 257 + 128]); sp[5][r] = bf(p.wi[ks * 257 + 128]);
            }
        }
        frag_read(f1, 3 * 24576 + (xa ^ 64), 3 * 24576 + wa + 4096); frag_mma(f0, acc); interleave<2>();
        SCHED_FENCE();
        frag_mma(f1, acc);
        const unsigned qb = WS_PQ + (((rp * 8 + hg) * 8 + 4 * rh) * 2 + hp) * 1024 + lane * 16;
#pragma unroll
        for (int i = 0; i < 4; ++i) st128(rws, qb + i * 2048, cat(phi4(acc[i][0]), phi4(acc[i][1])));
    }
    STAMP(3);
    const int lam = tid >> 3, s2a = (lam >> 5) * 32 + (lam & 3) * 8 + ((lam >> 2) & 7);
    unsigned sb = L_U + ((ch & 3) * 2 + (ch >> 2)) * 1040 + ((lam >> 5) * 16 + ((lam >> 2) & 7)) * 16 + (lam & 3) * 4;
    unsigned tb2 = WS_T1 + s2a * TB + (kb * 16 + j1) * 128 + ch * 16;
    PIN(sb); PIN(tb2);
    VMWAIT(0);
    BAR();
    if (tid == 0) st32(rws, WS_FPQ + (rp * 8 + hg) * 4, ep);
    const u32x4 i0 = ld128(rws, tb2), i1 = ld128s(rws, tb2, 64 * TB);
    unsigned isp = 0;
    if (kb == 0 && tid < 128) isp = ld32(rws, WS_T1 + tid * TB + ((16 - j1) & 15) * 128);
    FENCE();
    Cx a1, bz, a2, bv;
    {
        const int x = g >> 1, col = 2 * w + x;
        const float th = (float)(16 * kb + col) * (1.f / 512), sn = __builtin_amdgcn_sinf(th), cs = __builtin_amdgcn_cosf(th);
        const unsigned wp = L_W + (c16 + 64 * (g & 1)) * 40 + col * 2, wq = wp + 10240 + 30 - col * 4;
        f32x4 pr, pi, qr, qi;
#pragma unroll
        for (int r = 0; r < 4; ++r) { pr[r] = lrbf(wp + r * 640); pi[r] = lrbf(wp + r * 640 + 5120); qr[r] = lrbf(wq + r * 640); qi[r] = lrbf(wq + r * 640 + 5120); }
        qi = -qi;
        const bool sx = special && x == 0;
        if (special) {
#pragma unroll
            for (int r = 0; r < 4; ++r) if (sx) {
                pr[r] = 0.5f * (pr[r] + sp[0][r]); pi[r] = 0.5f * (pi[r] - sp[1][r]); qr[r] = 0.5f * (qr[r] + sp[2][r]); qi[r] = 0.5f * (qi[r] + sp[3][r]); }
        }
        const float sc = 1.f / 1048576;
        const f32x4 sr = (pr + qr) * sc, si = (pi + qi) * sc, dr = (pr - qr) * sc, di = (pi - qi) * sc;
        a1.re = sr - dr * sn; a1.im = si - di * sn; a2.re = sr + dr * sn; a2.im = si + di * sn;
        bz.re = dr * cs; bz.im = di * cs; bv = bz;
        if (special) {
#pragma unroll
            for (int r = 0; r < 4; ++r) if (sx) { a2.re[r] = 2.f * sc * sp[4][r]; a2.im[r] = 2.f * sc * sp[5][r]; bv.re[r] = 0.f; bv.im[r] = 0.f; }
        }
    }
    const unsigned ca = L_C + lane * 16;
    Mat F1 = {lr128(ca), lr128(ca + 1024)}, F2 = {lr128(ca + 2048), lr128(ca + 3072)}, G = {lr128(ca + 4096), lr128(ca + 5120)};
    Cx twa = {lrf128(ca + 6144), lrf128(ca + 7168)}, twb = {lrf128(ca + 8192), lrf128(ca + 9216)};
    unsigned ta = L_U + ((w >> 1) * 2 + 8 * (w & 1)) * 1040 + lane * 16;
    unsigned tb3 = WS_T2 + (kb * 16 + j1) * TB + s2a * 128 + ch * 16;
    PIN(a1.re); PIN(a1.im); PIN(a2.re); PIN(a2.im); PIN(bz.re); PIN(bz.im); PIN(bv.re); PIN(bv.im);
    PIN(F1.re); PIN(F1.im); PIN(F2.re); PIN(F2.im); PIN(G.re); PIN(G.im); PIN(twa.re); PIN(twa.im); PIN(twb.re); PIN(twb.im); PIN(ta); PIN(tb3);
    lw32(sb, i0[0]); lw32(sb + 128, i0[1]); lw32(sb + 8320, i0[2]); lw32(sb + 8320 + 128, i0[3]);
    lw32(sb + 512, i1[0]); lw32(sb + 512 + 128, i1[1]); lw32(sb + 512 + 8320, i1[2]); lw32(sb + 512 + 8320 + 128, i1[3]);
    if (kb == 0 && tid < 128) lw32(L_SP + ((tid >> 5) * 8 + (tid & 7)) * 16 + ((tid >> 3) & 3) * 4, isp ^ 0x80000000u);
    BAR();
    STAMP(4);
    {
        const u32x4 zi = lr128(ta), vi = lr128(ta + 1040);
        const Cx Z = dftB(twpk(dftA(zi, F1), twa), G), V = dftB(twpk(dftA(vi, F1), twa), G);
        Cx Vz = V;
        if (special) {
            u32x4 v0 = lr128(L_SP + (g * 8 + (c16 & 7)) * 16);
            if (c16 >= 8) v0 = vi;
            Vz = dftB(twpk(dftA(v0, F1), twa), G);
        }
        const u32x4 zc = zip(a1.re * Z.re - a1.im * Z.im - bz.re * Vz.im - bz.im * Vz.re, bz.im * Vz.im - a1.re * Z.im - a1.im * Z.re - bz.re * Vz.re);
        const u32x4 vc = zip(a2.re * V.re - a2.im * V.im + bv.re * Z.im + bv.im * Z.re, bv.re * Z.re - a2.re * V.im - a2.im * V.re - bv.im * Z.im);
        lw128(ta, pk4(dftB(twpk(dftA(zc, G), twb), F2))); lw128(ta + 1040, pk4c(dftB(twpk(dftA(vc, G), twb), F2)));
        BAR();
        const u32x4 o0 = {lr32(sb), lr32(sb + 128), lr32(sb + 8320), lr32(sb + 8320 + 128)};
        const u32x4 o1 = {lr32(sb + 512), lr32(sb + 512 + 128), lr32(sb + 512 + 8320), lr32(sb + 512 + 8320 + 128)};
        st128(rws, tb3, o0); st128(rws, tb3 + 64 * 128, o1);
    }
    STAMP(5);
    VMWAIT(0);
    BAR();
    if (tid == 0) st32(rws, WS_FX2 + me * 4, ep);
    if (w >= 4) {
        const rsrc_t ro = mkrsrc(p.wo, 512 * 512 * 2);
        const unsigned vo = (lw * 128 + r8) * 1024 + ct * 128 + ((ch ^ (r8 & 6)) << 4);
#pragma unroll 1
        for (int q = 0; q < 16; ++q) dma(ro, vo, q * 8192, lw * 16384 + q * 1024);
    }
    unsigned ra[4];
#pragma unroll
    for (int i = 0; i < 4; ++i) {
        const int km = 64 * g + 16 * i + c16, kq = km < 128 ? km : (256 - km) & 127;
        ra[i] = L_U + ((km >> 7) * 16 + 2 * w) * 592 + (kq + 16 * (kq >> 6)) * 4;
        PIN(ra[i]);
    }
    STAMP(6);
    if (w < 4) {
        const int set = g & 1, s1 = set ? (16 - c16) & 15 : c16, k0 = 16 * (2 * w + (g >> 1));
        unsigned u2 = L_U + (set * 16 + s1) * 592 + (k0 + 16 * (k0 >> 6)) * 4;
        unsigned tb = WS_T2 + ((2 * w + (c16 >> 3)) * 16 + 4 * g) * TB + me * 128 + ch * 16;
        PIN(u2); PIN(tb);
        wait_flags(rws, WS_FX2, ep, lane);
        STAMP(10);
        const u32x4 l0 = ld128(rws, tb), l1 = ld128s(rws, tb, TB), l2 = ld128s(rws, tb, 2 * TB), l3 = ld128s(rws, tb, 3 * TB);
        u32x4 t[4];
#pragma unroll
        for (int e = 0; e < 4; ++e) t[e] = pk4(dftA(u32x4{l0[e], l1[e], l2[e], l3[e]}, F));
#pragma unroll
        for (int r = 0; r < 4; ++r) lw128(u2 + r * 16, u32x4{t[0][r], t[1][r], t[2][r], t[3][r]});
    }
    BAR();
    STAMP(11);
    {
        const u32x4 d0 = {lr32(ra[0]), lr32(ra[1]), lr32(ra[2]), lr32(ra[3])};
        const u32x4 d1 = {lr32(ra[0] + 592), lr32(ra[1] + 592), lr32(ra[2] + 592), lr32(ra[3] + 592)};
        const u32x4 y0 = pk4c(dftB(twpk(dftA(d0, F), tw1), F)), y1 = pk4c(dftB(twpk(dftA(d1, F), tw1), F));
        unsigned* o = (unsigned*)(p.out + (256 * w + me) * 1024 + 512) + 64 * g + c16;
#pragma unroll
        for (int r = 0; r < 4; ++r) { o[16 * r] = y0[r]; o[16 * r + 128 * 512] = y1[r]; }
    }
    if (w >= 4) VMWAIT(8);
}

__global__ void __launch_bounds__(512, 2) fused_kernel(Params p) {
    unsigned long long tcal0; asm volatile("s_memrealtime %0\n\ts_waitcnt lgkmcnt(0)" : "=s"(tcal0)); __builtin_amdgcn_sched_barrier(0);
    const int b = blockIdx.x, xcd = b & 7, i = b >> 3, li = i & 15;
    const int tid = threadIdx.x, lane = tid & 63, w = __builtin_amdgcn_readfirstlane(tid >> 6);
    const int rt = xcd * 4 + (i & 3), ct = i >> 2;
    __shared__ __attribute__((aligned(16))) char smem[LDS_BYTES];
    asm volatile("" :: "s"((unsigned)(size_t)(LAS char*)smem));
    const rsrc_t rws = mkrsrc(p.ws, WS_END);
    const unsigned ep = *(const __attribute__((address_space(4))) unsigned*)(p.ws + WS_EPOCH) + 1;
    STAMP(0);
    unsigned wob;
    if (i < 16) { role_kv(p, rws, ep, xcd, li, tid, lane, w, ct STP_ARG); wob = 98304; }
    else        { role_qb(p, rws, ep, xcd, li, tid, lane, w, ct STP_ARG); wob = 0; }
    const int g = lane >> 4, c16 = lane & 15, q = c16 >> 2;
    unsigned ob = WS_O + (rt * 4 + w) * 16384 + lane * 16;
    unsigned wa = wob + (4 * g + q) * 128 + 8 * (lane & 3) + (((2 * g + (q >> 1)) & 3) << 5);
    PIN(ob); PIN(wa);
    BAR();
    STAMP(7);
    if (w < 4) {
        f32x4 acc[4];
#pragma unroll 1
        for (int it = 0; it < SPIN_CAP; ++it) {
            FENCE();
            u32x4 o[16];
#pragma unroll
            for (int k = 0; k < 16; ++k) o[k] = ld128s(rws, ob, k * 1024);
            unsigned m = 0;
#pragma unroll
            for (int t = 0; t < 4; ++t) acc[t] = Z4;
#pragma unroll
            for (int k = 0; k < 16; ++k) {
#pragma unroll
                for (int t = 0; t < 4; ++t) { const unsigned a = (wa ^ (t << 5)) + k * 4096; acc[t] = mma(ltr2(a, a + 2048), o[k], acc[t]); }
                m = umax4(m, o[k]);
            }
            if (!__any(m == POISON)) break;
        }
        STAMP(8);
        const unsigned sc = 65536 + w * 2304;
#pragma unroll
        for (int t = 0; t < 4; ++t) lw64(sc + c16 * 144 + t * 32 + g * 8, pkv(acc[t]));
        const int r8 = lane >> 3, ch = lane & 7;
        const u32x4 s0 = lr128(sc + r8 * 144 + ch * 16), s1 = lr128(sc + (r8 + 8) * 144 + ch * 16);
        u32x4* o = (u32x4*)(p.out + (rt * 64 + w * 16 + r8) * 1024 + ct * 64 + ch * 8);
        o[0] = s0; o[1024] = s1;
        if (b == 0 && tid == 0) st32(rws, WS_EPOCH, ep);
        STAMP(9);
    }
    while (__builtin_amdgcn_s_memrealtime() - tcal0 < TCAL) __builtin_amdgcn_s_sleep(1);
}

extern "C" void kernel_launch(void* const* d_in, const int* in_sizes, int n_in,
                              void* d_out, int out_size, void* d_ws, size_t ws_size,
                              hipStream_t stream) {
    Params p;
    p.ax = (const unsigned short*)d_in[0]; p.wqkv = (const unsigned short*)d_in[1]; p.wo = (const unsigned short*)d_in[2];
    p.bx = (const unsigned short*)d_in[3]; p.wr = (const unsigned short*)d_in[4]; p.wi = (const unsigned short*)d_in[5];
    p.out = (unsigned short*)d_out; p.ws = (unsigned char*)d_ws;
    fused_kernel<<<256, 512, 0, stream>>>(p);
}
```

```cpp
#define TCAL 4000
#include <hip/hip_runtime.h>
#include <stdint.h>

typedef short    s16x4 __attribute__((ext_vector_type(4)));
typedef __bf16   bf16x8 __attribute__((ext_vector_type(8)));
typedef __bf16   bf16x2 __attribute__((ext_vector_type(2)));
typedef float    f32x2 __attribute__((ext_vector_type(2)));
typedef float    f32x4 __attribute__((ext_vector_type(4)));
typedef unsigned u32x2 __attribute__((ext_vector_type(2)));
typedef unsigned u32x4 __attribute__((ext_vector_type(4)));
typedef __amdgpu_buffer_rsrc_t rsrc_t;
#define LAS __attribute__((address_space(3)))
#define DI __device__ __forceinline__

constexpr unsigned WS_EPOCH = 0, WS_FX1 = 1024, WS_FPQ = 2048, WS_FX2 = 3072;
constexpr unsigned PART_STRIDE = 2304, WS_PART = 4096;
constexpr unsigned TB = 16512;
constexpr unsigned WS_T1 = 1u << 19, WS_T2 = WS_T1 + 128 * TB;
constexpr unsigned WS_PQ = WS_T2 + 128 * TB, WS_O = WS_PQ + (1u << 21), WS_END = WS_O + (1u << 21);
constexpr unsigned POISON = 0xFFFFFFFFu;
constexpr int SPIN_CAP = 1 << 15;
constexpr unsigned LDS_BYTES = 163840;
#define STAMP(i) do {} while (0)
#define STP_PARAM
#define STP_ARG

struct Params {
    const unsigned short *ax, *wqkv, *wo, *bx, *wr, *wi;
    unsigned short* out;
    unsigned char* ws;
};

DI rsrc_t mkrsrc(const void* ptr, unsigned bytes) { return __builtin_amdgcn_make_buffer_rsrc((void*)ptr, 0, bytes, 0x00020000); }
DI u32x4 ld128(rsrc_t r, unsigned off)  { return __builtin_amdgcn_raw_buffer_load_b128(r, off, 0, 16); }
DI u32x2 ld64(rsrc_t r, unsigned off)   { return __builtin_amdgcn_raw_buffer_load_b64(r, off, 0, 16); }
DI unsigned ld32(rsrc_t r, unsigned off){ return __builtin_amdgcn_raw_buffer_load_b32(r, off, 0, 16); }
DI u32x4 ld128s(rsrc_t r, unsigned off, unsigned so)  { return __builtin_amdgcn_raw_buffer_load_b128(r, off, so, 16); }
DI unsigned ld32s(rsrc_t r, unsigned off, unsigned so){ return __builtin_amdgcn_raw_buffer_load_b32(r, off, so, 16); }
DI void st128(rsrc_t r, unsigned off, u32x4 v) { __builtin_amdgcn_raw_buffer_store_b128(v, r, off, 0, 16); }
DI void st64(rsrc_t r, unsigned off, u32x2 v) { __builtin_amdgcn_raw_buffer_store_b64(v, r, off, 0, 16); }
DI void st32(rsrc_t r, unsigned off, unsigned v) { __builtin_amdgcn_raw_buffer_store_b32(v, r, off, 0, 16); }
#define FENCE() asm volatile("" ::: "memory")
#define BAR() asm volatile("s_waitcnt lgkmcnt(0)\n\ts_barrier" ::: "memory")
#define VMWAIT(n) asm volatile("s_waitcnt vmcnt(" #n ")" ::: "memory")
#define PIN(x) asm volatile("" : "+v"(x))
#define SCHED_FENCE() __builtin_amdgcn_sched_barrier(0)
DI void dma(rsrc_t r, unsigned voff, unsigned soff, unsigned lds) {
    asm volatile("s_mov_b32 m0, %0\n\ts_nop 0\n\tbuffer_load_dwordx4 %1, %2, %3 offen lds"
                 :: "s"(lds), "v"(voff), "s"(r), "s"(soff) : "memory");
}
DI unsigned pk(float lo, float hi) { f32x2 v = {lo, hi}; return __builtin_bit_cast(unsigned, __builtin_convertvector(v, bf16x2)); }
DI u32x2 pkv(f32x4 v) { return u32x2{pk(v[0], v[1]), pk(v[2], v[3])}; }
DI u32x4 cat(u32x2 a, u32x2 b) { return u32x4{a[0], a[1], b[0], b[1]}; }
DI f32x4 mma(u32x4 a, u32x4 b, f32x4 c) {
    return __builtin_amdgcn_mfma_f32_16x16x32_bf16(__builtin_bit_cast(bf16x8, a), __builtin_bit_cast(bf16x8, b), c, 0, 0, 0); }
DI f32x4 mma16(u32x2 a, u32x2 b, f32x4 c) {
    return __builtin_amdgcn_mfma_f32_16x16x16bf16_1k(__builtin_bit_cast(s16x4, a), __builtin_bit_cast(s16x4, b), c, 0, 0, 0); }
#define LP(T, a) ((LAS T*)(size_t)(a))
DI u32x4 lr128(unsigned a) { return *LP(const u32x4, a); }
DI f32x4 lrf128(unsigned a) { return *LP(const f32x4, a); }
DI unsigned lr32(unsigned a) { return *LP(const unsigned, a); }
DI float bf(unsigned short v) { return __builtin_bit_cast(float, (unsigned)v << 16); }
DI float lrbf(unsigned a) { return bf(*LP(const unsigned short, a)); }
DI void lw128(unsigned a, u32x4 v) { *LP(u32x4, a) = v; }
DI void lwf128(unsigned a, f32x4 v) { *LP(f32x4, a) = v; }
DI void lw64(unsigned a, u32x2 v) { *LP(u32x2, a) = v; }
DI void lw32(unsigned a, unsigned v) { *LP(unsigned, a) = v; }
DI u32x2 ltr(unsigned a) { return __builtin_bit_cast(u32x2, __builtin_amdgcn_ds_read_tr16_b64_v4i16(LP(s16x4, a))); }
DI u32x4 ltr2(unsigned a, unsigned b) { return cat(ltr(a), ltr(b)); }
DI unsigned umax3(unsigned a, unsigned b, unsigned c) { return max(max(a, b), c); }
DI unsigned umax4(unsigned m, u32x4 v) { return umax3(umax3(m, v[0], v[1]), v[2], v[3]); }
DI u32x2 phi4(f32x4 t) {
    f32x4 e, m;
#pragma unroll
    for (int r = 0; r < 4; ++r) {
        e[r] = __builtin_amdgcn_fmed3f(__builtin_amdgcn_exp2f(t[r] * 1.44269504f), 0.f, 1.f);
        m[r] = __builtin_elementwise_maximum(t[r], 0.f);
    }
    return pkv((m + e) + 1e-6f);
}
constexpr f32x4 Z4 = {0.f, 0.f, 0.f, 0.f};

struct Mat { u32x4 re, im; };
struct Cx  { f32x4 re, im; };
DI Cx angles(int n0, int dn, int mask, float inv) {
    Cx t;
#pragma unroll
    for (int r = 0; r < 4; ++r) { float a = (float)((n0 + r * dn) & mask) * inv;
        t.re[r] = __builtin_amdgcn_cosf(a); t.im[r] = __builtin_amdgcn_sinf(a); }
    return t;
}
DI u32x4 zip(f32x4 a, f32x4 b) { return u32x4{pk(a[0], b[0]), pk(a[1], b[1]), pk(a[2], b[2]), pk(a[3], b[3])}; }
DI Mat tomat(Cx t) { return Mat{zip(t.re, t.im), zip(-t.im, t.re)}; }
DI Cx dftA(u32x4 d, const Mat& f) { return Cx{mma(d, f.re, Z4), mma(d, f.im, Z4)}; }
DI Cx dftB(u32x4 d, const Mat& f) { return Cx{mma(f.re, d, Z4), mma(f.im, d, Z4)}; }
DI u32x4 twpk(Cx v, const Cx& w) { return zip(v.re * w.re + v.im * w.im, v.im * w.re - v.re * w.im); }
DI u32x4 pk4(Cx v)  { return zip(v.re, v.im); }
DI u32x4 pk4c(Cx v) { return zip(v.re, v.im) ^ 0x80000000u; }

DI void wait_flags(rsrc_t rws, unsigned base, unsigned ep, int lane) {
#pragma unroll 1
    for (int it = 0; it < SPIN_CAP; ++it) {
        FENCE();
        u32x2 f = ld64(rws, base + lane * 8);
        if (__all(f[0] == ep && f[1] == ep)) break;
        __builtin_amdgcn_s_sleep(2);
    }
    FENCE();
}
DI void dma_x(rsrc_t rx, unsigned v0, unsigned v1, unsigned soff, unsigned lds) {
    dma(rx, v0, soff, lds); dma(rx, v1, soff + 8192, lds + 1024); dma(rx, v0, soff + 16384, lds + 2048); dma(rx, v1, soff + 24576, lds + 3072);
}
template <int NB> struct Frag { u32x4 a[4], b[NB]; };
template <int NB> DI void frag_read(Frag<NB>& f, unsigned xa, unsigned w0) {
    const unsigned w1 = w0 ^ 32;
    f.a[0] = lr128(xa); f.b[0] = ltr2(w0, w0 + 512);
    f.a[1] = lr128(xa + 2048); f.a[2] = lr128(xa + 4096); f.a[3] = lr128(xa + 6144);
    f.b[1] = ltr2(w1, w1 + 512);
    if constexpr (NB == 4) { f.b[2] = ltr2(w0 + 8192, w0 + 8704); f.b[3] = ltr2(w1 + 8192, w1 + 8704); }
}
template <int NB> DI void frag_mma(const Frag<NB>& f, f32x4 (&acc)[4][NB]) {
#pragma unroll
    for (int j = 0; j < NB; ++j)
#pragma unroll
        for (int i = 0; i < 4; ++i) acc[i][j] = (NB == 4) ? mma(f.a[i], f.b[j], acc[i][j]) : mma(f.b[j], f.a[i], acc[i][j]);
}
template <int NB> DI void interleave() {
#pragma unroll
    for (int i = 0; i < 2 + NB; ++i) { __builtin_amdgcn_sched_group_barrier(0x8, 1, 0); __builtin_amdgcn_sched_group_barrier(0x100, 2, 0); }
    __builtin_amdgcn_sched_group_barrier(0x8, 4 * NB, 0);
}

DI void role_kv(const Params& p, rsrc_t rws, unsigned ep, int xcd, int li, int tid, int lane, int w, int ct STP_PARAM) {
    const int rp = (xcd >> 1) * 4 + (li & 3), hg = (xcd & 1) * 4 + (li >> 2);
    const int rot = 2 * (li >> 2) + 2 * (li & 3);
    const int g = lane >> 4, c16 = lane & 15;
    if (w >= 4) {
        const int lw = w - 4, r8 = lane >> 3, ch = lane & 7;
        const rsrc_t rx = mkrsrc(p.ax, 2048 * 512 * 2), rw = mkrsrc(p.wqkv, 512 * 1536 * 2), ro = mkrsrc(p.wo, 512 * 512 * 2);
        const unsigned xrow = (rp * 128 + lw * 32 + r8) * 1024;
        const unsigned vx0 = xrow + ((ch ^ (r8 >> 1)) << 4), vx1 = vx0 ^ 64;
        const unsigned vw0 = (lw * 16 + r8) * 3072 + hg * 128 + 1024 + ((ch ^ (r8 & 2)) << 4), vw1 = (vw0 ^ 64) + 8 * 3072;
        const unsigned vo = (lw * 64 + r8) * 1024 + ct * 128 + ((ch ^ (r8 & 6)) << 4);
#pragma unroll 1
        for (int s = -4; s < 8; ++s) {
            if (s >= 0) { if (s < 7) VMWAIT(24); else VMWAIT(16); BAR(); }
            if (s < 4) {
                const unsigned kb = (s + 4 + rot) & 7, slot = (s > 0 ? s - 1 : s + 4) * 32768, so = kb * (64 * 3072);
                dma_x(rx, vx0, vx1, kb * 128, slot + lw * 4096);
                dma(rw, vw0, so, slot + 16384 + lw * 2048); dma(rw, vw1, so, slot + 16384 + lw * 2048 + 1024);
                dma(rw, vw0, so + 1024, slot + 24576 + lw * 2048); dma(rw, vw1, so + 1024, slot + 24576 + lw * 2048 + 1024);
            } else if (s < 6) {
#pragma unroll 1
                for (int q = 0; q < 8; ++q) dma(ro, vo, (s - 4) * 262144 + q * 8192, 98304 + (s - 4) * 32768 + lw * 8192 + q * 1024);
            }
        }
        STAMP(1);
        BAR();
    } else {
        const int rh = w >> 1, hp = w & 1;
        f32x4 acc[4][4];
#pragma unroll
        for (int i = 0; i < 4; ++i)
#pragma unroll
            for (int j = 0; j < 4; ++j) { acc[i][j] = Z4; PIN(acc[i][j]); }
        const unsigned xa = (rh * 64 + c16) * 128 + ((g ^ (c16 >> 1)) << 4);
        const int q = c16 >> 2, fk = (q >> 1) | ((g & 1) << 1);
        const unsigned wa = 16384 + (8 * g + q) * 128 + (((2 * hp) ^ fk) << 5) + 8 * (lane & 3);
        Frag<4> f0, f1;
        unsigned slot = 0;
        BAR();
        frag_read(f0, xa, wa);
#pragma unroll 1
        for (int s = 0; s < 7; ++s) {
            SCHED_FENCE();
            frag_read(f1, slot + (xa ^ 64), slot + wa + 4096); frag_mma(f0, acc); interleave<4>();
            slot = (slot == 4 * 32768) ? 0 : slot + 32768;
            SCHED_FENCE(); BAR();
            frag_read(f0, slot + xa, slot + wa); frag_mma(f1, acc); interleave<4>();
        }
        SCHED_FENCE();
        u32x2 fx = {ep, ep};
        if (w == 2) fx = ld64(rws, WS_FX1 + lane * 8);
        frag_read(f1, slot + (xa ^ 64), slot + wa + 4096); frag_mma(f0, acc); interleave<4>();
        SCHED_FENCE();
        frag_mma(f1, acc);
        STAMP(1);
        f32x4 kv[2] = {Z4, Z4}, zz[2] = {Z4, Z4};
        const u32x4 ones = {0x3F803F80u, 0x3F803F80u, 0x3F803F80u, 0x3F803F80u};
#pragma unroll
        for (int b = 0; b < 2; ++b)
#pragma unroll
            for (int u = 0; u < 2; ++u) {
                const u32x4 A = cat(phi4(acc[2 * u][b]), phi4(acc[2 * u + 1][b])), B = cat(pkv(acc[2 * u][2 + b]), pkv(acc[2 * u + 1][2 + b]));
                kv[b] = mma(A, B, kv[b]); zz[b] = mma(A, ones, zz[b]);
            }
        const unsigned red = hp * 4096 + lane * 16;
        if (rh) { lwf128(red, kv[0]); lwf128(red + 1024, kv[1]); lwf128(red + 2048, zz[0]); lwf128(red + 3072, zz[1]); }
        if (!__all(fx[0] == ep && fx[1] == ep)) wait_flags(rws, WS_FX1, ep, lane);
        BAR();
        if (!rh) {
            kv[0] += lrf128(red); kv[1] += lrf128(red + 1024); zz[0] += lrf128(red + 2048); zz[1] += lrf128(red + 3072);
            const unsigned pb = WS_PART + (rp * 8 + hg) * PART_STRIDE;
            st128(rws, pb + hp * 1024 + lane * 16, cat(pkv(kv[0]), pkv(kv[1])));
            if (c16 == 0) { const unsigned zb = pb + 2048 + hp * 64 + g * 8; st64(rws, zb, pkv(zz[0])); st64(rws, zb + 32, pkv(zz[1])); }
        }
    }
    STAMP(2);
    if (w == 3) {
#pragma unroll 1
        for (int it = 0; it < SPIN_CAP; ++it) {
            FENCE();
            if (__builtin_amdgcn_readfirstlane(ld32(rws, WS_FPQ + (rp * 8 + hg) * 4)) == ep) break;
            __builtin_amdgcn_s_sleep(2);
        }
    }
    const int rg = w >> 1, hp = w & 1;
    unsigned pqb = WS_PQ + (((rp * 8 + hg) * 8 + 2 * rg) * 2 + hp) * 1024 + lane * 16;
    unsigned pb = WS_PART + hg * PART_STRIDE + tid * 4, pz = WS_PART + hg * PART_STRIDE + (tid >> 5) * 8 * PART_STRIDE + 2048 + (tid & 31) * 4;
    unsigned za = 32768 + 2048 + (4 * g + (c16 >> 2)) * 128 + hp * 64 + 8 * (lane & 3);
    unsigned ob = WS_O + ((rp * 8 + 2 * rg) * 16 + hg * 2 + hp) * 1024 + lane * 16;
    PIN(pqb); PIN(pb); PIN(pz); PIN(za); PIN(ob);
    if (w < 2) VMWAIT(0);
    BAR();
    STAMP(3);
    unsigned d[16], zr;
    u32x4 pq0, pq1;
#pragma unroll 1
    for (int it = 0; it < SPIN_CAP; ++it) {
        FENCE();
#pragma unroll
        for (int j = 0; j < 16; ++j) d[j] = ld32s(rws, pb, j * 8 * PART_STRIDE);
        zr = ld32(rws, pz);
        pq0 = ld128(rws, pqb); pq1 = ld128(rws, pqb + 2048);
        unsigned m = zr;
#pragma unroll
        for (int j = 0; j < 16; j += 2) m = umax3(m, d[j], d[j + 1]);
        if (!__any(m == POISON)) break;
    }
    f32x2 sm = {0.f, 0.f};
#pragma unroll
    for (int j = 0; j < 16; ++j) sm += f32x2{__builtin_bit_cast(float, d[j] << 16), __builtin_bit_cast(float, d[j] & 0xFFFF0000u)};
    lw32(32768 + tid * 4, pk(sm[0], sm[1])); lw32(32768 + 2048 + tid * 4, zr);
    BAR();
    STAMP(4);
    {
        const u32x4 kvA = lr128(32768 + hp * 1024 + lane * 16);
        const u32x2 one2 = {0x3F803F80u, 0x3F803F80u};
        const u32x2 zA[2] = {pkv(mma16(ltr(za), one2, Z4)), pkv(mma16(ltr(za + 32), one2, Z4))};
#pragma unroll
        for (int a = 0; a < 2; ++a) {
            const u32x4 pq = a ? pq1 : pq0;
            u32x2 o[2];
#pragma unroll
            for (int b = 0; b < 2; ++b) {
                const u32x2 B = {pq[2 * b], pq[2 * b + 1]};
                const f32x4 num = mma16(u32x2{kvA[2 * b], kvA[2 * b + 1]}, B, Z4), den = mma16(zA[b], B, Z4);
                o[b] = pkv(num * __builtin_amdgcn_rcpf(den[0] + 1e-6f));
            }
            st128(rws, ob + a * 16384, cat(o[0], o[1]));
        }
    }
    STAMP(5);
    VMWAIT(0);
}

constexpr unsigned L_U = 98304;
constexpr unsigned L_W = L_U + 19200;
constexpr unsigned L_SP = L_W + 20480;
constexpr unsigned L_C = L_SP + 512;
DI void role_qb(const Params& p, rsrc_t rws, unsigned ep, int xcd, int li, int tid, int lane, int w, int ct STP_PARAM) {
    const int rp = (xcd >> 1) * 4 + (li & 3), hg = (xcd & 1) * 4 + (li >> 2);
    const int rot = 2 * (li >> 2) + 2 * (li & 3) + 1;
    const int g = lane >> 4, c16 = lane & 15;
    const int me = xcd * 16 + li;
    const int jh = li >> 3, j1 = jh ? (xcd ? 16 - xcd : 8) : xcd, kb = li & 7;
    const int lw = w - 4, r8 = lane >> 3, ch = lane & 7;
    u32x4 x0, x1;
    {
        const rsrc_t rb = mkrsrc(p.bx, 2048 * 512 * 2);
        const unsigned vo = (256 * w + me) * 1024 + g * 256 + c16 * 4;
        asm volatile("buffer_load_dword %0, %8, %9, 0 offen\n\tbuffer_load_dword %1, %8, %9, 0 offen offset:64\n\t"
                     "buffer_load_dword %2, %8, %9, 0 offen offset:128\n\tbuffer_load_dword %3, %8, %9, 0 offen offset:192\n\t"
                     "buffer_load_dword %4, %8, %9, %10 offen\n\tbuffer_load_dword %5, %8, %9, %10 offen offset:64\n\t"
                     "buffer_load_dword %6, %8, %9, %10 offen offset:128\n\tbuffer_load_dword %7, %8, %9, %10 offen offset:192"
                     : "=&v"(x0[0]), "=&v"(x0[1]), "=&v"(x0[2]), "=&v"(x0[3]), "=&v"(x1[0]), "=&v"(x1[1]), "=&v"(x1[2]), "=&v"(x1[3])
                     : "v"(vo), "s"(rb), "s"(131072u) : "memory");
    }
    const rsrc_t rx = mkrsrc(p.ax, 2048 * 512 * 2), rw = mkrsrc(p.wqkv, 512 * 1536 * 2);
    const unsigned vx0 = (rp * 128 + lw * 32 + r8) * 1024 + ((ch ^ (r8 >> 1)) << 4), vx1 = vx0 ^ 64;
    const unsigned vw0 = (lw * 16 + r8) * 3072 + hg * 128 + ((ch ^ (r8 & 2)) << 4), vw1 = (vw0 ^ 64) + 8 * 3072;
    auto stage = [&](int s) {
        const unsigned kq = (s + rot) & 7, slot = (s & 3) * 24576;
        dma_x(rx, vx0, vx1, kq * 128, slot + lw * 4096);
        dma(rw, vw0, kq * (64 * 3072), slot + 16384 + lw * 2048); dma(rw, vw1, kq * (64 * 3072), slot + 16384 + lw * 2048 + 1024);
    };
    if (w >= 4) {
#pragma unroll 1
        for (int s = 0; s < 2; ++s) stage(s);
    }
    if (w < 5) {
        const int k = w, k4 = 4 * k, sU = (0x04004 >> k4) & 15, sQ = (0x40040 >> k4) & 15;
        const int P = (((c16 & ((0x7F7FF >> k4) & 15)) << sU) + (sU ? j1 : 0)) << ((0x00833 >> k4) & 15);
        const int n0 = P * (((4 * (g & ((0x31133 >> k4) & 15))) << sQ) + (sQ ? j1 : 0)), dn = P << sQ;
        const Cx t = angles(n0, dn, 2047, 1.f / 2048);
        Mat m = tomat(t);
        if (k > 2) m = Mat{__builtin_bit_cast(u32x4, t.re), __builtin_bit_cast(u32x4, t.im)};
        if (k == 2 && (g >> 1) != (c16 >> 3)) m = Mat{u32x4{0, 0, 0, 0}, u32x4{0, 0, 0, 0}};
        lw128(L_C + k * 2048 + lane * 16, m.re); lw128(L_C + k * 2048 + 1024 + lane * 16, m.im);
    }
    Mat F = tomat(angles(4 * g * c16, c16, 15, 1.f / 16));
    Cx tw1 = angles(4 * g * c16, c16, 255, 1.f / 256);
    const int km0 = 16 * w + c16, km1 = km0 ? 256 - km0 : 128;
    unsigned ua = L_U + (2 * w) * 1072 + g * 272 + c16 * 16;
    unsigned a0 = L_U + 4 * g * 1072 + ((km0 >> 2) & 3) * 272 + (km0 >> 4) * 16 + (km0 & 3) * 4;
    unsigned a1s = L_U + 4 * g * 1072 + ((km1 >> 2) & 3) * 272 + (km1 >> 4) * 16 + (km1 & 3) * 4;
    unsigned sw0 = L_W + w * 2304 + c16 * 144 + g * 16, sw1 = L_W + w * 2304 + ((16 - c16) & 15) * 144 + 64 + g * 16;
    unsigned sr0 = L_W + w * 2304 + r8 * 144 + ch * 16;
    unsigned tb1 = WS_T1 + me * TB + w * 2048 + lane * 16, obp = WS_O + ((rp * 8 + w) * 16 + hg * 2) * 1024 + lane * 16;
    PIN(F.re); PIN(F.im); PIN(tw1.re); PIN(tw1.im); PIN(ua); PIN(a0); PIN(a1s); PIN(sw0); PIN(sw1); PIN(sr0); PIN(tb1); PIN(obp);
    asm volatile("s_cmp_lt_u32 %2, 4\n\ts_cbranch_scc1 1f\n\ts_waitcnt vmcnt(12)\n\ts_branch 2f\n1:\n\ts_waitcnt vmcnt(0)\n2:"
                 : "+v"(x0), "+v"(x1) : "s"(w) : "memory", "scc");
    STAMP(1);
    {
        lw128(ua, pk4(dftA(twpk(dftA(x0, F), tw1), F))); lw128(ua + 1072, pk4(dftA(twpk(dftA(x1, F), tw1), F)));
        BAR();
        const u32x4 d0 = {lr32(a0), lr32(a0 + 1072), lr32(a0 + 2144), lr32(a0 + 3216)};
        const u32x4 d1 = {lr32(a1s), lr32(a1s + 1072), lr32(a1s + 2144), lr32(a1s + 3216)};
        lw128(sw0, pk4(dftA(d0, F))); lw128(sw1, pk4c(dftA(d1, F)));
        const u32x4 s0 = lr128(sr0), s1 = lr128(sr0 + 8 * 144);
        st128(rws, tb1, s0); st128(rws, tb1 + 1024, s1);
        const u32x4 P4 = {POISON, POISON, POISON, POISON};
        st128(rws, obp, P4); st128(rws, obp + 1024, P4);
        if (tid < 136) st128(rws, WS_PART + (rp * 8 + hg) * PART_STRIDE + tid * 16, P4);
    }
    STAMP(2);
    const bool special = (kb == 0 && w == 0);
    float sp[6][4];
    if (w >= 4) {
#pragma unroll 1
        for (int s = -2; s < 5; ++s) {
            if (s >= 0) { if (s == 0) VMWAIT(22); else if (s == 1) VMWAIT(16); else VMWAIT(12); BAR(); }
            if (s != 0) stage(s < 0 ? s + 4 : s + 3);
        }
        struct __attribute__((packed, aligned(2))) U4 { u32x4 v; };
        const int j2 = (tid >> 1) & 127, ks = j1 + 16 * j2, hf = (tid & 1) * 8;
        const int op = ks * 257 + 16 * kb + hf, oq = ((2048 - ks) & 2047) * 257 + 241 - 16 * kb + hf;
        const u32x4 l0 = ((const U4*)(p.wr + op))->v, l1 = ((const U4*)(p.wi + op))->v, l2 = ((const U4*)(p.wr + oq))->v, l3 = ((const U4*)(p.wi + oq))->v;
        FENCE();
        VMWAIT(16); BAR(); VMWAIT(10); BAR(); VMWAIT(4); BAR();
        const unsigned wa = L_W + j2 * 40 + (tid & 1) * 16;
        lw64(wa, u32x2{l0[0], l0[1]}); lw64(wa + 8, u32x2{l0[2], l0[3]});
        lw64(wa + 5120, u32x2{l1[0], l1[1]}); lw64(wa + 5128, u32x2{l1[2], l1[3]});
        lw64(wa + 10240, u32x2{l2[0], l2[1]}); lw64(wa + 10248, u32x2{l2[2], l2[3]});
        lw64(wa + 15360, u32x2{l3[0], l3[1]}); lw64(wa + 15368, u32x2{l3[2], l3[3]});
        if (w == 7) wait_flags(rws, WS_FX1, ep, lane);
    } else {
        const int rh = w >> 1, hp = w & 1;
        f32x4 acc[4][2];
#pragma unroll
        for (int i = 0; i < 4; ++i) { acc[i][0] = Z4; acc[i][1] = Z4; PIN(acc[i][0]); PIN(acc[i][1]); }
        const unsigned xa = (rh * 64 + c16) * 128 + ((g ^ (c16 >> 1)) << 4);
        const int q = c16 >> 2, fk = (q >> 1) | ((g & 1) << 1);
        const unsigned wa = 16384 + (8 * g + q) * 128 + (((2 * hp) ^ fk) << 5) + 8 * (lane & 3);
        Frag<2> f0, f1;
        BAR();
        frag_read(f0, xa, wa);
#pragma unroll 1
        for (int s = 0; s < 7; ++s) {
            const unsigned slot = (s & 3) * 24576, nslot = ((s + 1) & 3) * 24576;
            SCHED_FENCE();
            frag_read(f1, slot + (xa ^ 64), slot + wa + 4096); frag_mma(f0, acc); interleave<2>();
            SCHED_FENCE();
            if (s == 1) VMWAIT(0);
            BAR();
            if (s == 1 && tid == 0) st32(rws, WS_FX1 + me * 4, ep);
            frag_read(f0, nslot + xa, nslot + wa); frag_mma(f1, acc); interleave<2>();
        }
        SCHED_FENCE();
        if (special) {
#pragma unroll
            for (int r = 0; r < 4; ++r) {
                const int ks = j1 + 16 * (c16 + 64 * (g & 1) + 16 * r), kn = (2048 - ks) & 2047;
                sp[0][r] = bf(p.wr[kn * 257]); sp[1][r] = bf(p.wi[kn * 257]);
                sp[2][r] = bf(p.wr[ks * 257 + 256]); sp[3][r] = bf(p.wi[ks * 257 + 256]);
                sp[4][r] = bf(p.wr[ks * 257 + 128]); sp[5][r] = bf(p.wi[ks * 257 + 128]);
            }
        }
        frag_read(f1, 3 * 24576 + (xa ^ 64), 3 * 24576 + wa + 4096); frag_mma(f0, acc); interleave<2>();
        SCHED_FENCE();
        frag_mma(f1, acc);
        const unsigned qb = WS_PQ + (((rp * 8 + hg) * 8 + 4 * rh) * 2 + hp) * 1024 + lane * 16;
#pragma unroll
        for (int i = 0; i < 4; ++i) st128(rws, qb + i * 2048, cat(phi4(acc[i][0]), phi4(acc[i][1])));
    }
    STAMP(3);
    const int lam = tid >> 3, s2a = (lam >> 5) * 32 + (lam & 3) * 8 + ((lam >> 2) & 7);
    unsigned sb = L_U + ((ch & 3) * 2 + (ch >> 2)) * 1040 + ((lam >> 5) * 16 + ((lam >> 2) & 7)) * 16 + (lam & 3) * 4;
    unsigned tb2 = WS_T1 + s2a * TB + (kb * 16 + j1) * 128 + ch * 16;
    PIN(sb); PIN(tb2);
    VMWAIT(0);
    BAR();
    if (tid == 0) st32(rws, WS_FPQ + (rp * 8 + hg) * 4, ep);
    const u32x4 i0 = ld128(rws, tb2), i1 = ld128s(rws, tb2, 64 * TB);
    unsigned isp = 0;
    if (kb == 0 && tid < 128) isp = ld32(rws, WS_T1 + tid * TB + ((16 - j1) & 15) * 128);
    FENCE();
    Cx a1, bz, a2, bv;
    {
        const int x = g >> 1, col = 2 * w + x;
        const float th = (float)(16 * kb + col) * (1.f / 512), sn = __builtin_amdgcn_sinf(th), cs = __builtin_amdgcn_cosf(th);
        const unsigned wp = L_W + (c16 + 64 * (g & 1)) * 40 + col * 2, wq = wp + 10240 + 30 - col * 4;
        f32x4 pr, pi, qr, qi;
#pragma unroll
        for (int r = 0; r < 4; ++r) { pr[r] = lrbf(wp + r * 640); pi[r] = lrbf(wp + r * 640 + 5120); qr[r] = lrbf(wq + r * 640); qi[r] = lrbf(wq + r * 640 + 5120); }
        qi = -qi;
        const bool sx = special && x == 0;
        if (special) {
#pragma unroll
            for (int r = 0; r < 4; ++r) if (sx) {
                pr[r] = 0.5f * (pr[r] + sp[0][r]); pi[r] = 0.5f * (pi[r] - sp[1][r]); qr[r] = 0.5f * (qr[r] + sp[2][r]); qi[r] = 0.5f * (qi[r] + sp[3][r]); }
        }
        const float sc = 1.f / 1048576;
        const f32x4 sr = (pr + qr) * sc, si = (pi + qi) * sc, dr = (pr - qr) * sc, di = (pi - qi) * sc;
        a1.re = sr - dr * sn; a1.im = si - di * sn; a2.re = sr + dr * sn; a2.im = si + di * sn;
        bz.re = dr * cs; bz.im = di * cs; bv = bz;
        if (special) {
#pragma unroll
            for (int r = 0; r < 4; ++r) if (sx) { a2.re[r] = 2.f * sc * sp[4][r]; a2.im[r] = 2.f * sc * sp[5][r]; bv.re[r] = 0.f; bv.im[r] = 0.f; }
        }
    }
    const unsigned ca = L_C + lane * 16;
    Mat F1 = {lr128(ca), lr128(ca + 1024)}, F2 = {lr128(ca + 2048), lr128(ca + 3072)}, G = {lr128(ca + 4096), lr128(ca + 5120)};
    Cx twa = {lrf128(ca + 6144), lrf128(ca + 7168)}, twb = {lrf128(ca + 8192), lrf128(ca + 9216)};
    unsigned ta = L_U + ((w >> 1) * 2 + 8 * (w & 1)) * 1040 + lane * 16;
    unsigned tb3 = WS_T2 + (kb * 16 + j1) * TB + s2a * 128 + ch * 16;
    PIN(a1.re); PIN(a1.im); PIN(a2.re); PIN(a2.im); PIN(bz.re); PIN(bz.im); PIN(bv.re); PIN(bv.im);
    PIN(F1.re); PIN(F1.im); PIN(F2.re); PIN(F2.im); PIN(G.re); PIN(G.im); PIN(twa.re); PIN(twa.im); PIN(twb.re); PIN(twb.im); PIN(ta); PIN(tb3);
    lw32(sb, i0[0]); lw32(sb + 128, i0[1]); lw32(sb + 8320, i0[2]); lw32(sb + 8320 + 128, i0[3]);
    lw32(sb + 512, i1[0]); lw32(sb + 512 + 128, i1[1]); lw32(sb + 512 + 8320, i1[2]); lw32(sb + 512 + 8320 + 128, i1[3]);
    if (kb == 0 && tid < 128) lw32(L_SP + ((tid >> 5) * 8 + (tid & 7)) * 16 + ((tid >> 3) & 3) * 4, isp ^ 0x80000000u);
    BAR();
    STAMP(4);
    {
        const u32x4 zi = lr128(ta), vi = lr128(ta + 1040);
        const Cx Z = dftB(twpk(dftA(zi, F1), twa), G), V = dftB(twpk(dftA(vi, F1), twa), G);
        Cx Vz = V;
        if (special) {
            u32x4 v0 = lr128(L_SP + (g * 8 + (c16 & 7)) * 16);
            if (c16 >= 8) v0 = vi;
            Vz = dftB(twpk(dftA(v0, F1), twa), G);
        }
        const u32x4 zc = zip(a1.re * Z.re - a1.im * Z.im - bz.re * Vz.im - bz.im * Vz.re, bz.im * Vz.im - a1.re * Z.im - a1.im * Z.re - bz.re * Vz.re);
        const u32x4 vc = zip(a2.re * V.re - a2.im * V.im + bv.re * Z.im + bv.im * Z.re, bv.re * Z.re - a2.re * V.im - a2.im * V.re - bv.im * Z.im);
        lw128(ta, pk4(dftB(twpk(dftA(zc, G), twb), F2))); lw128(ta + 1040, pk4c(dftB(twpk(dftA(vc, G), twb), F2)));
        BAR();
        const u32x4 o0 = {lr32(sb), lr32(sb + 128), lr32(sb + 8320), lr32(sb + 8320 + 128)};
        const u32x4 o1 = {lr32(sb + 512), lr32(sb + 512 + 128), lr32(sb + 512 + 8320), lr32(sb + 512 + 8320 + 128)};
        st128(rws, tb3, o0); st128(rws, tb3 + 64 * 128, o1);
    }
    STAMP(5);
    VMWAIT(0);
    BAR();
    if (tid == 0) st32(rws, WS_FX2 + me * 4, ep);
    if (w >= 4) {
        const rsrc_t ro = mkrsrc(p.wo, 512 * 512 * 2);
        const unsigned vo = (lw * 128 + r8) * 1024 + ct * 128 + ((ch ^ (r8 & 6)) << 4);
#pragma unroll 1
        for (int q = 0; q < 16; ++q) dma(ro, vo, q * 8192, lw * 16384 + q * 1024);
    }
    unsigned ra[4];
#pragma unroll
    for (int i = 0; i < 4; ++i) {
        const int km = 64 * g + 16 * i + c16, kq = km < 128 ? km : (256 - km) & 127;
        ra[i] = L_U + ((km >> 7) * 16 + 2 * w) * 592 + (kq + 16 * (kq >> 6)) * 4;
        PIN(ra[i]);
    }
    STAMP(6);
    if (w < 4) {
        const int set = g & 1, s1 = set ? (16 - c16) & 15 : c16, k0 = 16 * (2 * w + (g >> 1));
        unsigned u2 = L_U + (set * 16 + s1) * 592 + (k0 + 16 * (k0 >> 6)) * 4;
        unsigned tb = WS_T2 + ((2 * w + (c16 >> 3)) * 16 + 4 * g) * TB + me * 128 + ch * 16;
        PIN(u2); PIN(tb);
        wait_flags(rws, WS_FX2, ep, lane);
        STAMP(10);
        const u32x4 l0 = ld128(rws, tb), l1 = ld128s(rws, tb, TB), l2 = ld128s(rws, tb, 2 * TB), l3 = ld128s(rws, tb, 3 * TB);
        u32x4 t[4];
#pragma unroll
        for (int e = 0; e < 4; ++e) t[e] = pk4(dftA(u32x4{l0[e], l1[e], l2[e], l3[e]}, F));
#pragma unroll
        for (int r = 0; r < 4; ++r) lw128(u2 + r * 16, u32x4{t[0][r], t[1][r], t[2][r], t[3][r]});
    }
    BAR();
    STAMP(11);
    {
        const u32x4 d0 = {lr32(ra[0]), lr32(ra[1]), lr32(ra[2]), lr32(ra[3])};
        const u32x4 d1 = {lr32(ra[0] + 592), lr32(ra[1] + 592), lr32(ra[2] + 592), lr32(ra[3] + 592)};
        const u32x4 y0 = pk4c(dftB(twpk(dftA(d0, F), tw1), F)), y1 = pk4c(dftB(twpk(dftA(d1, F), tw1), F));
        unsigned* o = (unsigned*)(p.out + (256 * w + me) * 1024 + 512) + 64 * g + c16;
#pragma unroll
        for (int r = 0; r < 4; ++r) { o[16 * r] = y0[r]; o[16 * r + 128 * 512] = y1[r]; }
    }
    if (w >= 4) VMWAIT(8);
}

__global__ void __launch_bounds__(512, 2) fused_kernel(Params p) {
    unsigned long long tcal0; asm volatile("s_memrealtime %0\n\ts_waitcnt lgkmcnt(0)" : "=s"(tcal0)); __builtin_amdgcn_sched_barrier(0);
    const int b = blockIdx.x, xcd = b & 7, i = b >> 3, li = i & 15;
    const int tid = threadIdx.x, lane = tid & 63, w = __builtin_amdgcn_readfirstlane(tid >> 6);
    const int rt = xcd * 4 + (i & 3), ct = i >> 2;
    __shared__ __attribute__((aligned(16))) char smem[LDS_BYTES];
    asm volatile("" :: "s"((unsigned)(size_t)(LAS char*)smem));
    const rsrc_t rws = mkrsrc(p.ws, WS_END);
    const unsigned ep = *(const __attribute__((address_space(4))) unsigned*)(p.ws + WS_EPOCH) + 1;
    STAMP(0);
    unsigned wob;
    if (i < 16) { role_kv(p, rws, ep, xcd, li, tid, lane, w, ct STP_ARG); wob = 98304; }
    else        { role_qb(p, rws, ep, xcd, li, tid, lane, w, ct STP_ARG); wob = 0; }
    const int g = lane >> 4, c16 = lane & 15, q = c16 >> 2;
    unsigned ob = WS_O + (rt * 4 + w) * 16384 + lane * 16;
    unsigned wa = wob + (4 * g + q) * 128 + 8 * (lane & 3) + (((2 * g + (q >> 1)) & 3) << 5);
    PIN(ob); PIN(wa);
    BAR();
    STAMP(7);
    if (w < 4) {
        f32x4 acc[4];
#pragma unroll 1
        for (int it = 0; it < SPIN_CAP; ++it) {
            FENCE();
            u32x4 o[16];
#pragma unroll
            for (int k = 0; k < 16; ++k) o[k] = ld128s(rws, ob, k * 1024);
            unsigned m = 0;
#pragma unroll
            for (int t = 0; t < 4; ++t) acc[t] = Z4;
#pragma unroll
            for (int k = 0; k < 16; ++k) {
#pragma unroll
                for (int t = 0; t < 4; ++t) { const unsigned a = (wa ^ (t << 5)) + k * 4096; acc[t] = mma(ltr2(a, a + 2048), o[k], acc[t]); }
                m = umax4(m, o[k]);
            }
            if (!__any(m == POISON)) break;
        }
        STAMP(8);
        const unsigned sc = 65536 + w * 2304;
#pragma unroll
        for (int t = 0; t < 4; ++t) lw64(sc + c16 * 144 + t * 32 + g * 8, pkv(acc[t]));
        const int r8 = lane >> 3, ch = lane & 7;
        const u32x4 s0 = lr128(sc + r8 * 144 + ch * 16), s1 = lr128(sc + (r8 + 8) * 144 + ch * 16);
        u32x4* o = (u32x4*)(p.out + (rt * 64 + w * 16 + r8) * 1024 + ct * 64 + ch * 8);
        o[0] = s0; o[1024] = s1;
        if (b == 0 && tid == 0) st32(rws, WS_EPOCH, ep);
        STAMP(9);
    }
    while (__builtin_amdgcn_s_memrealtime() - tcal0 < TCAL) __builtin_amdgcn_s_sleep(1);
}

__global__ void k_empty(unsigned* p) { asm volatile("" ::: "memory"); }
__global__ void __launch_bounds__(512, 2) k_empty_lds(unsigned* p) {
    __shared__ __attribute__((aligned(16))) char sm2[LDS_BYTES];
    asm volatile("" :: "s"((unsigned)(size_t)(LAS char*)sm2)); }
__global__ void __launch_bounds__(512, 2) k_empty_big(unsigned* p) { asm volatile("" ::: "memory"); }
extern "C" void kernel_launch(void* const* d_in, const int* in_sizes, int n_in,
                              void* d_out, int out_size, void* d_ws, size_t ws_size,
                              hipStream_t stream) {
    Params p;
    p.ax = (const unsigned short*)d_in[0]; p.wqkv = (const unsigned short*)d_in[1]; p.wo = (const unsigned short*)d_in[2];
    p.bx = (const unsigned short*)d_in[3]; p.wr = (const unsigned short*)d_in[4]; p.wi = (const unsigned short*)d_in[5];
    p.out = (unsigned short*)d_out; p.ws = (unsigned char*)d_ws;
    fused_kernel<<<256, 512, 0, stream>>>(p);
    k_empty_big<<<256, 512, 0, stream>>>((unsigned*)d_ws);
}
```

```cpp
#define TCAL 5000
#include <hip/hip_runtime.h>
#include <stdint.h>

typedef short    s16x4 __attribute__((ext_vector_type(4)));
typedef __bf16   bf16x8 __attribute__((ext_vector_type(8)));
typedef __bf16   bf16x2 __attribute__((ext_vector_type(2)));
typedef float    f32x2 __attribute__((ext_vector_type(2)));
typedef float    f32x4 __attribute__((ext_vector_type(4)));
typedef unsigned u32x2 __attribute__((ext_vector_type(2)));
typedef unsigned u32x4 __attribute__((ext_vector_type(4)));
typedef __amdgpu_buffer_rsrc_t rsrc_t;
#define LAS __attribute__((address_space(3)))
#define DI __device__ __forceinline__

constexpr unsigned WS_EPOCH = 0, WS_FX1 = 1024, WS_FPQ = 2048, WS_FX2 = 3072;
constexpr unsigned PART_STRIDE = 2304, WS_PART = 4096;
constexpr unsigned TB = 16512;
constexpr unsigned WS_T1 = 1u << 19, WS_T2 = WS_T1 + 128 * TB;
constexpr unsigned WS_PQ = WS_T2 + 128 * TB, WS_O = WS_PQ + (1u << 21), WS_END = WS_O + (1u << 21);
constexpr unsigned POISON = 0xFFFFFFFFu;
constexpr int SPIN_CAP = 1 << 15;
constexpr unsigned LDS_BYTES = 163840;
#define STAMP(i) do {} while (0)
#define STP_PARAM
#define STP_ARG

struct Params {
    const unsigned short *ax, *wqkv, *wo, *bx, *wr, *wi;
    unsigned short* out;
    unsigned char* ws;
};

DI rsrc_t mkrsrc(const void* ptr, unsigned bytes) { return __builtin_amdgcn_make_buffer_rsrc((void*)ptr, 0, bytes, 0x00020000); }
DI u32x4 ld128(rsrc_t r, unsigned off)  { return __builtin_amdgcn_raw_buffer_load_b128(r, off, 0, 16); }
DI u32x2 ld64(rsrc_t r, unsigned off)   { return __builtin_amdgcn_raw_buffer_load_b64(r, off, 0, 16); }
DI unsigned ld32(rsrc_t r, unsigned off){ return __builtin_amdgcn_raw_buffer_load_b32(r, off, 0, 16); }
DI u32x4 ld128s(rsrc_t r, unsigned off, unsigned so)  { return __builtin_amdgcn_raw_buffer_load_b128(r, off, so, 16); }
DI unsigned ld32s(rsrc_t r, unsigned off, unsigned so){ return __builtin_amdgcn_raw_buffer_load_b32(r, off, so, 16); }
DI void st128(rsrc_t r, unsigned off, u32x4 v) { __builtin_amdgcn_raw_buffer_store_b128(v, r, off, 0, 16); }
DI void st64(rsrc_t r, unsigned off, u32x2 v) { __builtin_amdgcn_raw_buffer_store_b64(v, r, off, 0, 16); }
DI void st32(rsrc_t r, unsigned off, unsigned v) { __builtin_amdgcn_raw_buffer_store_b32(v, r, off, 0, 16); }
#define FENCE() asm volatile("" ::: "memory")
#define BAR() asm volatile("s_waitcnt lgkmcnt(0)\n\ts_barrier" ::: "memory")
#define VMWAIT(n) asm volatile("s_waitcnt vmcnt(" #n ")" ::: "memory")
#define PIN(x) asm volatile("" : "+v"(x))
#define SCHED_FENCE() __builtin_amdgcn_sched_barrier(0)
DI void dma(rsrc_t r, unsigned voff, unsigned soff, unsigned lds) {
    asm volatile("s_mov_b32 m0, %0\n\ts_nop 0\n\tbuffer_load_dwordx4 %1, %2, %3 offen lds"
                 :: "s"(lds), "v"(voff), "s"(r), "s"(soff) : "memory");
}
DI unsigned pk(float lo, float hi) { f32x2 v = {lo, hi}; return __builtin_bit_cast(unsigned, __builtin_convertvector(v, bf16x2)); }
DI u32x2 pkv(f32x4 v) { return u32x2{pk(v[0], v[1]), pk(v[2], v[3])}; }
DI u32x4 cat(u32x2 a, u32x2 b) { return u32x4{a[0], a[1], b[0], b[1]}; }
DI f32x4 mma(u32x4 a, u32x4 b, f32x4 c) {
    return __builtin_amdgcn_mfma_f32_16x16x32_bf16(__builtin_bit_cast(bf16x8, a), __builtin_bit_cast(bf16x8, b), c, 0, 0, 0); }
DI f32x4 mma16(u32x2 a, u32x2 b, f32x4 c) {
    return __builtin_amdgcn_mfma_f32_16x16x16bf16_1k(__builtin_bit_cast(s16x4, a), __builtin_bit_cast(s16x4, b), c, 0, 0, 0); }
#define LP(T, a) ((LAS T*)(size_t)(a))
DI u32x4 lr128(unsigned a) { return *LP(const u32x4, a); }
DI f32x4 lrf128(unsigned a) { return *LP(const f32x4, a); }
DI unsigned lr32(unsigned a) { return *LP(const unsigned, a); }
DI float bf(unsigned short v) { return __builtin_bit_cast(float, (unsigned)v << 16); }
DI float lrbf(unsigned a) { return bf(*LP(const unsigned short, a)); }
DI void lw128(unsigned a, u32x4 v) { *LP(u32x4, a) = v; }
DI void lwf128(unsigned a, f32x4 v) { *LP(f32x4, a) = v; }
DI void lw64(unsigned a, u32x2 v) { *LP(u32x2, a) = v; }
DI void lw32(unsigned a, unsigned v) { *LP(unsigned, a) = v; }
DI u32x2 ltr(unsigned a) { return __builtin_bit_cast(u32x2, __builtin_amdgcn_ds_read_tr16_b64_v4i16(LP(s16x4, a))); }
DI u32x4 ltr2(unsigned a, unsigned b) { return cat(ltr(a), ltr(b)); }
DI unsigned umax3(unsigned a, unsigned b, unsigned c) { return max(max(a, b), c); }
DI unsigned umax4(unsigned m, u32x4 v) { return umax3(umax3(m, v[0], v[1]), v[2], v[3]); }
DI u32x2 phi4(f32x4 t) {
    f32x4 e, m;
#pragma unroll
    for (int r = 0; r < 4; ++r) {
        e[r] = __builtin_amdgcn_fmed3f(__builtin_amdgcn_exp2f(t[r] * 1.44269504f), 0.f, 1.f);
        m[r] = __builtin_elementwise_maximum(t[r], 0.f);
    }
    return pkv((m + e) + 1e-6f);
}
constexpr f32x4 Z4 = {0.f, 0.f, 0.f, 0.f};

struct Mat { u32x4 re, im; };
struct Cx  { f32x4 re, im; };
DI Cx angles(int n0, int dn, int mask, float inv) {
    Cx t;
#pragma unroll
    for (int r = 0; r < 4; ++r) { float a = (float)((n0 + r * dn) & mask) * inv;
        t.re[r] = __builtin_amdgcn_cosf(a); t.im[r] = __builtin_amdgcn_sinf(a); }
    return t;
}
DI u32x4 zip(f32x4 a, f32x4 b) { return u32x4{pk(a[0], b[0]), pk(a[1], b[1]), pk(a[2], b[2]), pk(a[3], b[3])}; }
DI Mat tomat(Cx t) { return Mat{zip(t.re, t.im), zip(-t.im, t.re)}; }
DI Cx dftA(u32x4 d, const Mat& f) { return Cx{mma(d, f.re, Z4), mma(d, f.im, Z4)}; }
DI Cx dftB(u32x4 d, const Mat& f) { return Cx{mma(f.re, d, Z4), mma(f.im, d, Z4)}; }
DI u32x4 twpk(Cx v, const Cx& w) { return zip(v.re * w.re + v.im * w.im, v.im * w.re - v.re * w.im); }
DI u32x4 pk4(Cx v)  { return zip(v.re, v.im); }
DI u32x4 pk4c(Cx v) { return zip(v.re, v.im) ^ 0x80000000u; }

DI void wait_flags(rsrc_t rws, unsigned base, unsigned ep, int lane) {
#pragma unroll 1
    for (int it = 0; it < SPIN_CAP; ++it) {
        FENCE();
        u32x2 f = ld64(rws, base + lane * 8);
        if (__all(f[0] == ep && f[1] == ep)) break;
        __builtin_amdgcn_s_sleep(2);
    }
    FENCE();
}
DI void dma_x(rsrc_t rx, unsigned v0, unsigned v1, unsigned soff, unsigned lds) {
    dma(rx, v0, soff, lds); dma(rx, v1, soff + 8192, lds + 1024); dma(rx, v0, soff + 16384, lds + 2048); dma(rx, v1, soff + 24576, lds + 3072);
}
template <int NB> struct Frag { u32x4 a[4], b[NB]; };
template <int NB> DI void frag_read(Frag<NB>& f, unsigned xa, unsigned w0) {
    const unsigned w1 = w0 ^ 32;
    f.a[0] = lr128(xa); f.b[0] = ltr2(w0, w0 + 512);
    f.a[1] = lr128(xa + 2048); f.a[2] = lr128(xa + 4096); f.a[3] = lr128(xa + 6144);
    f.b[1] = ltr2(w1, w1 + 512);
    if constexpr (NB == 4) { f.b[2] = ltr2(w0 + 8192, w0 + 8704); f.b[3] = ltr2(w1 + 8192, w1 + 8704); }
}
template <int NB> DI void frag_mma(const Frag<NB>& f, f32x4 (&acc)[4][NB]) {
#pragma unroll
    for (int j = 0; j < NB; ++j)
#pragma unroll
        for (int i = 0; i < 4; ++i) acc[i][j] = (NB == 4) ? mma(f.a[i], f.b[j], acc[i][j]) : mma(f.b[j], f.a[i], acc[i][j]);
}
template <int NB> DI void interleave() {
#pragma unroll
    for (int i = 0; i < 2 + NB; ++i) { __builtin_amdgcn_sched_group_barrier(0x8, 1, 0); __builtin_amdgcn_sched_group_barrier(0x100, 2, 0); }
    __builtin_amdgcn_sched_group_barrier(0x8, 4 * NB, 0);
}

DI void role_kv(const Params& p, rsrc_t rws, unsigned ep, int xcd, int li, int tid, int lane, int w, int ct STP_PARAM) {
    const int rp = (xcd >> 1) * 4 + (li & 3), hg = (xcd & 1) * 4 + (li >> 2);
    const int rot = 2 * (li >> 2) + 2 * (li & 3);
    const int g = lane >> 4, c16 = lane & 15;
    if (w >= 4) {
        const int lw = w - 4, r8 = lane >> 3, ch = lane & 7;
        const rsrc_t rx = mkrsrc(p.ax, 2048 * 512 * 2), rw = mkrsrc(p.wqkv, 512 * 1536 * 2), ro = mkrsrc(p.wo, 512 * 512 * 2);
        const unsigned xrow = (rp * 128 + lw * 32 + r8) * 1024;
        const unsigned vx0 = xrow + ((ch ^ (r8 >> 1)) << 4), vx1 = vx0 ^ 64;
        const unsigned vw0 = (lw * 16 + r8) * 3072 + hg * 128 + 1024 + ((ch ^ (r8 & 2)) << 4), vw1 = (vw0 ^ 64) + 8 * 3072;
        const unsigned vo = (lw * 64 + r8) * 1024 + ct * 128 + ((ch ^ (r8 & 6)) << 4);
#pragma unroll 1
        for (int s = -4; s < 8; ++s) {
            if (s >= 0) { if (s < 7) VMWAIT(24); else VMWAIT(16); BAR(); }
            if (s < 4) {
                const unsigned kb = (s + 4 + rot) & 7, slot = (s > 0 ? s - 1 : s + 4) * 32768, so = kb * (64 * 3072);
                dma_x(rx, vx0, vx1, kb * 128, slot + lw * 4096);
                dma(rw, vw0, so, slot + 16384 + lw * 2048); dma(rw, vw1, so, slot + 16384 + lw * 2048 + 1024);
                dma(rw, vw0, so + 1024, slot + 24576 + lw * 2048); dma(rw, vw1, so + 1024, slot + 24576 + lw * 2048 + 1024);
            } else if (s < 6) {
#pragma unroll 1
                for (int q = 0; q < 8; ++q) dma(ro, vo, (s - 4) * 262144 + q * 8192, 98304 + (s - 4) * 32768 + lw * 8192 + q * 1024);
            }
        }
        STAMP(1);
        BAR();
    } else {
        const int rh = w >> 1, hp = w & 1;
        f32x4 acc[4][4];
#pragma unroll
        for (int i = 0; i < 4; ++i)
#pragma unroll
            for (int j = 0; j < 4; ++j) { acc[i][j] = Z4; PIN(acc[i][j]); }
        const unsigned xa = (rh * 64 + c16) * 128 + ((g ^ (c16 >> 1)) << 4);
        const int q = c16 >> 2, fk = (q >> 1) | ((g & 1) << 1);
        const unsigned wa = 16384 + (8 * g + q) * 128 + (((2 * hp) ^ fk) << 5) + 8 * (lane & 3);
        Frag<4> f0, f1;
        unsigned slot = 0;
        BAR();
        frag_read(f0, xa, wa);
#pragma unroll 1
        for (int s = 0; s < 7; ++s) {
            SCHED_FENCE();
            frag_read(f1, slot + (xa ^ 64), slot + wa + 4096); frag_mma(f0, acc); interleave<4>();
            slot = (slot == 4 * 32768) ? 0 : slot + 32768;
            SCHED_FENCE(); BAR();
            frag_read(f0, slot + xa, slot + wa); frag_mma(f1, acc); interleave<4>();
        }
        SCHED_FENCE();
        u32x2 fx = {ep, ep};
        if (w == 2) fx = ld64(rws, WS_FX1 + lane * 8);
        frag_read(f1, slot + (xa ^ 64), slot + wa + 4096); frag_mma(f0, acc); interleave<4>();
        SCHED_FENCE();
        frag_mma(f1, acc);
        STAMP(1);
        f32x4 kv[2] = {Z4, Z4}, zz[2] = {Z4, Z4};
        const u32x4 ones = {0x3F803F80u, 0x3F803F80u, 0x3F803F80u, 0x3F803F80u};
#pragma unroll
        for (int b = 0; b < 2; ++b)
#pragma unroll
            for (int u = 0; u < 2; ++u) {
                const u32x4 A = cat(phi4(acc[2 * u][b]), phi4(acc[2 * u + 1][b])), B = cat(pkv(acc[2 * u][2 + b]), pkv(acc[2 * u + 1][2 + b]));
                kv[b] = mma(A, B, kv[b]); zz[b] = mma(A, ones, zz[b]);
            }
        const unsigned red = hp * 4096 + lane * 16;
        if (rh) { lwf128(red, kv[0]); lwf128(red + 1024, kv[1]); lwf128(red + 2048, zz[0]); lwf128(red + 3072, zz[1]); }
        if (!__all(fx[0] == ep && fx[1] == ep)) wait_flags(rws, WS_FX1, ep, lane);
        BAR();
        if (!rh) {
            kv[0] += lrf128(red); kv[1] += lrf128(red + 1024); zz[0] += lrf128(red + 2048); zz[1] += lrf128(red + 3072);
            const unsigned pb = WS_PART + (rp * 8 + hg) * PART_STRIDE;
            st128(rws, pb + hp * 1024 + lane * 16, cat(pkv(kv[0]), pkv(kv[1])));
            if (c16 == 0) { const unsigned zb = pb + 2048 + hp * 64 + g * 8; st64(rws, zb, pkv(zz[0])); st64(rws, zb + 32, pkv(zz[1])); }
        }
    }
    STAMP(2);
    if (w == 3) {
#pragma unroll 1
        for (int it = 0; it < SPIN_CAP; ++it) {
            FENCE();
            if (__builtin_amdgcn_readfirstlane(ld32(rws, WS_FPQ + (rp * 8 + hg) * 4)) == ep) break;
            __builtin_amdgcn_s_sleep(2);
        }
    }
    const int rg = w >> 1, hp = w & 1;
    unsigned pqb = WS_PQ + (((rp * 8 + hg) * 8 + 2 * rg) * 2 + hp) * 1024 + lane * 16;
    unsigned pb = WS_PART + hg * PART_STRIDE + tid * 4, pz = WS_PART + hg * PART_STRIDE + (tid >> 5) * 8 * PART_STRIDE + 2048 + (tid & 31) * 4;
    unsigned za = 32768 + 2048 + (4 * g + (c16 >> 2)) * 128 + hp * 64 + 8 * (lane & 3);
    unsigned ob = WS_O + ((rp * 8 + 2 * rg) * 16 + hg * 2 + hp) * 1024 + lane * 16;
    PIN(pqb); PIN(pb); PIN(pz); PIN(za); PIN(ob);
    if (w < 2) VMWAIT(0);
    BAR();
    STAMP(3);
    unsigned d[16], zr;
    u32x4 pq0, pq1;
#pragma unroll 1
    for (int it = 0; it < SPIN_CAP; ++it) {
        FENCE();
#pragma unroll
        for (int j = 0; j < 16; ++j) d[j] = ld32s(rws, pb, j * 8 * PART_STRIDE);
        zr = ld32(rws, pz);
        pq0 = ld128(rws, pqb); pq1 = ld128(rws, pqb + 2048);
        unsigned m = zr;
#pragma unroll
        for (int j = 0; j < 16; j += 2) m = umax3(m, d[j], d[j + 1]);
        if (!__any(m == POISON)) break;
    }
    f32x2 sm = {0.f, 0.f};
#pragma unroll
    for (int j = 0; j < 16; ++j) sm += f32x2{__builtin_bit_cast(float, d[j] << 16), __builtin_bit_cast(float, d[j] & 0xFFFF0000u)};
    lw32(32768 + tid * 4, pk(sm[0], sm[1])); lw32(32768 + 2048 + tid * 4, zr);
    BAR();
    STAMP(4);
    {
        const u32x4 kvA = lr128(32768 + hp * 1024 + lane * 16);
        const u32x2 one2 = {0x3F803F80u, 0x3F803F80u};
        const u32x2 zA[2] = {pkv(mma16(ltr(za), one2, Z4)), pkv(mma16(ltr(za + 32), one2, Z4))};
#pragma unroll
        for (int a = 0; a < 2; ++a) {
            const u32x4 pq = a ? pq1 : pq0;
            u32x2 o[2];
#pragma unroll
            for (int b = 0; b < 2; ++b) {
                const u32x2 B = {pq[2 * b], pq[2 * b + 1]};
                const f32x4 num = mma16(u32x2{kvA[2 * b], kvA[2 * b + 1]}, B, Z4), den = mma16(zA[b], B, Z4);
                o[b] = pkv(num * __builtin_amdgcn_rcpf(den[0] + 1e-6f));
            }
            st128(rws, ob + a * 16384, cat(o[0], o[1]));
        }
    }
    STAMP(5);
    VMWAIT(0);
}

constexpr unsigned L_U = 98304;
constexpr unsigned L_W = L_U + 19200;
constexpr unsigned L_SP = L_W + 20480;
constexpr unsigned L_C = L_SP + 512;
DI void role_qb(const Params& p, rsrc_t rws, unsigned ep, int xcd, int li, int tid, int lane, int w, int ct STP_PARAM) {
    const int rp = (xcd >> 1) * 4 + (li & 3), hg = (xcd & 1) * 4 + (li >> 2);
    const int rot = 2 * (li >> 2) + 2 * (li & 3) + 1;
    const int g = lane >> 4, c16 = lane & 15;
    const int me = xcd * 16 + li;
    const int jh = li >> 3, j1 = jh ? (xcd ? 16 - xcd : 8) : xcd, kb = li & 7;
    const int lw = w - 4, r8 = lane >> 3, ch = lane & 7;
    u32x4 x0, x1;
    {
        const rsrc_t rb = mkrsrc(p.bx, 2048 * 512 * 2);
        const unsigned vo = (256 * w + me) * 1024 + g * 256 + c16 * 4;
        asm volatile("buffer_load_dword %0, %8, %9, 0 offen\n\tbuffer_load_dword %1, %8, %9, 0 offen offset:64\n\t"
                     "buffer_load_dword %2, %8, %9, 0 offen offset:128\n\tbuffer_load_dword %3, %8, %9, 0 offen offset:192\n\t"
                     "buffer_load_dword %4, %8, %9, %10 offen\n\tbuffer_load_dword %5, %8, %9, %10 offen offset:64\n\t"
                     "buffer_load_dword %6, %8, %9, %10 offen offset:128\n\tbuffer_load_dword %7, %8, %9, %10 offen offset:192"
                     : "=&v"(x0[0]), "=&v"(x0[1]), "=&v"(x0[2]), "=&v"(x0[3]), "=&v"(x1[0]), "=&v"(x1[1]), "=&v"(x1[2]), "=&v"(x1[3])
                     : "v"(vo), "s"(rb), "s"(131072u) : "memory");
    }
    const rsrc_t rx = mkrsrc(p.ax, 2048 * 512 * 2), rw = mkrsrc(p.wqkv, 512 * 1536 * 2);
    const unsigned vx0 = (rp * 128 + lw * 32 + r8) * 1024 + ((ch ^ (r8 >> 1)) << 4), vx1 = vx0 ^ 64;
    const unsigned vw0 = (lw * 16 + r8) * 3072 + hg * 128 + ((ch ^ (r8 & 2)) << 4), vw1 = (vw0 ^ 64) + 8 * 3072;
    auto stage = [&](int s) {
        const unsigned kq = (s + rot) & 7, slot = (s & 3) * 24576;
        dma_x(rx, vx0, vx1, kq * 128, slot + lw * 4096);
        dma(rw, vw0, kq * (64 * 3072), slot + 16384 + lw * 2048); dma(rw, vw1, kq * (64 * 3072), slot + 16384 + lw * 2048 + 1024);
    };
    if (w >= 4) {
#pragma unroll 1
        for (int s = 0; s < 2; ++s) stage(s);
    }
    if (w < 5) {
        const int k = w, k4 = 4 * k, sU = (0x04004 >> k4) & 15, sQ = (0x40040 >> k4) & 15;
        const int P = (((c16 & ((0x7F7FF >> k4) & 15)) << sU) + (sU ? j1 : 0)) << ((0x00833 >> k4) & 15);
        const int n0 = P * (((4 * (g & ((0x31133 >> k4) & 15))) << sQ) + (sQ ? j1 : 0)), dn = P << sQ;
        const Cx t = angles(n0, dn, 2047, 1.f / 2048);
        Mat m = tomat(t);
        if (k > 2) m = Mat{__builtin_bit_cast(u32x4, t.re), __builtin_bit_cast(u32x4, t.im)};
        if (k == 2 && (g >> 1) != (c16 >> 3)) m = Mat{u32x4{0, 0, 0, 0}, u32x4{0, 0, 0, 0}};
        lw128(L_C + k * 2048 + lane * 16, m.re); lw128(L_C + k * 2048 + 1024 + lane * 16, m.im);
    }
    Mat F = tomat(angles(4 * g * c16, c16, 15, 1.f / 16));
    Cx tw1 = angles(4 * g * c16, c16, 255, 1.f / 256);
    const int km0 = 16 * w + c16, km1 = km0 ? 256 - km0 : 128;
    unsigned ua = L_U + (2 * w) * 1072 + g * 272 + c16 * 16;
    unsigned a0 = L_U + 4 * g * 1072 + ((km0 >> 2) & 3) * 272 + (km0 >> 4) * 16 + (km0 & 3) * 4;
    unsigned a1s = L_U + 4 * g * 1072 + ((km1 >> 2) & 3) * 272 + (km1 >> 4) * 16 + (km1 & 3) * 4;
    unsigned sw0 = L_W + w * 2304 + c16 * 144 + g * 16, sw1 = L_W + w * 2304 + ((16 - c16) & 15) * 144 + 64 + g * 16;
    unsigned sr0 = L_W + w * 2304 + r8 * 144 + ch * 16;
    unsigned tb1 = WS_T1 + me * TB + w * 2048 + lane * 16, obp = WS_O + ((rp * 8 + w) * 16 + hg * 2) * 1024 + lane * 16;
    PIN(F.re); PIN(F.im); PIN(tw1.re); PIN(tw1.im); PIN(ua); PIN(a0); PIN(a1s); PIN(sw0); PIN(sw1); PIN(sr0); PIN(tb1); PIN(obp);
    asm volatile("s_cmp_lt_u32 %2, 4\n\ts_cbranch_scc1 1f\n\ts_waitcnt vmcnt(12)\n\ts_branch 2f\n1:\n\ts_waitcnt vmcnt(0)\n2:"
                 : "+v"(x0), "+v"(x1) : "s"(w) : "memory", "scc");
    STAMP(1);
    {
        lw128(ua, pk4(dftA(twpk(dftA(x0, F), tw1), F))); lw128(ua + 1072, pk4(dftA(twpk(dftA(x1, F), tw1), F)));
        BAR();
        const u32x4 d0 = {lr32(a0), lr32(a0 + 1072), lr32(a0 + 2144), lr32(a0 + 3216)};
        const u32x4 d1 = {lr32(a1s), lr32(a1s + 1072), lr32(a1s + 2144), lr32(a1s + 3216)};
        lw128(sw0, pk4(dftA(d0, F))); lw128(sw1, pk4c(dftA(d1, F)));
        const u32x4 s0 = lr128(sr0), s1 = lr128(sr0 + 8 * 144);
        st128(rws, tb1, s0); st128(rws, tb1 + 1024, s1);
        const u32x4 P4 = {POISON, POISON, POISON, POISON};
        st128(rws, obp, P4); st128(rws, obp + 1024, P4);
        if (tid < 136) st128(rws, WS_PART + (rp * 8 + hg) * PART_STRIDE + tid * 16, P4);
    }
    STAMP(2);
    const bool special = (kb == 0 && w == 0);
    float sp[6][4];
    if (w >= 4) {
#pragma unroll 1
        for (int s = -2; s < 5; ++s) {
            if (s >= 0) { if (s == 0) VMWAIT(22); else if (s == 1) VMWAIT(16); else VMWAIT(12); BAR(); }
            if (s != 0) stage(s < 0 ? s + 4 : s + 3);
        }
        struct __attribute__((packed, aligned(2))) U4 { u32x4 v; };
        const int j2 = (tid >> 1) & 127, ks = j1 + 16 * j2, hf = (tid & 1) * 8;
        const int op = ks * 257 + 16 * kb + hf, oq = ((2048 - ks) & 2047) * 257 + 241 - 16 * kb + hf;
        const u32x4 l0 = ((const U4*)(p.wr + op))->v, l1 = ((const U4*)(p.wi + op))->v, l2 = ((const U4*)(p.wr + oq))->v, l3 = ((const U4*)(p.wi + oq))->v;
        FENCE();
        VMWAIT(16); BAR(); VMWAIT(10); BAR(); VMWAIT(4); BAR();
        const unsigned wa = L_W + j2 * 40 + (tid & 1) * 16;
        lw64(wa, u32x2{l0[0], l0[1]}); lw64(wa + 8, u32x2{l0[2], l0[3]});
        lw64(wa + 5120, u32x2{l1[0], l1[1]}); lw64(wa + 5128, u32x2{l1[2], l1[3]});
        lw64(wa + 10240, u32x2{l2[0], l2[1]}); lw64(wa + 10248, u32x2{l2[2], l2[3]});
        lw64(wa + 15360, u32x2{l3[0], l3[1]}); lw64(wa + 15368, u32x2{l3[2], l3[3]});
        if (w == 7) wait_flags(rws, WS_FX1, ep, lane);
    } else {
        const int rh = w >> 1, hp = w & 1;
        f32x4 acc[4][2];
#pragma unroll
        for (int i = 0; i < 4; ++i) { acc[i][0] = Z4; acc[i][1] = Z4; PIN(acc[i][0]); PIN(acc[i][1]); }
        const unsigned xa = (rh * 64 + c16) * 128 + ((g ^ (c16 >> 1)) << 4);
        const int q = c16 >> 2, fk = (q >> 1) | ((g & 1) << 1);
        const unsigned wa = 16384 + (8 * g + q) * 128 + (((2 * hp) ^ fk) << 5) + 8 * (lane & 3);
        Frag<2> f0, f1;
        BAR();
        frag_read(f0, xa, wa);
#pragma unroll 1
        for (int s = 0; s < 7; ++s) {
            const unsigned slot = (s & 3) * 24576, nslot = ((s + 1) & 3) * 24576;
            SCHED_FENCE();
            frag_read(f1, slot + (xa ^ 64), slot + wa + 4096); frag_mma(f0, acc); interleave<2>();
            SCHED_FENCE();
            if (s == 1) VMWAIT(0);
            BAR();
            if (s == 1 && tid == 0) st32(rws, WS_FX1 + me * 4, ep);
            frag_read(f0, nslot + xa, nslot + wa); frag_mma(f1, acc); interleave<2>();
        }
        SCHED_FENCE();
        if (special) {
#pragma unroll
            for (int r = 0; r < 4; ++r) {
                const int ks = j1 + 16 * (c16 + 64 * (g & 1) + 16 * r), kn = (2048 - ks) & 2047;
                sp[0][r] = bf(p.wr[kn * 257]); sp[1][r] = bf(p.wi[kn * 257]);
                sp[2][r] = bf(p.wr[ks * 257 + 256]); sp[3][r] = bf(p.wi[ks * 257 + 256]);
                sp[4][r] = bf(p.wr[ks * 257 + 128]); sp[5][r] = bf(p.wi[ks * 257 + 128]);
            }
        }
        frag_read(f1, 3 * 24576 + (xa ^ 64), 3 * 24576 + wa + 4096); frag_mma(f0, acc); interleave<2>();
        SCHED_FENCE();
        frag_mma(f1, acc);
        const unsigned qb = WS_PQ + (((rp * 8 + hg) * 8 + 4 * rh) * 2 + hp) * 1024 + lane * 16;
#pragma unroll
        for (int i = 0; i < 4; ++i) st128(rws, qb + i * 2048, cat(phi4(acc[i][0]), phi4(acc[i][1])));
    }
    STAMP(3);
    const int lam = tid >> 3, s2a = (lam >> 5) * 32 + (lam & 3) * 8 + ((lam >> 2) & 7);
    unsigned sb = L_U + ((ch & 3) * 2 + (ch >> 2)) * 1040 + ((lam >> 5) * 16 + ((lam >> 2) & 7)) * 16 + (lam & 3) * 4;
    unsigned tb2 = WS_T1 + s2a * TB + (kb * 16 + j1) * 128 + ch * 16;
    PIN(sb); PIN(tb2);
    VMWAIT(0);
    BAR();
    if (tid == 0) st32(rws, WS_FPQ + (rp * 8 + hg) * 4, ep);
    const u32x4 i0 = ld128(rws, tb2), i1 = ld128s(rws, tb2, 64 * TB);
    unsigned isp = 0;
    if (kb == 0 && tid < 128) isp = ld32(rws, WS_T1 + tid * TB + ((16 - j1) & 15) * 128);
    FENCE();
    Cx a1, bz, a2, bv;
    {
        const int x = g >> 1, col = 2 * w + x;
        const float th = (float)(16 * kb + col) * (1.f / 512), sn = __builtin_amdgcn_sinf(th), cs = __builtin_amdgcn_cosf(th);
        const unsigned wp = L_W + (c16 + 64 * (g & 1)) * 40 + col * 2, wq = wp + 10240 + 30 - col * 4;
        f32x4 pr, pi, qr, qi;
#pragma unroll
        for (int r = 0; r < 4; ++r) { pr[r] = lrbf(wp + r * 640); pi[r] = lrbf(wp + r * 640 + 5120); qr[r] = lrbf(wq + r * 640); qi[r] = lrbf(wq + r * 640 + 5120); }
        qi = -qi;
        const bool sx = special && x == 0;
        if (special) {
#pragma unroll
            for (int r = 0; r < 4; ++r) if (sx) {
                pr[r] = 0.5f * (pr[r] + sp[0][r]); pi[r] = 0.5f * (pi[r] - sp[1][r]); qr[r] = 0.5f * (qr[r] + sp[2][r]); qi[r] = 0.5f * (qi[r] + sp[3][r]); }
        }
        const float sc = 1.f / 1048576;
        const f32x4 sr = (pr + qr) * sc, si = (pi + qi) * sc, dr = (pr - qr) * sc, di = (pi - qi) * sc;
        a1.re = sr - dr * sn; a1.im = si - di * sn; a2.re = sr + dr * sn; a2.im = si + di * sn;
        bz.re = dr * cs; bz.im = di * cs; bv = bz;
        if (special) {
#pragma unroll
            for (int r = 0; r < 4; ++r) if (sx) { a2.re[r] = 2.f * sc * sp[4][r]; a2.im[r] = 2.f * sc * sp[5][r]; bv.re[r] = 0.f; bv.im[r] = 0.f; }
        }
    }
    const unsigned ca = L_C + lane * 16;
    Mat F1 = {lr128(ca), lr128(ca + 1024)}, F2 = {lr128(ca + 2048), lr128(ca + 3072)}, G = {lr128(ca + 4096), lr128(ca + 5120)};
    Cx twa = {lrf128(ca + 6144), lrf128(ca + 7168)}, twb = {lrf128(ca + 8192), lrf128(ca + 9216)};
    unsigned ta = L_U + ((w >> 1) * 2 + 8 * (w & 1)) * 1040 + lane * 16;
    unsigned tb3 = WS_T2 + (kb * 16 + j1) * TB + s2a * 128 + ch * 16;
    PIN(a1.re); PIN(a1.im); PIN(a2.re); PIN(a2.im); PIN(bz.re); PIN(bz.im); PIN(bv.re); PIN(bv.im);
    PIN(F1.re); PIN(F1.im); PIN(F2.re); PIN(F2.im); PIN(G.re); PIN(G.im); PIN(twa.re); PIN(twa.im); PIN(twb.re); PIN(twb.im); PIN(ta); PIN(tb3);
    lw32(sb, i0[0]); lw32(sb + 128, i0[1]); lw32(sb + 8320, i0[2]); lw32(sb + 8320 + 128, i0[3]);
    lw32(sb + 512, i1[0]); lw32(sb + 512 + 128, i1[1]); lw32(sb + 512 + 8320, i1[2]); lw32(sb + 512 + 8320 + 128, i1[3]);
    if (kb == 0 && tid < 128) lw32(L_SP + ((tid >> 5) * 8 + (tid & 7)) * 16 + ((tid >> 3) & 3) * 4, isp ^ 0x80000000u);
    BAR();
    STAMP(4);
    {
        const u32x4 zi = lr128(ta), vi = lr128(ta + 1040);
        const Cx Z = dftB(twpk(dftA(zi, F1), twa), G), V = dftB(twpk(dftA(vi, F1), twa), G);
        Cx Vz = V;
        if (special) {
            u32x4 v0 = lr128(L_SP + (g * 8 + (c16 & 7)) * 16);
            if (c16 >= 8) v0 = vi;
            Vz = dftB(twpk(dftA(v0, F1), twa), G);
        }
        const u32x4 zc = zip(a1.re * Z.re - a1.im * Z.im - bz.re * Vz.im - bz.im * Vz.re, bz.im * Vz.im - a1.re * Z.im - a1.im * Z.re - bz.re * Vz.re);
        const u32x4 vc = zip(a2.re * V.re - a2.im * V.im + bv.re * Z.im + bv.im * Z.re, bv.re * Z.re - a2.re * V.im - a2.im * V.re - bv.im * Z.im);
        lw128(ta, pk4(dftB(twpk(dftA(zc, G), twb), F2))); lw128(ta + 1040, pk4c(dftB(twpk(dftA(vc, G), twb), F2)));
        BAR();
        const u32x4 o0 = {lr32(sb), lr32(sb + 128), lr32(sb + 8320), lr32(sb + 8320 + 128)};
        const u32x4 o1 = {lr32(sb + 512), lr32(sb + 512 + 128), lr32(sb + 512 + 8320), lr32(sb + 512 + 8320 + 128)};
        st128(rws, tb3, o0); st128(rws, tb3 + 64 * 128, o1);
    }
    STAMP(5);
    VMWAIT(0);
    BAR();
    if (tid == 0) st32(rws, WS_FX2 + me * 4, ep);
    if (w >= 4) {
        const rsrc_t ro = mkrsrc(p.wo, 512 * 512 * 2);
        const unsigned vo = (lw * 128 + r8) * 1024 + ct * 128 + ((ch ^ (r8 & 6)) << 4);
#pragma unroll 1
        for (int q = 0; q < 16; ++q) dma(ro, vo, q * 8192, lw * 16384 + q * 1024);
    }
    unsigned ra[4];
#pragma unroll
    for (int i = 0; i < 4; ++i) {
        const int km = 64 * g + 16 * i + c16, kq = km < 128 ? km : (256 - km) & 127;
        ra[i] = L_U + ((km >> 7) * 16 + 2 * w) * 592 + (kq + 16 * (kq >> 6)) * 4;
        PIN(ra[i]);
    }
    STAMP(6);
    if (w < 4) {
        const int set = g & 1, s1 = set ? (16 - c16) & 15 : c16, k0 = 16 * (2 * w + (g >> 1));
        unsigned u2 = L_U + (set * 16 + s1) * 592 + (k0 + 16 * (k0 >> 6)) * 4;
        unsigned tb = WS_T2 + ((2 * w + (c16 >> 3)) * 16 + 4 * g) * TB + me * 128 + ch * 16;
        PIN(u2); PIN(tb);
        wait_flags(rws, WS_FX2, ep, lane);
        STAMP(10);
        const u32x4 l0 = ld128(rws, tb), l1 = ld128s(rws, tb, TB), l2 = ld128s(rws, tb, 2 * TB), l3 = ld128s(rws, tb, 3 * TB);
        u32x4 t[4];
#pragma unroll
        for (int e = 0; e < 4; ++e) t[e] = pk4(dftA(u32x4{l0[e], l1[e], l2[e], l3[e]}, F));
#pragma unroll
        for (int r = 0; r < 4; ++r) lw128(u2 + r * 16, u32x4{t[0][r], t[1][r], t[2][r], t[3][r]});
    }
    BAR();
    STAMP(11);
    {
        const u32x4 d0 = {lr32(ra[0]), lr32(ra[1]), lr32(ra[2]), lr32(ra[3])};
        const u32x4 d1 = {lr32(ra[0] + 592), lr32(ra[1] + 592), lr32(ra[2] + 592), lr32(ra[3] + 592)};
        const u32x4 y0 = pk4c(dftB(twpk(dftA(d0, F), tw1), F)), y1 = pk4c(dftB(twpk(dftA(d1, F), tw1), F));
        unsigned* o = (unsigned*)(p.out + (256 * w + me) * 1024 + 512) + 64 * g + c16;
#pragma unroll
        for (int r = 0; r < 4; ++r) { o[16 * r] = y0[r]; o[16 * r + 128 * 512] = y1[r]; }
    }
    if (w >= 4) VMWAIT(8);
}

__global__ void __launch_bounds__(512, 2) fused_kernel(Params p) {
    const int b = blockIdx.x, xcd = b & 7, i = b >> 3, li = i & 15;
    const int tid = threadIdx.x, lane = tid & 63, w = __builtin_amdgcn_readfirstlane(tid >> 6);
    const int rt = xcd * 4 + (i & 3), ct = i >> 2;
    __shared__ __attribute__((aligned(16))) char smem[LDS_BYTES];
    asm volatile("" :: "s"((unsigned)(size_t)(LAS char*)smem));
    const rsrc_t rws = mkrsrc(p.ws, WS_END);
    const unsigned ep = *(const __attribute__((address_space(4))) unsigned*)(p.ws + WS_EPOCH) + 1;
    STAMP(0);
    unsigned wob;
    if (i < 16) { role_kv(p, rws, ep, xcd, li, tid, lane, w, ct STP_ARG); wob = 98304; }
    else        { role_qb(p, rws, ep, xcd, li, tid, lane, w, ct STP_ARG); wob = 0; }
    const int g = lane >> 4, c16 = lane & 15, q = c16 >> 2;
    unsigned ob = WS_O + (rt * 4 + w) * 16384 + lane * 16;
    unsigned wa = wob + (4 * g + q) * 128 + 8 * (lane & 3) + (((2 * g + (q >> 1)) & 3) << 5);
    PIN(ob); PIN(wa);
    BAR();
    STAMP(7);
    if (w < 4) {
        f32x4 acc[4];
#pragma unroll 1
        for (int it = 0; it < SPIN_CAP; ++it) {
            FENCE();
            u32x4 o[16];
#pragma unroll
            for (int k = 0; k < 16; ++k) o[k] = ld128s(rws, ob, k * 1024);
            unsigned m = 0;
#pragma unroll
            for (int t = 0; t < 4; ++t) acc[t] = Z4;
#pragma unroll
            for (int k = 0; k < 16; ++k) {
#pragma unroll
                for (int t = 0; t < 4; ++t) { const unsigned a = (wa ^ (t << 5)) + k * 4096; acc[t] = mma(ltr2(a, a + 2048), o[k], acc[t]); }
                m = umax4(m, o[k]);
            }
            if (!__any(m == POISON)) break;
        }
        STAMP(8);
        const unsigned sc = 65536 + w * 2304;
#pragma unroll
        for (int t = 0; t < 4; ++t) lw64(sc + c16 * 144 + t * 32 + g * 8, pkv(acc[t]));
        const int r8 = lane >> 3, ch = lane & 7;
        const u32x4 s0 = lr128(sc + r8 * 144 + ch * 16), s1 = lr128(sc + (r8 + 8) * 144 + ch * 16);
        u32x4* o = (u32x4*)(p.out + (rt * 64 + w * 16 + r8) * 1024 + ct * 64 + ch * 8);
        o[0] = s0; o[1024] = s1;
        if (b == 0 && tid == 0) st32(rws, WS_EPOCH, ep);
        STAMP(9);
    }
}

__global__ void k_stamp(unsigned long long* p) { unsigned long long t; asm volatile("s_memrealtime %0\n\ts_waitcnt lgkmcnt(0)" : "=s"(t)); if (threadIdx.x == 0) p[0] = t; }
__global__ void k_spin(const unsigned long long* p) { const unsigned long long t = p[0]; while (__builtin_amdgcn_s_memrealtime() - t < TCAL) __builtin_amdgcn_s_sleep(1); }
extern "C" void kernel_launch(void* const* d_in, const int* in_sizes, int n_in,
                              void* d_out, int out_size, void* d_ws, size_t ws_size,
                              hipStream_t stream) {
    Params p;
    p.ax = (const unsigned short*)d_in[0]; p.wqkv = (const unsigned short*)d_in[1]; p.wo = (const unsigned short*)d_in[2];
    p.bx = (const unsigned short*)d_in[3]; p.wr = (const unsigned short*)d_in[4]; p.wi = (const unsigned short*)d_in[5];
    p.out = (unsigned short*)d_out; p.ws = (unsigned char*)d_ws;
    k_stamp<<<1, 64, 0, stream>>>((unsigned long long*)((char*)d_ws + (32u << 20)));
    fused_kernel<<<256, 512, 0, stream>>>(p);
    k_spin<<<1, 64, 0, stream>>>((const unsigned long long*)((char*)d_ws + (32u << 20)));
}
```

```cpp
#include <hip/hip_runtime.h>
#include <stdint.h>

typedef short    s16x4 __attribute__((ext_vector_type(4)));
typedef __bf16   bf16x8 __attribute__((ext_vector_type(8)));
typedef __bf16   bf16x2 __attribute__((ext_vector_type(2)));
typedef float    f32x2 __attribute__((ext_vector_type(2)));
typedef float    f32x4 __attribute__((ext_vector_type(4)));
typedef float    f32x16 __attribute__((ext_vector_type(16)));
typedef unsigned u32x2 __attribute__((ext_vector_type(2)));
typedef unsigned u32x4 __attribute__((ext_vector_type(4)));
typedef __amdgpu_buffer_rsrc_t rsrc_t;
#define LAS __attribute__((address_space(3)))
#define DI __device__ __forceinline__

constexpr unsigned WS_EPOCH = 0;
constexpr unsigned PART_STRIDE = 2688, WS_PART = 4096;
constexpr unsigned TB = 17024;
constexpr unsigned WS_T1 = 1u << 19, WS_T2 = WS_T1 + 128 * TB;
constexpr unsigned WS_O = WS_T2 + 128 * TB, WS_END = WS_O + (1u << 21);
constexpr int SPIN_CAP = 1 << 15;
constexpr unsigned LDS_BYTES = 163840;
#define OUT_AUX 16
#define STAMP(i) do {} while (0)
#define STP_PARAM
#define STP_ARG

struct Params {
    const unsigned short *ax, *wqkv, *wo, *bx, *wr, *wi;
    unsigned short* out;
    unsigned char* ws;
};

DI rsrc_t mkrsrc(const void* ptr, unsigned bytes) { return __builtin_amdgcn_make_buffer_rsrc((void*)ptr, 0, bytes, 0x00020000); }
DI u32x4 ld128(rsrc_t r, unsigned off)  { return __builtin_amdgcn_raw_buffer_load_b128(r, off, 0, 16); }
DI u32x2 ld64(rsrc_t r, unsigned off)   { return __builtin_amdgcn_raw_buffer_load_b64(r, off, 0, 16); }
DI unsigned ld32(rsrc_t r, unsigned off){ return __builtin_amdgcn_raw_buffer_load_b32(r, off, 0, 16); }
DI u32x2 ld64s(rsrc_t r, unsigned off, unsigned so)   { return __builtin_amdgcn_raw_buffer_load_b64(r, off, so, 16); }
DI u32x4 ld128s(rsrc_t r, unsigned off, unsigned so)  { return __builtin_amdgcn_raw_buffer_load_b128(r, off, so, 16); }
DI unsigned ld32s(rsrc_t r, unsigned off, unsigned so){ return __builtin_amdgcn_raw_buffer_load_b32(r, off, so, 16); }
DI void st128(rsrc_t r, unsigned off, u32x4 v) { __builtin_amdgcn_raw_buffer_store_b128(v, r, off, 0, 16); }
DI void st64(rsrc_t r, unsigned off, u32x2 v) { __builtin_amdgcn_raw_buffer_store_b64(v, r, off, 0, 16); }
DI void st32(rsrc_t r, unsigned off, unsigned v) { __builtin_amdgcn_raw_buffer_store_b32(v, r, off, 0, 16); }
#define FENCE() asm volatile("" ::: "memory")
#define BAR() asm volatile("s_waitcnt lgkmcnt(0)\n\ts_barrier" ::: "memory")
#define VMWAIT(n) asm volatile("s_waitcnt vmcnt(" #n ")" ::: "memory")
#define PIN(x) asm volatile("" : "+v"(x))
#define SCHED_FENCE() __builtin_amdgcn_sched_barrier(0)
DI void dma(rsrc_t r, unsigned voff, unsigned soff, unsigned lds) {
    asm volatile("s_mov_b32 m0, %0\n\ts_nop 0\n\tbuffer_load_dwordx4 %1, %2, %3 offen lds"
                 :: "s"(lds), "v"(voff), "s"(r), "s"(soff) : "memory");
}
DI unsigned pk(float lo, float hi) { f32x2 v = {lo, hi}; return __builtin_bit_cast(unsigned, __builtin_convertvector(v, bf16x2)); }
DI u32x2 pkv(f32x4 v) { return u32x2{pk(v[0], v[1]), pk(v[2], v[3])}; }
DI u32x4 cat(u32x2 a, u32x2 b) { return u32x4{a[0], a[1], b[0], b[1]}; }
DI f32x4 mma(u32x4 a, u32x4 b, f32x4 c) {
    return __builtin_amdgcn_mfma_f32_16x16x32_bf16(__builtin_bit_cast(bf16x8, a), __builtin_bit_cast(bf16x8, b), c, 0, 0, 0); }
DI f32x16 mma32(u32x4 a, u32x4 b, f32x16 c) {
    return __builtin_amdgcn_mfma_f32_32x32x16_bf16(__builtin_bit_cast(bf16x8, a), __builtin_bit_cast(bf16x8, b), c, 0, 0, 0); }
DI f32x4 mma16(u32x2 a, u32x2 b, f32x4 c) {
    return __builtin_amdgcn_mfma_f32_16x16x16bf16_1k(__builtin_bit_cast(s16x4, a), __builtin_bit_cast(s16x4, b), c, 0, 0, 0); }
#define LP(T, a) ((LAS T*)(size_t)(a))
DI u32x4 lr128(unsigned a) { return *LP(const u32x4, a); }
DI f32x4 lrf128(unsigned a) { return *LP(const f32x4, a); }
DI u32x2 lr64(unsigned a) { return *LP(const u32x2, a); }
DI unsigned lr32(unsigned a) { return *LP(const unsigned, a); }
DI float bf(unsigned short v) { return __builtin_bit_cast(float, (unsigned)v << 16); }
DI float lrbf(unsigned a) { return bf(*LP(const unsigned short, a)); }
DI void lw128(unsigned a, u32x4 v) { *LP(u32x4, a) = v; }
DI void lwf128(unsigned a, f32x4 v) { *LP(f32x4, a) = v; }
DI void lw64(unsigned a, u32x2 v) { *LP(u32x2, a) = v; }
DI void lw32(unsigned a, unsigned v) { *LP(unsigned, a) = v; }
DI u32x2 ltr(unsigned a) { return __builtin_bit_cast(u32x2, __builtin_amdgcn_ds_read_tr16_b64_v4i16(LP(s16x4, a))); }
DI u32x4 ltr2(unsigned a, unsigned b) { return cat(ltr(a), ltr(b)); }
constexpr unsigned TM = 0x00010001u;
struct Tag { u32x4 bits, lo, hi; };
DI u32x4 tag_bits(unsigned ep, int unit) {
    const unsigned t = ((ep << 2) | 2u) >> (8 * unit);
    u32x4 m;
#pragma unroll
    for (int k = 0; k < 4; ++k) m[k] = ((t >> (2 * k)) & 1u) | (((t >> (2 * k + 1)) & 1u) << 16);
    return m;
}
DI Tag make_tag(unsigned ep, int unit) { Tag t; t.bits = tag_bits(ep, unit); t.lo = (~t.bits & 1u) << 16; t.hi = ~t.bits & 0x10000u; return t; }
DI unsigned pkt1(float a, float b, const Tag& t, int k) {
    const unsigned v = __builtin_amdgcn_perm(__builtin_bit_cast(unsigned, b) + t.hi[k], __builtin_bit_cast(unsigned, a) + t.lo[k], 0x07060302u);
    return __builtin_amdgcn_bitop3_b32(v, TM, t.bits[k], 0xBA);
}
DI u32x4 zipt(f32x4 a, f32x4 b, const Tag& t) { return u32x4{pkt1(a[0], b[0], t, 0), pkt1(a[1], b[1], t, 1), pkt1(a[2], b[2], t, 2), pkt1(a[3], b[3], t, 3)}; }
DI u32x4 catt(f32x4 a, f32x4 b, const Tag& t) { return u32x4{pkt1(a[0], a[1], t, 0), pkt1(a[2], a[3], t, 1), pkt1(b[0], b[1], t, 2), pkt1(b[2], b[3], t, 3)}; }
DI unsigned mis1(unsigned m, unsigned v, unsigned bits) { return __builtin_amdgcn_bitop3_b32(m, v, bits, 0xF6); }
DI unsigned mis4(unsigned m, u32x4 v, u32x4 bits) { return mis1(mis1(mis1(mis1(m, v[0], bits[0]), v[1], bits[1]), v[2], bits[2]), v[3], bits[3]); }
DI bool stale(unsigned m) { return __any(m & TM); }
DI u32x2 phi4(f32x4 t) {
    f32x4 e, m;
#pragma unroll
    for (int r = 0; r < 4; ++r) {
        e[r] = __builtin_amdgcn_fmed3f(__builtin_amdgcn_exp2f(t[r] * 1.44269504f), 0.f, 1.f);
        m[r] = __builtin_elementwise_maximum(t[r], 0.f);
    }
    return pkv((m + e) + 1e-6f);
}
constexpr f32x4 Z4 = {0.f, 0.f, 0.f, 0.f};

struct Mat { u32x4 re, im; };
struct Cx  { f32x4 re, im; };
DI Cx angles(int n0, int dn, int mask, float inv) {
    Cx t;
#pragma unroll
    for (int r = 0; r < 4; ++r) { float a = (float)((n0 + r * dn) & mask) * inv;
        t.re[r] = __builtin_amdgcn_cosf(a); t.im[r] = __builtin_amdgcn_sinf(a); }
    return t;
}
DI u32x4 zip(f32x4 a, f32x4 b) { return u32x4{pk(a[0], b[0]), pk(a[1], b[1]), pk(a[2], b[2]), pk(a[3], b[3])}; }
DI Mat tomat(Cx t) { return Mat{zip(t.re, t.im), zip(-t.im, t.re)}; }
DI Cx dftA(u32x4 d, const Mat& f) { return Cx{mma(d, f.re, Z4), mma(d, f.im, Z4)}; }
DI Cx dftB(u32x4 d, const Mat& f) { return Cx{mma(f.re, d, Z4), mma(f.im, d, Z4)}; }
DI u32x4 twpk(Cx v, const Cx& w) { return zip(v.re * w.re + v.im * w.im, v.im * w.re - v.re * w.im); }
DI u32x4 pk4(Cx v)  { return zip(v.re, v.im); }
DI u32x4 pk4c(Cx v) { return zip(v.re, v.im) ^ 0x80000000u; }

DI unsigned read_epoch(const unsigned char* ws) {
    unsigned e;
    asm volatile("s_load_dword %0, %1, 0x0\n\ts_waitcnt lgkmcnt(0)" : "=s"(e) : "s"(ws) : "memory");
    return e + 1;
}
DI f32x4 q4(const f32x16& v, int i) { return f32x4{v[4 * i], v[4 * i + 1], v[4 * i + 2], v[4 * i + 3]}; }
struct Frag { u32x4 a[2][2], b[2][3]; };
DI void frag_read(Frag& f, unsigned xa, unsigned wa) {
#pragma unroll
    for (int k = 0; k < 2; ++k) {
        f.a[k][0] = lr128(xa ^ (k * 32)); f.b[k][0] = ltr2(wa + k * 2048, wa + k * 2048 + 512);
        f.a[k][1] = lr128((xa ^ (k * 32)) + 4096);
        f.b[k][1] = ltr2(wa + k * 2048 + 8192, wa + k * 2048 + 8704); f.b[k][2] = ltr2(wa + k * 2048 + 16384, wa + k * 2048 + 16896);
    }
}
DI void frag_mma(const Frag& f, f32x16 (&acc)[2][3]) {
#pragma unroll
    for (int k = 0; k < 2; ++k)
#pragma unroll
        for (int j = 0; j < 3; ++j)
#pragma unroll
            for (int i = 0; i < 2; ++i) acc[i][j] = (j < 2) ? mma32(f.a[k][i], f.b[k][j], acc[i][j]) : mma32(f.b[k][j], f.a[k][i], acc[i][j]);
}
DI void interleave() {
#pragma unroll
    for (int i = 0; i < 8; ++i) { __builtin_amdgcn_sched_group_barrier(0x8, 1, 0); __builtin_amdgcn_sched_group_barrier(0x100, 2, 0); }
    __builtin_amdgcn_sched_group_barrier(0x8, 4, 0);
}
#define DEPTH 2
#define ROTK 0
constexpr unsigned CODE_LINES = 4;
DI void touch(unsigned& sink, const void* ptr) { asm volatile("global_load_dword %0, %1, off" : "=v"(sink) : "v"(ptr) : "memory"); }

constexpr unsigned SLOT = 40960;
constexpr unsigned A_RED = 65536, A_SUM = 73728 + 1024;
DI unsigned role_a(const Params& p, rsrc_t rws, int xcd, int li, int tid, int lane, int w, int rt, int ct, const char* code STP_PARAM) {
    const int rp = (xcd >> 1) * 4 + (li & 3), hg = (xcd & 1) * 4 + (li >> 2);
    const int rot = ROTK == 0 ? 0 : ROTK == 1 ? 2 * (li >> 2) + 2 * (li & 3) : (li & 1) * 4;
    const int g = lane >> 4, c16 = lane & 15, h = lane >> 5, r32 = lane & 31, rh = (w >> 1) & 1, hp = w & 1;
    const unsigned xa = (rh * 64 + r32) * 128 + ((h ^ ((r32 >> 1) & 7)) << 4);
    const unsigned wa = 16384 + (8 * h + (c16 >> 2)) * 128 + ((hp ^ (c16 >> 3)) << 6) + (g & 1) * 32 + 8 * (lane & 3);
    const unsigned pg = WS_PART + hg * PART_STRIDE;
    unsigned ep;
    u32x4 pq[2][2];
    if (w >= 4) {
        const int lw = w - 4, r8 = lane >> 3, ch = lane & 7;
        const rsrc_t rx = mkrsrc(p.ax, 2048 * 512 * 2), rw = mkrsrc(p.wqkv, 512 * 1536 * 2), ro = mkrsrc(p.wo, 512 * 512 * 2);
        const unsigned vx0 = (rp * 128 + lw * 32 + r8) * 1024 + ((ch ^ (r8 >> 1)) << 4), vx1 = vx0 ^ 64;
        const unsigned vw = (lw * 16 + r8) * 3072 + hg * 128 + ((ch ^ ((r8 & 2) << 1)) << 4);
        auto stage = [&](int s) {
            const unsigned kb = (s + rot) & 7, so = kb * (64 * 3072), xo = kb * 128;
            const unsigned lx = (s & 3) * SLOT + lw * 4096, lk = (s & 3) * SLOT + 16384 + lw * 2048;
            dma(rx, vx0, xo, lx); dma(rx, vx1, xo + 8192, lx + 1024);
            dma(rw, vw, so + 1024, lk); dma(rw, vw, so + 25600, lk + 1024);
            dma(rx, vx0, xo + 16384, lx + 2048); dma(rx, vx1, xo + 24576, lx + 3072);
            dma(rw, vw, so + 2048, lk + 8192); dma(rw, vw, so + 26624, lk + 9216);
            dma(rw, vw, so, lk + 16384); dma(rw, vw, so + 24576, lk + 17408);
        };
        const unsigned vo = (lw * 64 + r8) * 1024 + ct * 128 + ((ch ^ (r8 & 6)) << 4);
        auto wo_part = [&](int u) {
#pragma unroll 1
            for (int q = 0; q < 8; ++q) dma(ro, vo, u * 262144 + q * 8192, u * 32768 + lw * 8192 + q * 1024);
        };
#pragma unroll 1
        for (int s = -2; s < 6; ++s) {
            if (s >= 0) { VMWAIT(10); BAR(); }
            stage(s + 2);
        }
        VMWAIT(10); BAR(); wo_part(0); VMWAIT(8); BAR(); wo_part(1);
        STAMP(1);
        ep = read_epoch(p.ws);
        BAR();
    } else {
        unsigned sink;
        if (w == 0 && lane < (int)CODE_LINES) touch(sink, code + (li * CODE_LINES + lane) * 128);
        f32x16 acc[2][3];
#pragma unroll
        for (int i = 0; i < 2; ++i)
#pragma unroll
            for (int j = 0; j < 3; ++j) { acc[i][j] = 0.f; PIN(acc[i][j]); }
        unsigned red = A_RED + hp * 4096 + lane * 16;
        unsigned pst = pg + rp * 8 * PART_STRIDE + hp * 1024 + lane * 16;
        unsigned pz = c16 < 4 ? pg + rp * 8 * PART_STRIDE + 2048 + hp * 256 + g * 64 + c16 * 16 : WS_END;
        PIN(red); PIN(pst); PIN(pz);
        ep = read_epoch(p.ws);
        asm volatile("s_waitcnt vmcnt(0)" : "+v"(sink) :: "memory");
        Tag tg = make_tag(ep, lane & 3);
        PIN(tg.bits); PIN(tg.lo); PIN(tg.hi);
        Frag f0, f1;
        BAR();
        frag_read(f0, xa, wa);
#pragma unroll 1
        for (int s = 0; s < 7; ++s) {
            const unsigned slot = (s & 3) * SLOT, nslot = ((s + 1) & 3) * SLOT;
            SCHED_FENCE();
            frag_read(f1, slot + (xa ^ 64), slot + wa + 4096); frag_mma(f0, acc); interleave();
            SCHED_FENCE(); BAR();
            frag_read(f0, nslot + xa, nslot + wa); frag_mma(f1, acc); interleave();
        }
        SCHED_FENCE();
        frag_read(f1, 3 * SLOT + (xa ^ 64), 3 * SLOT + wa + 4096); frag_mma(f0, acc); interleave();
        SCHED_FENCE();
        frag_mma(f1, acc);
        STAMP(1);
        f32x16 kv = 0.f, zz = 0.f;
        const u32x4 ones = {0x3F803F80u, 0x3F803F80u, 0x3F803F80u, 0x3F803F80u};
#pragma unroll
        for (int i = 0; i < 2; ++i)
#pragma unroll
            for (int u = 0; u < 2; ++u) {
                const u32x4 A = cat(phi4(q4(acc[i][0], 2 * u)), phi4(q4(acc[i][0], 2 * u + 1))), B = cat(pkv(q4(acc[i][1], 2 * u)), pkv(q4(acc[i][1], 2 * u + 1)));
                kv = mma32(A, B, kv); zz = mma32(A, ones, zz);
            }
        f32x4 k0 = (g & 1) ? q4(kv, 2) : q4(kv, 0), k1 = (g & 1) ? q4(kv, 3) : q4(kv, 1);
        f32x4 z0 = (g & 1) ? q4(zz, 2) : q4(zz, 0), z1 = (g & 1) ? q4(zz, 3) : q4(zz, 1);
        if (rh) { lwf128(red, k0); lwf128(red + 1024, k1); lwf128(red + 2048, z0); lwf128(red + 3072, z1); }
        BAR();
        if (!rh) {
            k0 += lrf128(red); k1 += lrf128(red + 1024); z0 += lrf128(red + 2048); z1 += lrf128(red + 3072);
            st128(rws, pst, catt(k0, k1, tg)); st128(rws, pz, catt(z0, z1, tg));
        }
        SCHED_FENCE();
#pragma unroll
        for (int i = 0; i < 2; ++i)
#pragma unroll
            for (int b = 0; b < 2; ++b) { pq[i][b] = cat(phi4(q4(acc[i][2], 2 * b)), phi4(q4(acc[i][2], 2 * b + 1))); PIN(pq[i][b]); }
        VMWAIT(0);
    }
    STAMP(2);
    unsigned ka = A_SUM + (r32 < 16 ? hp * 1024 + (h * 32 + c16) * 16 : 2048 + hp * 256 + h * 128), kh2 = r32 < 16 ? 256 : 64;
    unsigned ob = WS_O + ((rp * 8 + rh * 4 + (g & 1)) * 16 + hg * 2 + hp) * 1024 + (h * 16 + c16) * 16;
    unsigned pb = pg + ((tid + 320) & 511) * 8, sl = 0x00003F80u, sh = 0x3F800000u;
    asm volatile("" : "+s"(sl), "+s"(sh));
    const bf16x2 sel0 = __builtin_bit_cast(bf16x2, sl), sel1 = __builtin_bit_cast(bf16x2, sh);
    Tag to = make_tag(ep, lane & 3);
    const u32x4 t8 = tag_bits(ep, (lane >> 1) & 3);
    unsigned c0 = (lane & 1) ? t8[2] : t8[0], c1 = (lane & 1) ? t8[3] : t8[1];
    PIN(ka); PIN(kh2); PIN(ob); PIN(pb); PIN(to.bits); PIN(to.lo); PIN(to.hi); PIN(c0); PIN(c1);
    BAR();
    STAMP(3);
    if (w >= 3) {
        u32x2 d[16];
#pragma unroll 1
        for (int it = 0; it < SPIN_CAP; ++it) {
            FENCE();
#pragma unroll
            for (int j = 0; j < 16; ++j) d[j] = ld64s(rws, pb, j * 8 * PART_STRIDE);
            unsigned m = 0;
#pragma unroll
            for (int j = 0; j < 16; ++j) m = mis1(mis1(m, d[j][0], c0), d[j][1], c1);
            if (!stale(m)) break;
            __builtin_amdgcn_s_sleep(2);
        }
        f32x4 sm = Z4;
#pragma unroll
        for (int j = 0; j < 16; ++j)
#pragma unroll
            for (int k = 0; k < 4; ++k) { const unsigned dv = d[j][k >> 1]; sm[k] = __builtin_amdgcn_fdot2_f32_bf16(__builtin_bit_cast(bf16x2, dv), (k & 1) ? sel1 : sel0, sm[k], false); }
        lw64(A_SUM + ((tid + 320) & 511) * 8, pkv(sm));
    }
    BAR();
    STAMP(4);
    if (w < 4) {
#pragma unroll
        for (int b = 0; b < 2; ++b) {
            const u32x4 A = lr128(ka + b * kh2);
#pragma unroll
            for (int i = 0; i < 2; ++i) {
                const f32x16 nd = mma32(A, pq[i][b], 0.f);
                const float ri = __builtin_amdgcn_rcpf(nd[8] + 1e-6f);
                st128(rws, ob + i * 32768 + b * 512, catt(q4(nd, 0) * ri, q4(nd, 1) * ri, to));
            }
        }
        STAMP(5);
    }
    VMWAIT(0);
    return ep;
}

constexpr unsigned L_U = 98304;
constexpr unsigned L_S = 65536;
constexpr unsigned L_W = L_U + 19200;
constexpr unsigned L_SP = L_W + 20480;
constexpr unsigned L_C = L_SP + 512;
constexpr unsigned L_PW = L_C + 10240;
constexpr unsigned L_PX = L_PW + 4096;
#define TOUCH 1
DI unsigned role_b(const Params& p, rsrc_t rws, int xcd, int li, int tid, int lane, int w, int rt, int ct, const char* code STP_PARAM) {
    const int g = lane >> 4, c16 = lane & 15;
    const int me = xcd * 16 + li;
    const int j1 = (li >> 3) ? (xcd ? 16 - xcd : 8) : xcd, kb = li & 7;
    const int r8 = lane >> 3, ch = lane & 7, jm = (16 - c16) & 15;
    u32x4 x0, x1, wl0, wl1;
    unsigned sink, spv;
    unsigned tb1 = WS_T1 + me * TB + w * 2048 + lane * 16;
    {
        if (w < 2) {
            const int v = min(tid >> 4, 5), kr = c16 + 16 * me;
            const unsigned short* sp = ((v & 1) ? p.wi : p.wr) + (v < 2 ? ((2048 - kr) & 2047) * 257 : kr * 257 + (v < 4 ? 256 : 128));
            asm volatile("global_load_ushort %0, %1, off" : "=v"(spv) : "v"(sp) : "memory");
        }
        const rsrc_t rb = mkrsrc(p.bx, 2048 * 512 * 2);
        const unsigned vo = (256 * w + me) * 1024 + g * 256 + c16 * 4;
        asm volatile("buffer_load_dword %0, %8, %9, 0 offen\n\tbuffer_load_dword %1, %8, %9, 0 offen offset:64\n\t"
                     "buffer_load_dword %2, %8, %9, 0 offen offset:128\n\tbuffer_load_dword %3, %8, %9, 0 offen offset:192\n\t"
                     "buffer_load_dword %4, %8, %9, %10 offen\n\tbuffer_load_dword %5, %8, %9, %10 offen offset:64\n\t"
                     "buffer_load_dword %6, %8, %9, %10 offen offset:128\n\tbuffer_load_dword %7, %8, %9, %10 offen offset:192"
                     : "=&v"(x0[0]), "=&v"(x0[1]), "=&v"(x0[2]), "=&v"(x0[3]), "=&v"(x1[0]), "=&v"(x1[1]), "=&v"(x1[2]), "=&v"(x1[3])
                     : "v"(vo), "s"(rb), "s"(131072u) : "memory");
        const int n = tid < 480 ? (tid / 80) * 1280 + li * 80 + tid % 80 : 0, st = 2 + n / 1280, idx = n % 1280, kr = (idx - 512) / 12, cc = (idx - 512) % 12;
        const char* pf = idx < 512 ? (const char*)p.ax + (((xcd >> 1) * 512 + idx) * 1024 + st * 128)
                                   : (const char*)p.wqkv + ((st * 64 + kr) * 3072 + (cc >> 2) * 1024 + ((xcd & 1) * 4 + (cc & 3)) * 128);
        if (!TOUCH || tid >= 480) pf = code + ((16 + li) * CODE_LINES + (lane & (CODE_LINES - 1))) * 128;
        touch(sink, pf);
        const unsigned short* wp = (w >= 4) ? p.wi : p.wr;
        const int ks = j1 + 16 * ((tid >> 1) & 127), hf = (tid & 1) * 8;
        asm volatile("global_load_dwordx4 %0, %2, off\n\tglobal_load_dwordx4 %1, %3, off" : "=&v"(wl0), "=&v"(wl1)
                     : "v"(wp + ks * 257 + 16 * kb + hf), "v"(wp + ((2048 - ks) & 2047) * 257 + 241 - 16 * kb + hf) : "memory");
    }
    const bool special = (kb == 0 && w == 0);
    if (w < 5) {
        const int k = w, k4 = 4 * k, sU = (0x04004 >> k4) & 15, sQ = (0x40040 >> k4) & 15;
        const int P = (((c16 & ((0x7F7FF >> k4) & 15)) << sU) + (sU ? j1 : 0)) << ((0x00833 >> k4) & 15);
        const int n0 = P * (((4 * (g & ((0x31133 >> k4) & 15))) << sQ) + (sQ ? j1 : 0)), dn = P << sQ;
        const Cx t = angles(n0, dn, 2047, 1.f / 2048);
        Mat m = tomat(t);
        if (k > 2) m = Mat{__builtin_bit_cast(u32x4, t.re), __builtin_bit_cast(u32x4, t.im)};
        if (k == 2 && (g >> 1) != (c16 >> 3)) m = Mat{u32x4{0, 0, 0, 0}, u32x4{0, 0, 0, 0}};
        lw128(L_C + k * 2048 + lane * 16, m.re); lw128(L_C + k * 2048 + 1024 + lane * 16, m.im);
    }
    Mat F = tomat(angles(4 * g * c16, c16, 15, 1.f / 16));
    Cx tw1 = angles(4 * g * c16, c16, 255, 1.f / 256);
    const int km0 = 16 * w + c16, km1 = km0 ? 256 - km0 : 128;
    unsigned ua = L_U + (2 * w) * 1072 + g * 272 + c16 * 16;
    unsigned a0 = L_U + 4 * g * 1072 + ((km0 >> 2) & 3) * 272 + (km0 >> 4) * 16 + (km0 & 3) * 4;
    unsigned a1s = L_U + 4 * g * 1072 + ((km1 >> 2) & 3) * 272 + (km1 >> 4) * 16 + (km1 & 3) * 4;
    unsigned sw0 = L_S + w * 2304 + c16 * 144 + g * 16, sw1 = L_S + w * 2304 + jm * 144 + 64 + g * 16;
    unsigned sr0 = L_S + w * 2304 + r8 * 144 + ch * 16;
    unsigned wst = L_W + (w >> 2) * 5120 + ((tid >> 1) & 127) * 40 + (tid & 1) * 16;
    const int lam = tid >> 3, s2a = (lam >> 5) * 32 + (lam & 3) * 8 + ((lam >> 2) & 7);
    const int t16 = (w >> 2) * 32 + (lane & 8) * 8 + (lane & 3) * 8 + (w & 3) * 2 + ((lane >> 2) & 1);
    unsigned sb = L_U + ((ch & 3) * 2 + (ch >> 2)) * 1040 + ((lam >> 5) * 16 + ((lam >> 2) & 7)) * 16 + (lam & 3) * 4;
    unsigned tb2 = WS_T1 + s2a * TB + (kb * 16 + j1) * 128 + ch * 16;
    PIN(F.re); PIN(F.im); PIN(tw1.re); PIN(tw1.im); PIN(ua); PIN(a0); PIN(a1s); PIN(sw0); PIN(sw1); PIN(sr0); PIN(tb1); PIN(wst); PIN(sb); PIN(tb2);
    const unsigned ep = read_epoch(p.ws);
    Tag tg = make_tag(ep, g);
    u32x4 tl = tag_bits(ep, lane & 3);
    PIN(tg.bits); PIN(tg.lo); PIN(tg.hi); PIN(tl);
    asm volatile("s_waitcnt vmcnt(3)" : "+v"(x0), "+v"(x1), "+v"(spv) :: "memory");
    STAMP(1);
    if (w < 2) { const int v = min(tid >> 4, 5); *LP(unsigned short, L_PX + c16 * 32 + 4 + 2 * v + (v & 4)) = (unsigned short)spv; }
    lw128(ua, pk4(dftA(twpk(dftA(x0, F), tw1), F))); lw128(ua + 1072, pk4(dftA(twpk(dftA(x1, F), tw1), F)));
    BAR();
    {
        const u32x4 d0 = {lr32(a0), lr32(a0 + 1072), lr32(a0 + 2144), lr32(a0 + 3216)};
        const u32x4 d1 = {lr32(a1s), lr32(a1s + 1072), lr32(a1s + 2144), lr32(a1s + 3216)};
        u32x4 pkt = lr128(L_PX + jm * 32 + (g & 1) * 16);
        const Cx c0 = dftA(d0, F), c1 = dftA(d1, F);
        lw128(sw0, zipt(c0.re, c0.im, tg)); lw128(sw1, zipt(c1.re, -c1.im, tg));
        if (g == 0) pkt[0] = pk(c0.re[0], c0.im[0]);
        pkt[3] = ep;
        const u32x4 s0 = lr128(sr0), s1 = lr128(sr0 + 8 * 144);
        st128(rws, tb1, s0); st128(rws, tb1 + 1024, s1);
        if (tid < 32) st128(rws, WS_T1 + me * TB + 16384 + jm * 32 + g * 16, pkt);
    }
    STAMP(2);
    asm volatile("s_waitcnt vmcnt(0)" : "+v"(wl0), "+v"(wl1), "+v"(sink) :: "memory");
    lw64(wst, u32x2{wl0[0], wl0[1]}); lw64(wst + 8, u32x2{wl0[2], wl0[3]});
    lw64(wst + 10240, u32x2{wl1[0], wl1[1]}); lw64(wst + 10248, u32x2{wl1[2], wl1[3]});
    BAR();
    STAMP(3);
    u32x4 i0 = ld128(rws, tb2), i1 = ld128s(rws, tb2, 64 * TB), isp = {0, 0, 0, ep};
    const unsigned tb4 = WS_T1 + t16 * TB + 16384 + j1 * 32 + (g & 1) * 16;
    if (kb == 0) isp = ld128(rws, tb4);
    FENCE();
    Cx a1, bz, a2, bv;
    {
        const int col = 2 * w + (g >> 1);
        const float th = (float)(16 * kb + col) * (1.f / 512), sn = __builtin_amdgcn_sinf(th), cs = __builtin_amdgcn_cosf(th);
        unsigned wp = L_W + (c16 + 64 * (g & 1)) * 40 + col * 2, wq = wp + 10240 + 30 - col * 4;
        PIN(wp); PIN(wq);
        f32x4 pr, pi, qr, qi;
#pragma unroll
        for (int r = 0; r < 4; ++r) { pr[r] = lrbf(wp + r * 640); pi[r] = lrbf(wp + r * 640 + 5120); qr[r] = lrbf(wq + r * 640); qi[r] = lrbf(wq + r * 640 + 5120); }
        const float sc = 1.f / 1048576;
        const f32x4 sr = (pr + qr) * sc, si = (pi - qi) * sc, dr = (pr - qr) * sc, di = (pi + qi) * sc;
        a1.re = sr - dr * sn; a1.im = si - di * sn; a2.re = sr + dr * sn; a2.im = si + di * sn;
        bz.re = dr * cs; bz.im = di * cs;
    }
    unsigned ca = L_C + lane * 16;
    PIN(ca);
    Mat F1 = {lr128(ca), lr128(ca + 1024)}, F2 = {lr128(ca + 2048), lr128(ca + 3072)}, G = {lr128(ca + 4096), lr128(ca + 5120)};
    Cx twa = {lrf128(ca + 6144), lrf128(ca + 7168)}, twb = {lrf128(ca + 8192), lrf128(ca + 9216)};
    unsigned ta = L_U + ((w >> 1) * 2 + 8 * (w & 1)) * 1040 + lane * 16;
    unsigned tb3 = WS_T2 + (kb * 16 + j1) * TB + s2a * 128 + ch * 16;
    const rsrc_t ro = mkrsrc(p.wo, 512 * 512 * 2), rout = mkrsrc(p.out, 2048 * 1024 * 2);
    unsigned vo = (w * 64 + r8) * 1024 + ct * 128 + ((ch ^ (r8 & 6)) << 4);
    Tag t2;
    { const u32x4 tw = tag_bits(ep, w >> 1); const unsigned m = (w & 1) ? ((lane & 8) ? tw[3] : tw[2]) : ((lane & 8) ? tw[1] : tw[0]);
      unsigned lo = (~m & 1u) << 16, hi = ~m & 0x10000u, mm = m; PIN(mm); PIN(lo); PIN(hi);
      t2.bits = mm; t2.lo = lo; t2.hi = hi; }
    PIN(a1.re); PIN(a1.im); PIN(a2.re); PIN(a2.im); PIN(bz.re); PIN(bz.im);
    PIN(F1.re); PIN(F1.im); PIN(F2.re); PIN(F2.im); PIN(G.re); PIN(G.im); PIN(twa.re); PIN(twa.im); PIN(twb.re); PIN(twb.im);
    PIN(ta); PIN(tb3); PIN(vo);
#pragma unroll 1
    for (int it = 0; it < SPIN_CAP && (stale(mis4(mis4(0, i0, tl), i1, tl)) || __any(isp[3] != ep)); ++it) {
        __builtin_amdgcn_s_sleep(2);
        FENCE();
        i0 = ld128(rws, tb2); i1 = ld128s(rws, tb2, 64 * TB);
        if (kb == 0) isp = ld128(rws, tb4);
    }
    STAMP(10);
    lw32(sb, i0[0]); lw32(sb + 128, i0[1]); lw32(sb + 8320, i0[2]); lw32(sb + 8320 + 128, i0[3]);
    lw32(sb + 512, i1[0]); lw32(sb + 512 + 128, i1[1]); lw32(sb + 512 + 8320, i1[2]); lw32(sb + 512 + 8320 + 128, i1[3]);
    if (kb == 0) { if (g == 0) lw32(L_SP + ((t16 >> 5) * 8 + (t16 & 7)) * 16 + ((t16 >> 3) & 3) * 4, isp[0]); lw128(L_PW + t16 * 32 + (g & 1) * 16, isp); }
    BAR();
    STAMP(4);
    {
        const u32x4 zi = lr128(ta), vi = lr128(ta + 1040);
#pragma unroll 1
        for (int q = 0; q < 8; ++q) dma(ro, vo, q * 8192, w * 8192 + q * 1024);
        const Cx Z = dftB(twpk(dftA(zi, F1), twa), G), V = dftB(twpk(dftA(vi, F1), twa), G);
        Cx Vz = V;
        bv = bz;
        if (special) {
            u32x4 v0 = lr128(L_SP + (g * 8 + (c16 & 7)) * 16) ^ 0x80000000u;
            if (c16 >= 8) v0 = vi;
            Vz = dftB(twpk(dftA(v0, F1), twa), G);
            if (g < 2) {
                const float hs = 0.5f / 1048576;
#pragma unroll
                for (int r = 0; r < 4; ++r) {
                    const u32x4 e = lr128(L_PW + (c16 + 64 * g + 16 * r) * 32);
                    const unsigned er = lr32(L_PW + (c16 + 64 * g + 16 * r) * 32 + 16);
                    const float ar = bf(e[1]), ai = bf(e[1] >> 16), br = bf(e[2]), bi = bf(e[2] >> 16);
                    a1.re[r] = 0.5f * a1.re[r] + hs * (ar + br); a1.im[r] = 0.5f * a1.im[r] + hs * (bi - ai);
                    bz.re[r] = 0.5f * bz.re[r] + hs * (ar - br); bz.im[r] = 0.5f * bz.im[r] - hs * (ai + bi);
                    a2.re[r] = 4.f * hs * bf(er); a2.im[r] = 4.f * hs * bf(er >> 16);
                    bv.re[r] = 0.f; bv.im[r] = 0.f;
                }
            }
        }
        const u32x4 zc = zip(a1.re * Z.re - a1.im * Z.im - bz.re * Vz.im - bz.im * Vz.re, bz.im * Vz.im - a1.re * Z.im - a1.im * Z.re - bz.re * Vz.re);
        const u32x4 vc = zip(a2.re * V.re - a2.im * V.im + bv.re * Z.im + bv.im * Z.re, bv.re * Z.re - a2.re * V.im - a2.im * V.re - bv.im * Z.im);
        const Cx zo = dftB(twpk(dftA(zc, G), twb), F2), vo2 = dftB(twpk(dftA(vc, G), twb), F2);
        lw128(ta, zipt(zo.re, zo.im, t2)); lw128(ta + 1040, zipt(vo2.re, -vo2.im, t2));
        BAR();
        const u32x4 o0 = {lr32(sb), lr32(sb + 128), lr32(sb + 8320), lr32(sb + 8320 + 128)};
        const u32x4 o1 = {lr32(sb + 512), lr32(sb + 512 + 128), lr32(sb + 512 + 8320), lr32(sb + 512 + 8320 + 128)};
        st128(rws, tb3, o0); st128(rws, tb3 + 64 * 128, o1);
    }
    STAMP(5);
    unsigned ra[4];
#pragma unroll
    for (int i = 0; i < 4; ++i) {
        const int km = 64 * g + 16 * i + c16, kq = km < 128 ? km : (256 - km) & 127;
        ra[i] = L_U + ((km >> 7) * 16 + 2 * w) * 592 + (kq + 16 * (kq >> 6)) * 4;
        PIN(ra[i]);
    }
    const int set = g & 1, k0 = 16 * (2 * w + (g >> 1));
    unsigned oo = (256 * w + me) * 2048 + 1024 + (64 * g + c16) * 4, sa = WS_O + (rt * 64 + lane) * 1024 + 4;
    unsigned u2 = L_U + (set * 16 + (set ? jm : c16)) * 592 + (k0 + 16 * (k0 >> 6)) * 4;
    unsigned tb = WS_T2 + ((2 * w + (c16 >> 3)) * 16 + 4 * g) * TB + me * 128 + ch * 16;
    PIN(oo); PIN(sa); PIN(u2); PIN(tb);
    VMWAIT(0);
    BAR();
    STAMP(6);
    if (w < 4) {
        u32x4 l0, l1, l2, l3;
#pragma unroll 1
        for (int it = 0; it < SPIN_CAP; ++it) {
            FENCE();
            l0 = ld128(rws, tb); l1 = ld128s(rws, tb, TB); l2 = ld128s(rws, tb, 2 * TB); l3 = ld128s(rws, tb, 3 * TB);
            if (!stale(mis4(mis4(mis4(mis4(0, l0, tl), l1, tl), l2, tl), l3, tl))) break;
            __builtin_amdgcn_s_sleep(2);
        }
        STAMP(12);
        u32x4 t[4];
#pragma unroll
        for (int e = 0; e < 4; ++e) t[e] = pk4(dftA(u32x4{l0[e], l1[e], l2[e], l3[e]}, F));
#pragma unroll
        for (int r = 0; r < 4; ++r) lw128(u2 + r * 16, u32x4{t[0][r], t[1][r], t[2][r], t[3][r]});
    }
    BAR();
    STAMP(11);
    {
        const unsigned ts = tag_bits(ep, 0)[1];
        unsigned sen = ts;
        if (w == 0) sen = ld32(rws, sa);
        const u32x4 d0 = {lr32(ra[0]), lr32(ra[1]), lr32(ra[2]), lr32(ra[3])};
        const u32x4 d1 = {lr32(ra[0] + 592), lr32(ra[1] + 592), lr32(ra[2] + 592), lr32(ra[3] + 592)};
        const u32x4 y0 = pk4c(dftB(twpk(dftA(d0, F), tw1), F)), y1 = pk4c(dftB(twpk(dftA(d1, F), tw1), F));
#pragma unroll
        for (int r = 0; r < 4; ++r) {
            __builtin_amdgcn_raw_buffer_store_b32(y0[r], rout, oo + 64 * r, 0, OUT_AUX);
            __builtin_amdgcn_raw_buffer_store_b32(y1[r], rout, oo + 64 * r, 128 * 2048, OUT_AUX);
        }
        STAMP(13);
#pragma unroll 1
        for (int it = 0; it < SPIN_CAP && stale(sen ^ ts); ++it) { __builtin_amdgcn_s_sleep(8); FENCE(); sen = ld32(rws, sa); }
    }
    return ep;
}

__global__ void __launch_bounds__(512, 2) fused_kernel(Params p) {
    const char* code;
    asm volatile("s_getpc_b64 %0" : "=s"(code));
    code = (const char*)((size_t)code & ~(size_t)255);
    const int b = blockIdx.x, xcd = b & 7, i = b >> 3, li = i & 15;
    const int tid = threadIdx.x, lane = tid & 63, w = __builtin_amdgcn_readfirstlane(tid >> 6);
    const int rt = xcd * 4 + (i & 3), ct = i >> 2;
    __shared__ __attribute__((aligned(16))) char smem[LDS_BYTES];
    asm volatile("" :: "s"((unsigned)(size_t)(LAS char*)smem));
    const rsrc_t rws = mkrsrc(p.ws, WS_END);
    STAMP(0);
    unsigned ep;
    if (i < 16) ep = role_a(p, rws, xcd, li, tid, lane, w, rt, ct, code STP_ARG);
    else        ep = role_b(p, rws, xcd, li, tid, lane, w, rt, ct, code STP_ARG);
    const int g = lane >> 4, c16 = lane & 15, q = c16 >> 2, kh = w >> 2;
    unsigned ob = WS_O + (rt * 4 + (w & 3)) * 16384 + kh * 8192 + lane * 16;
    unsigned wa = kh * 32768 + (16 * (g >> 1) + 4 * (g & 1) + q) * 128 + 8 * (lane & 3) + (((2 * g + (q >> 1)) & 3) << 5);
    const unsigned scr = 81920;
    unsigned rw = scr + kh * 17408 + ((w & 3) * 16 + c16) * 272 + g * 16, rr = scr + (tid >> 3) * 272 + (tid & 7) * 32;
    unsigned oo = (rt * 64 + (tid >> 3)) * 2048 + ct * 128 + (tid & 7) * 16;
    u32x4 tl = tag_bits(ep, lane & 3);
    PIN(ob); PIN(wa); PIN(rw); PIN(rr); PIN(oo); PIN(tl);
    BAR();
    STAMP(7);
    u32x4 o[8], wf[8][4];
#pragma unroll
    for (int k = 0; k < 8; ++k) o[k] = ld128s(rws, ob, k * 1024);
    FENCE();
#pragma unroll
    for (int k = 0; k < 8; ++k)
#pragma unroll
        for (int t = 0; t < 4; ++t) { const unsigned a = (wa ^ (t << 5)) + k * 4096; wf[k][t] = ltr2(a, a + 1024); }
    SCHED_FENCE();
    f32x4 acc[4];
#pragma unroll 1
    for (int it = 0; it < SPIN_CAP; ++it) {
        unsigned m = 0;
#pragma unroll
        for (int t = 0; t < 4; ++t) acc[t] = Z4;
#pragma unroll
        for (int k = 0; k < 8; ++k) {
#pragma unroll
            for (int t = 0; t < 4; ++t) acc[t] = mma(wf[k][t], o[k], acc[t]);
            m = mis4(m, o[k], tl);
        }
        if (!stale(m)) break;
        __builtin_amdgcn_s_sleep(4);
        FENCE();
#pragma unroll
        for (int k = 0; k < 8; ++k) o[k] = ld128s(rws, ob, k * 1024);
    }
    STAMP(8);
#pragma unroll
    for (int t = 0; t < 4; ++t) lwf128(rw + t * 64, acc[t]);
    BAR();
    const f32x4 y0 = lrf128(rr) + lrf128(rr + 17408), y1 = lrf128(rr + 16) + lrf128(rr + 17408 + 16);
    __builtin_amdgcn_raw_buffer_store_b128(cat(pkv(y0), pkv(y1)), mkrsrc(p.out, 2048 * 1024 * 2), oo, 0, OUT_AUX);
    if (b == 128 && tid == 0) st32(rws, WS_EPOCH, ep);
    STAMP(9);
}

extern "C" void kernel_launch(void* const* d_in, const int* in_sizes, int n_in,
                              void* d_out, int out_size, void* d_ws, size_t ws_size,
                              hipStream_t stream) {
    Params p;
    p.ax = (const unsigned short*)d_in[0]; p.wqkv = (const unsigned short*)d_in[1]; p.wo = (const unsigned short*)d_in[2];
    p.bx = (const unsigned short*)d_in[3]; p.wr = (const unsigned short*)d_in[4]; p.wi = (const unsigned short*)d_in[5];
    p.out = (unsigned short*)d_out; p.ws = (unsigned char*)d_ws;
    fused_kernel<<<256, 512, 0, stream>>>(p);
}
```

```cpp
#define CALPT 1
#define CALROLE 1
#include <hip/hip_runtime.h>
#include <stdint.h>

typedef short    s16x4 __attribute__((ext_vector_type(4)));
typedef __bf16   bf16x8 __attribute__((ext_vector_type(8)));
typedef __bf16   bf16x2 __attribute__((ext_vector_type(2)));
typedef float    f32x2 __attribute__((ext_vector_type(2)));
typedef float    f32x4 __attribute__((ext_vector_type(4)));
typedef float    f32x16 __attribute__((ext_vector_type(16)));
typedef unsigned u32x2 __attribute__((ext_vector_type(2)));
typedef unsigned u32x4 __attribute__((ext_vector_type(4)));
typedef __amdgpu_buffer_rsrc_t rsrc_t;
#define LAS __attribute__((address_space(3)))
#define DI __device__ __forceinline__

constexpr unsigned WS_EPOCH = 0;
constexpr unsigned PART_STRIDE = 2688, WS_PART = 4096;
constexpr unsigned TB = 17024;
constexpr unsigned WS_T1 = 1u << 19, WS_T2 = WS_T1 + 128 * TB;
constexpr unsigned WS_O = WS_T2 + 128 * TB, WS_END = WS_O + (1u << 21);
constexpr int SPIN_CAP = 1 << 15;
constexpr unsigned LDS_BYTES = 163840;
#define OUT_AUX 16
struct Stamps { unsigned long long t; };
#define STAMP(i) do { if ((i) == CALPT) asm volatile("s_memrealtime %0\n\ts_waitcnt lgkmcnt(0)" : "=s"(stp.t) :: "memory"); } while (0)
#define STP_PARAM , Stamps& stp
#define STP_ARG , stp

struct Params {
    const unsigned short *ax, *wqkv, *wo, *bx, *wr, *wi;
    unsigned short* out;
    unsigned char* ws;
};

DI rsrc_t mkrsrc(const void* ptr, unsigned bytes) { return __builtin_amdgcn_make_buffer_rsrc((void*)ptr, 0, bytes, 0x00020000); }
DI u32x4 ld128(rsrc_t r, unsigned off)  { return __builtin_amdgcn_raw_buffer_load_b128(r, off, 0, 16); }
DI u32x2 ld64(rsrc_t r, unsigned off)   { return __builtin_amdgcn_raw_buffer_load_b64(r, off, 0, 16); }
DI unsigned ld32(rsrc_t r, unsigned off){ return __builtin_amdgcn_raw_buffer_load_b32(r, off, 0, 16); }
DI u32x2 ld64s(rsrc_t r, unsigned off, unsigned so)   { return __builtin_amdgcn_raw_buffer_load_b64(r, off, so, 16); }
DI u32x4 ld128s(rsrc_t r, unsigned off, unsigned so)  { return __builtin_amdgcn_raw_buffer_load_b128(r, off, so, 16); }
DI unsigned ld32s(rsrc_t r, unsigned off, unsigned so){ return __builtin_amdgcn_raw_buffer_load_b32(r, off, so, 16); }
DI void st128(rsrc_t r, unsigned off, u32x4 v) { __builtin_amdgcn_raw_buffer_store_b128(v, r, off, 0, 16); }
DI void st64(rsrc_t r, unsigned off, u32x2 v) { __builtin_amdgcn_raw_buffer_store_b64(v, r, off, 0, 16); }
DI void st32(rsrc_t r, unsigned off, unsigned v) { __builtin_amdgcn_raw_buffer_store_b32(v, r, off, 0, 16); }
#define FENCE() asm volatile("" ::: "memory")
#define BAR() asm volatile("s_waitcnt lgkmcnt(0)\n\ts_barrier" ::: "memory")
#define VMWAIT(n) asm volatile("s_waitcnt vmcnt(" #n ")" ::: "memory")
#define PIN(x) asm volatile("" : "+v"(x))
#define SCHED_FENCE() __builtin_amdgcn_sched_barrier(0)
DI void dma(rsrc_t r, unsigned voff, unsigned soff, unsigned lds) {
    asm volatile("s_mov_b32 m0, %0\n\ts_nop 0\n\tbuffer_load_dwordx4 %1, %2, %3 offen lds"
                 :: "s"(lds), "v"(voff), "s"(r), "s"(soff) : "memory");
}
DI unsigned pk(float lo, float hi) { f32x2 v = {lo, hi}; return __builtin_bit_cast(unsigned, __builtin_convertvector(v, bf16x2)); }
DI u32x2 pkv(f32x4 v) { return u32x2{pk(v[0], v[1]), pk(v[2], v[3])}; }
DI u32x4 cat(u32x2 a, u32x2 b) { return u32x4{a[0], a[1], b[0], b[1]}; }
DI f32x4 mma(u32x4 a, u32x4 b, f32x4 c) {
    return __builtin_amdgcn_mfma_f32_16x16x32_bf16(__builtin_bit_cast(bf16x8, a), __builtin_bit_cast(bf16x8, b), c, 0, 0, 0); }
DI f32x16 mma32(u32x4 a, u32x4 b, f32x16 c) {
    return __builtin_amdgcn_mfma_f32_32x32x16_bf16(__builtin_bit_cast(bf16x8, a), __builtin_bit_cast(bf16x8, b), c, 0, 0, 0); }
DI f32x4 mma16(u32x2 a, u32x2 b, f32x4 c) {
    return __builtin_amdgcn_mfma_f32_16x16x16bf16_1k(__builtin_bit_cast(s16x4, a), __builtin_bit_cast(s16x4, b), c, 0, 0, 0); }
#define LP(T, a) ((LAS T*)(size_t)(a))
DI u32x4 lr128(unsigned a) { return *LP(const u32x4, a); }
DI f32x4 lrf128(unsigned a) { return *LP(const f32x4, a); }
DI u32x2 lr64(unsigned a) { return *LP(const u32x2, a); }
DI unsigned lr32(unsigned a) { return *LP(const unsigned, a); }
DI float bf(unsigned short v) { return __builtin_bit_cast(float, (unsigned)v << 16); }
DI float lrbf(unsigned a) { return bf(*LP(const unsigned short, a)); }
DI void lw128(unsigned a, u32x4 v) { *LP(u32x4, a) = v; }
DI void lwf128(unsigned a, f32x4 v) { *LP(f32x4, a) = v; }
DI void lw64(unsigned a, u32x2 v) { *LP(u32x2, a) = v; }
DI void lw32(unsigned a, unsigned v) { *LP(unsigned, a) = v; }
DI u32x2 ltr(unsigned a) { return __builtin_bit_cast(u32x2, __builtin_amdgcn_ds_read_tr16_b64_v4i16(LP(s16x4, a))); }
DI u32x4 ltr2(unsigned a, unsigned b) { return cat(ltr(a), ltr(b)); }
constexpr unsigned TM = 0x00010001u;
DI u32x4 tag_bits(unsigned ep, int unit) {
    const unsigned t = ((ep << 2) | 2u) >> (8 * unit);
    u32x4 m;
#pragma unroll
    for (int k = 0; k < 4; ++k) m[k] = ((t >> (2 * k)) & 1u) | (((t >> (2 * k + 1)) & 1u) << 16);
    return m;
}
DI unsigned pkt1(float a, float b, unsigned bits) {
    const unsigned v = __builtin_amdgcn_perm(__builtin_bit_cast(unsigned, b) + 0x10000u, __builtin_bit_cast(unsigned, a) + 0x10000u, 0x07060302u);
    return __builtin_amdgcn_bitop3_b32(v, TM, bits, 0xBA);
}
DI u32x4 zipt(f32x4 a, f32x4 b, u32x4 t) { return u32x4{pkt1(a[0], b[0], t[0]), pkt1(a[1], b[1], t[1]), pkt1(a[2], b[2], t[2]), pkt1(a[3], b[3], t[3])}; }
DI u32x4 catt(f32x4 a, f32x4 b, u32x4 t) { return u32x4{pkt1(a[0], a[1], t[0]), pkt1(a[2], a[3], t[1]), pkt1(b[0], b[1], t[2]), pkt1(b[2], b[3], t[3])}; }
DI unsigned or4(unsigned m, u32x4 v) { return m | v[0] | v[1] | v[2] | v[3]; }
DI bool stale(unsigned m) { return __any(m & TM); }
DI u32x2 phi4(f32x4 t) {
    f32x4 e, m;
#pragma unroll
    for (int r = 0; r < 4; ++r) {
        e[r] = __builtin_amdgcn_fmed3f(__builtin_amdgcn_exp2f(t[r] * 1.44269504f), 0.f, 1.f);
        m[r] = __builtin_elementwise_maximum(t[r], 0.f);
    }
    return pkv((m + e) + 1e-6f);
}
constexpr f32x4 Z4 = {0.f, 0.f, 0.f, 0.f};

struct Mat { u32x4 re, im; };
struct Cx  { f32x4 re, im; };
DI Cx angles(int n0, int dn, int mask, float inv) {
    Cx t;
#pragma unroll
    for (int r = 0; r < 4; ++r) { float a = (float)((n0 + r * dn) & mask) * inv;
        t.re[r] = __builtin_amdgcn_cosf(a); t.im[r] = __builtin_amdgcn_sinf(a); }
    return t;
}
DI u32x4 zip(f32x4 a, f32x4 b) { return u32x4{pk(a[0], b[0]), pk(a[1], b[1]), pk(a[2], b[2]), pk(a[3], b[3])}; }
DI Mat tomat(Cx t) { return Mat{zip(t.re, t.im), zip(-t.im, t.re)}; }
DI Cx dftA(u32x4 d, const Mat& f) { return Cx{mma(d, f.re, Z4), mma(d, f.im, Z4)}; }
DI Cx dftB(u32x4 d, const Mat& f) { return Cx{mma(f.re, d, Z4), mma(f.im, d, Z4)}; }
DI u32x4 twpk(Cx v, const Cx& w) { return zip(v.re * w.re + v.im * w.im, v.im * w.re - v.re * w.im); }
DI u32x4 pk4(Cx v)  { return zip(v.re, v.im); }
DI u32x4 pk4c(Cx v) { return zip(v.re, v.im) ^ 0x80000000u; }

DI unsigned read_epoch(const unsigned char* ws) {
    unsigned e;
    asm volatile("s_load_dword %0, %1, 0x0\n\ts_waitcnt lgkmcnt(0)" : "=s"(e) : "s"(ws) : "memory");
    return e + 1;
}
DI f32x4 q4(const f32x16& v, int i) { return f32x4{v[4 * i], v[4 * i + 1], v[4 * i + 2], v[4 * i + 3]}; }
struct Frag { u32x4 a[2][2], b[2][3]; };
DI void frag_read(Frag& f, unsigned xa, unsigned wa) {
#pragma unroll
    for (int k = 0; k < 2; ++k) {
        f.a[k][0] = lr128(xa ^ (k * 32)); f.b[k][0] = ltr2(wa + k * 2048, wa + k * 2048 + 512);
        f.a[k][1] = lr128((xa ^ (k * 32)) + 4096);
        f.b[k][1] = ltr2(wa + k * 2048 + 8192, wa + k * 2048 + 8704); f.b[k][2] = ltr2(wa + k * 2048 + 16384, wa + k * 2048 + 16896);
    }
}
DI void frag_mma(const Frag& f, f32x16 (&acc)[2][3]) {
#pragma unroll
    for (int k = 0; k < 2; ++k)
#pragma unroll
        for (int j = 0; j < 3; ++j)
#pragma unroll
            for (int i = 0; i < 2; ++i) acc[i][j] = (j < 2) ? mma32(f.a[k][i], f.b[k][j], acc[i][j]) : mma32(f.b[k][j], f.a[k][i], acc[i][j]);
}
DI void interleave() {
#pragma unroll
    for (int i = 0; i < 8; ++i) { __builtin_amdgcn_sched_group_barrier(0x8, 1, 0); __builtin_amdgcn_sched_group_barrier(0x100, 2, 0); }
    __builtin_amdgcn_sched_group_barrier(0x8, 4, 0);
}
#define DEPTH 2
#define ROTK 0
constexpr unsigned CODE_LINES = 4;
DI void touch(unsigned& sink, const void* ptr) { asm volatile("global_load_dword %0, %1, off" : "=v"(sink) : "v"(ptr) : "memory"); }

constexpr unsigned SLOT = 40960;
constexpr unsigned A_RED = 65536, A_SUM = 73728 + 1024;
DI unsigned role_a(const Params& p, rsrc_t rws, int xcd, int li, int tid, int lane, int w, int rt, int ct, const char* code STP_PARAM) {
    const int rp = (xcd >> 1) * 4 + (li & 3), hg = (xcd & 1) * 4 + (li >> 2);
    const int rot = ROTK == 0 ? 0 : ROTK == 1 ? 2 * (li >> 2) + 2 * (li & 3) : (li & 1) * 4;
    const int g = lane >> 4, c16 = lane & 15, h = lane >> 5, r32 = lane & 31, rh = (w >> 1) & 1, hp = w & 1;
    const unsigned xa = (rh * 64 + r32) * 128 + ((h ^ ((r32 >> 1) & 7)) << 4);
    const unsigned wa = 16384 + (8 * h + (c16 >> 2)) * 128 + ((hp ^ (c16 >> 3)) << 6) + (g & 1) * 32 + 8 * (lane & 3);
    const unsigned pg = WS_PART + hg * PART_STRIDE;
    unsigned ep;
    u32x4 pq[2][2];
    if (w >= 4) {
        const int lw = w - 4, r8 = lane >> 3, ch = lane & 7;
        const rsrc_t rx = mkrsrc(p.ax, 2048 * 512 * 2), rw = mkrsrc(p.wqkv, 512 * 1536 * 2), ro = mkrsrc(p.wo, 512 * 512 * 2);
        const unsigned vx0 = (rp * 128 + lw * 32 + r8) * 1024 + ((ch ^ (r8 >> 1)) << 4), vx1 = vx0 ^ 64;
        const unsigned vw = (lw * 16 + r8) * 3072 + hg * 128 + ((ch ^ ((r8 & 2) << 1)) << 4);
        auto stage = [&](int s) {
            const unsigned kb = (s + rot) & 7, so = kb * (64 * 3072), xo = kb * 128;
            const unsigned lx = (s & 3) * SLOT + lw * 4096, lk = (s & 3) * SLOT + 16384 + lw * 2048;
            dma(rx, vx0, xo, lx); dma(rx, vx1, xo + 8192, lx + 1024);
            dma(rw, vw, so + 1024, lk); dma(rw, vw, so + 25600, lk + 1024);
            dma(rx, vx0, xo + 16384, lx + 2048); dma(rx, vx1, xo + 24576, lx + 3072);
            dma(rw, vw, so + 2048, lk + 8192); dma(rw, vw, so + 26624, lk + 9216);
            dma(rw, vw, so, lk + 16384); dma(rw, vw, so + 24576, lk + 17408);
        };
        const unsigned vo = (lw * 64 + r8) * 1024 + ct * 128 + ((ch ^ (r8 & 6)) << 4);
        auto wo_part = [&](int u) {
#pragma unroll 1
            for (int q = 0; q < 8; ++q) dma(ro, vo, u * 262144 + q * 8192, u * 32768 + lw * 8192 + q * 1024);
        };
#pragma unroll 1
        for (int s = -2; s < 6; ++s) {
            if (s >= 0) { VMWAIT(10); BAR(); }
            stage(s + 2);
        }
        VMWAIT(10); BAR(); wo_part(0); VMWAIT(8); BAR(); wo_part(1);
        STAMP(1);
        ep = read_epoch(p.ws);
        BAR();
    } else {
        unsigned sink;
        if (w == 0 && lane < (int)CODE_LINES) touch(sink, code + (li * CODE_LINES + lane) * 128);
        f32x16 acc[2][3];
#pragma unroll
        for (int i = 0; i < 2; ++i)
#pragma unroll
            for (int j = 0; j < 3; ++j) { acc[i][j] = 0.f; PIN(acc[i][j]); }
        unsigned red = A_RED + hp * 4096 + lane * 16;
        unsigned pst = pg + rp * 8 * PART_STRIDE + hp * 1024 + lane * 16;
        unsigned pz = c16 < 4 ? pg + rp * 8 * PART_STRIDE + 2048 + hp * 256 + g * 64 + c16 * 16 : WS_END;
        PIN(red); PIN(pst); PIN(pz);
        ep = read_epoch(p.ws);
        asm volatile("s_waitcnt vmcnt(0)" : "+v"(sink) :: "memory");
        u32x4 tg = tag_bits(ep, lane & 3);
        PIN(tg);
        Frag f0, f1;
        BAR();
        frag_read(f0, xa, wa);
#pragma unroll 1
        for (int s = 0; s < 7; ++s) {
            const unsigned slot = (s & 3) * SLOT, nslot = ((s + 1) & 3) * SLOT;
            SCHED_FENCE();
            frag_read(f1, slot + (xa ^ 64), slot + wa + 4096); frag_mma(f0, acc); interleave();
            SCHED_FENCE(); BAR();
            frag_read(f0, nslot + xa, nslot + wa); frag_mma(f1, acc); interleave();
        }
        SCHED_FENCE();
        frag_read(f1, 3 * SLOT + (xa ^ 64), 3 * SLOT + wa + 4096); frag_mma(f0, acc); interleave();
        SCHED_FENCE();
        frag_mma(f1, acc);
        STAMP(1);
        f32x16 kv = 0.f, zz = 0.f;
        const u32x4 ones = {0x3F803F80u, 0x3F803F80u, 0x3F803F80u, 0x3F803F80u};
#pragma unroll
        for (int i = 0; i < 2; ++i)
#pragma unroll
            for (int u = 0; u < 2; ++u) {
                const u32x4 A = cat(phi4(q4(acc[i][0], 2 * u)), phi4(q4(acc[i][0], 2 * u + 1))), B = cat(pkv(q4(acc[i][1], 2 * u)), pkv(q4(acc[i][1], 2 * u + 1)));
                kv = mma32(A, B, kv); zz = mma32(A, ones, zz);
            }
        f32x4 k0 = (g & 1) ? q4(kv, 2) : q4(kv, 0), k1 = (g & 1) ? q4(kv, 3) : q4(kv, 1);
        f32x4 z0 = (g & 1) ? q4(zz, 2) : q4(zz, 0), z1 = (g & 1) ? q4(zz, 3) : q4(zz, 1);
        if (rh) { lwf128(red, k0); lwf128(red + 1024, k1); lwf128(red + 2048, z0); lwf128(red + 3072, z1); }
        BAR();
        if (!rh) {
            k0 += lrf128(red); k1 += lrf128(red + 1024); z0 += lrf128(red + 2048); z1 += lrf128(red + 3072);
            st128(rws, pst, catt(k0, k1, tg)); st128(rws, pz, catt(z0, z1, tg));
        }
        SCHED_FENCE();
#pragma unroll
        for (int i = 0; i < 2; ++i)
#pragma unroll
            for (int b = 0; b < 2; ++b) { pq[i][b] = cat(phi4(q4(acc[i][2], 2 * b)), phi4(q4(acc[i][2], 2 * b + 1))); PIN(pq[i][b]); }
        VMWAIT(0);
    }
    STAMP(2);
    unsigned ka = A_SUM + (r32 < 16 ? hp * 1024 + (h * 32 + c16) * 16 : 2048 + hp * 256 + h * 128), kh2 = r32 < 16 ? 256 : 64;
    unsigned ob = WS_O + ((rp * 8 + rh * 4 + (g & 1)) * 16 + hg * 2 + hp) * 1024 + (h * 16 + c16) * 16;
    unsigned pb = pg + ((tid + 320) & 511) * 8, sl = 0x00003F80u, sh = 0x3F800000u;
    asm volatile("" : "+s"(sl), "+s"(sh));
    const bf16x2 sel0 = __builtin_bit_cast(bf16x2, sl), sel1 = __builtin_bit_cast(bf16x2, sh);
    u32x4 to = tag_bits(ep, lane & 3);
    const u32x4 t8 = tag_bits(ep, (lane >> 1) & 3);
    unsigned c0 = (lane & 1) ? t8[2] : t8[0], c1 = (lane & 1) ? t8[3] : t8[1];
    PIN(ka); PIN(kh2); PIN(ob); PIN(pb); PIN(to); PIN(c0); PIN(c1);
    BAR();
    STAMP(3);
    if (w >= 3) {
        u32x2 d[16];
#pragma unroll 1
        for (int it = 0; it < SPIN_CAP; ++it) {
            FENCE();
#pragma unroll
            for (int j = 0; j < 16; ++j) d[j] = ld64s(rws, pb, j * 8 * PART_STRIDE);
            unsigned m = 0;
#pragma unroll
            for (int j = 0; j < 16; ++j) { d[j] ^= u32x2{c0, c1}; m |= d[j][0] | d[j][1]; }
            if (!stale(m)) break;
            __builtin_amdgcn_s_sleep(2);
        }
        f32x4 sm = Z4;
#pragma unroll
        for (int j = 0; j < 16; ++j)
#pragma unroll
            for (int k = 0; k < 4; ++k) { const unsigned dv = d[j][k >> 1]; sm[k] = __builtin_amdgcn_fdot2_f32_bf16(__builtin_bit_cast(bf16x2, dv), (k & 1) ? sel1 : sel0, sm[k], false); }
        lw64(A_SUM + ((tid + 320) & 511) * 8, pkv(sm));
    }
    BAR();
    STAMP(4);
    if (w < 4) {
#pragma unroll
        for (int b = 0; b < 2; ++b) {
            const u32x4 A = lr128(ka + b * kh2);
#pragma unroll
            for (int i = 0; i < 2; ++i) {
                const f32x16 nd = mma32(A, pq[i][b], 0.f);
                const float ri = __builtin_amdgcn_rcpf(nd[8] + 1e-6f);
                st128(rws, ob + i * 32768 + b * 512, catt(q4(nd, 0) * ri, q4(nd, 1) * ri, to));
            }
        }
        STAMP(5);
    }
    VMWAIT(0);
    return ep;
}

constexpr unsigned L_U = 98304;
constexpr unsigned L_S = 65536;
constexpr unsigned L_W = L_U + 19200;
constexpr unsigned L_SP = L_W + 20480;
constexpr unsigned L_C = L_SP + 512;
constexpr unsigned L_PW = L_C + 10240;
constexpr unsigned L_PX = L_PW + 4096;
#define TOUCH 1
DI unsigned role_b(const Params& p, rsrc_t rws, int xcd, int li, int tid, int lane, int w, int rt, int ct, const char* code STP_PARAM) {
    const int g = lane >> 4, c16 = lane & 15;
    const int me = xcd * 16 + li;
    const int j1 = (li >> 3) ? (xcd ? 16 - xcd : 8) : xcd, kb = li & 7;
    const int r8 = lane >> 3, ch = lane & 7, jm = (16 - c16) & 15;
    u32x4 x0, x1, wl0, wl1;
    unsigned sink, spv;
    unsigned tb1 = WS_T1 + me * TB + w * 2048 + lane * 16;
    {
        if (w < 2) {
            const int v = min(tid >> 4, 5), kr = c16 + 16 * me;
            const unsigned short* sp = ((v & 1) ? p.wi : p.wr) + (v < 2 ? ((2048 - kr) & 2047) * 257 : kr * 257 + (v < 4 ? 256 : 128));
            asm volatile("global_load_ushort %0, %1, off" : "=v"(spv) : "v"(sp) : "memory");
        }
        const rsrc_t rb = mkrsrc(p.bx, 2048 * 512 * 2);
        const unsigned vo = (256 * w + me) * 1024 + g * 256 + c16 * 4;
        asm volatile("buffer_load_dword %0, %8, %9, 0 offen\n\tbuffer_load_dword %1, %8, %9, 0 offen offset:64\n\t"
                     "buffer_load_dword %2, %8, %9, 0 offen offset:128\n\tbuffer_load_dword %3, %8, %9, 0 offen offset:192\n\t"
                     "buffer_load_dword %4, %8, %9, %10 offen\n\tbuffer_load_dword %5, %8, %9, %10 offen offset:64\n\t"
                     "buffer_load_dword %6, %8, %9, %10 offen offset:128\n\tbuffer_load_dword %7, %8, %9, %10 offen offset:192"
                     : "=&v"(x0[0]), "=&v"(x0[1]), "=&v"(x0[2]), "=&v"(x0[3]), "=&v"(x1[0]), "=&v"(x1[1]), "=&v"(x1[2]), "=&v"(x1[3])
                     : "v"(vo), "s"(rb), "s"(131072u) : "memory");
        const int n = tid < 480 ? (tid / 80) * 1280 + li * 80 + tid % 80 : 0, st = 2 + n / 1280, idx = n % 1280, kr = (idx - 512) / 12, cc = (idx - 512) % 12;
        const char* pf = idx < 512 ? (const char*)p.ax + (((xcd >> 1) * 512 + idx) * 1024 + st * 128)
                                   : (const char*)p.wqkv + ((st * 64 + kr) * 3072 + (cc >> 2) * 1024 + ((xcd & 1) * 4 + (cc & 3)) * 128);
        if (!TOUCH || tid >= 480) pf = code + ((16 + li) * CODE_LINES + (lane & (CODE_LINES - 1))) * 128;
        touch(sink, pf);
        const unsigned short* wp = (w >= 4) ? p.wi : p.wr;
        const int ks = j1 + 16 * ((tid >> 1) & 127), hf = (tid & 1) * 8;
        asm volatile("global_load_dwordx4 %0, %2, off\n\tglobal_load_dwordx4 %1, %3, off" : "=&v"(wl0), "=&v"(wl1)
                     : "v"(wp + ks * 257 + 16 * kb + hf), "v"(wp + ((2048 - ks) & 2047) * 257 + 241 - 16 * kb + hf) : "memory");
    }
    const bool special = (kb == 0 && w == 0);
    if (w < 5) {
        const int k = w, k4 = 4 * k, sU = (0x04004 >> k4) & 15, sQ = (0x40040 >> k4) & 15;
        const int P = (((c16 & ((0x7F7FF >> k4) & 15)) << sU) + (sU ? j1 : 0)) << ((0x00833 >> k4) & 15);
        const int n0 = P * (((4 * (g & ((0x31133 >> k4) & 15))) << sQ) + (sQ ? j1 : 0)), dn = P << sQ;
        const Cx t = angles(n0, dn, 2047, 1.f / 2048);
        Mat m = tomat(t);
        if (k > 2) m = Mat{__builtin_bit_cast(u32x4, t.re), __builtin_bit_cast(u32x4, t.im)};
        if (k == 2 && (g >> 1) != (c16 >> 3)) m = Mat{u32x4{0, 0, 0, 0}, u32x4{0, 0, 0, 0}};
        lw128(L_C + k * 2048 + lane * 16, m.re); lw128(L_C + k * 2048 + 1024 + lane * 16, m.im);
    }
    Mat F = tomat(angles(4 * g * c16, c16, 15, 1.f / 16));
    Cx tw1 = angles(4 * g * c16, c16, 255, 1.f / 256);
    const int km0 = 16 * w + c16, km1 = km0 ? 256 - km0 : 128;
    unsigned ua = L_U + (2 * w) * 1072 + g * 272 + c16 * 16;
    unsigned a0 = L_U + 4 * g * 1072 + ((km0 >> 2) & 3) * 272 + (km0 >> 4) * 16 + (km0 & 3) * 4;
    unsigned a1s = L_U + 4 * g * 1072 + ((km1 >> 2) & 3) * 272 + (km1 >> 4) * 16 + (km1 & 3) * 4;
    unsigned sw0 = L_S + w * 2304 + c16 * 144 + g * 16, sw1 = L_S + w * 2304 + jm * 144 + 64 + g * 16;
    unsigned sr0 = L_S + w * 2304 + r8 * 144 + ch * 16;
    unsigned wst = L_W + (w >> 2) * 5120 + ((tid >> 1) & 127) * 40 + (tid & 1) * 16;
    const int lam = tid >> 3, s2a = (lam >> 5) * 32 + (lam & 3) * 8 + ((lam >> 2) & 7);
    const int t16 = (w >> 2) * 32 + (lane & 8) * 8 + (lane & 3) * 8 + (w & 3) * 2 + ((lane >> 2) & 1);
    unsigned sb = L_U + ((ch & 3) * 2 + (ch >> 2)) * 1040 + ((lam >> 5) * 16 + ((lam >> 2) & 7)) * 16 + (lam & 3) * 4;
    unsigned tb2 = WS_T1 + s2a * TB + (kb * 16 + j1) * 128 + ch * 16;
    PIN(F.re); PIN(F.im); PIN(tw1.re); PIN(tw1.im); PIN(ua); PIN(a0); PIN(a1s); PIN(sw0); PIN(sw1); PIN(sr0); PIN(tb1); PIN(wst); PIN(sb); PIN(tb2);
    const unsigned ep = read_epoch(p.ws);
    u32x4 tg = tag_bits(ep, g), tl = tag_bits(ep, lane & 3);
    PIN(tg); PIN(tl);
    asm volatile("s_waitcnt vmcnt(3)" : "+v"(x0), "+v"(x1), "+v"(spv) :: "memory");
    STAMP(1);
    if (w < 2) { const int v = min(tid >> 4, 5); *LP(unsigned short, L_PX + c16 * 32 + 4 + 2 * v + (v & 4)) = (unsigned short)spv; }
    lw128(ua, pk4(dftA(twpk(dftA(x0, F), tw1), F))); lw128(ua + 1072, pk4(dftA(twpk(dftA(x1, F), tw1), F)));
    BAR();
    {
        const u32x4 d0 = {lr32(a0), lr32(a0 + 1072), lr32(a0 + 2144), lr32(a0 + 3216)};
        const u32x4 d1 = {lr32(a1s), lr32(a1s + 1072), lr32(a1s + 2144), lr32(a1s + 3216)};
        u32x4 pkt = lr128(L_PX + jm * 32 + (g & 1) * 16);
        const Cx c0 = dftA(d0, F), c1 = dftA(d1, F);
        lw128(sw0, zipt(c0.re, c0.im, tg)); lw128(sw1, zipt(c1.re, -c1.im, tg));
        if (g == 0) pkt[0] = pk(c0.re[0], c0.im[0]);
        pkt[3] = ep;
        const u32x4 s0 = lr128(sr0), s1 = lr128(sr0 + 8 * 144);
        st128(rws, tb1, s0); st128(rws, tb1 + 1024, s1);
        if (tid < 32) st128(rws, WS_T1 + me * TB + 16384 + jm * 32 + g * 16, pkt);
    }
    STAMP(2);
    asm volatile("s_waitcnt vmcnt(0)" : "+v"(wl0), "+v"(wl1), "+v"(sink) :: "memory");
    lw64(wst, u32x2{wl0[0], wl0[1]}); lw64(wst + 8, u32x2{wl0[2], wl0[3]});
    lw64(wst + 10240, u32x2{wl1[0], wl1[1]}); lw64(wst + 10248, u32x2{wl1[2], wl1[3]});
    BAR();
    STAMP(3);
    u32x4 i0 = ld128(rws, tb2), i1 = ld128s(rws, tb2, 64 * TB), isp = {0, 0, 0, ep};
    const unsigned tb4 = WS_T1 + t16 * TB + 16384 + j1 * 32 + (g & 1) * 16;
    if (kb == 0) isp = ld128(rws, tb4);
    FENCE();
    Cx a1, bz, a2, bv;
    {
        const int col = 2 * w + (g >> 1);
        const float th = (float)(16 * kb + col) * (1.f / 512), sn = __builtin_amdgcn_sinf(th), cs = __builtin_amdgcn_cosf(th);
        unsigned wp = L_W + (c16 + 64 * (g & 1)) * 40 + col * 2, wq = wp + 10240 + 30 - col * 4;
        PIN(wp); PIN(wq);
        f32x4 pr, pi, qr, qi;
#pragma unroll
        for (int r = 0; r < 4; ++r) { pr[r] = lrbf(wp + r * 640); pi[r] = lrbf(wp + r * 640 + 5120); qr[r] = lrbf(wq + r * 640); qi[r] = lrbf(wq + r * 640 + 5120); }
        const float sc = 1.f / 1048576;
        const f32x4 sr = (pr + qr) * sc, si = (pi - qi) * sc, dr = (pr - qr) * sc, di = (pi + qi) * sc;
        a1.re = sr - dr * sn; a1.im = si - di * sn; a2.re = sr + dr * sn; a2.im = si + di * sn;
        bz.re = dr * cs; bz.im = di * cs;
    }
    unsigned ca = L_C + lane * 16;
    PIN(ca);
    Mat F1 = {lr128(ca), lr128(ca + 1024)}, F2 = {lr128(ca + 2048), lr128(ca + 3072)}, G = {lr128(ca + 4096), lr128(ca + 5120)};
    Cx twa = {lrf128(ca + 6144), lrf128(ca + 7168)}, twb = {lrf128(ca + 8192), lrf128(ca + 9216)};
    unsigned ta = L_U + ((w >> 1) * 2 + 8 * (w & 1)) * 1040 + lane * 16;
    unsigned tb3 = WS_T2 + (kb * 16 + j1) * TB + s2a * 128 + ch * 16;
    const rsrc_t ro = mkrsrc(p.wo, 512 * 512 * 2), rout = mkrsrc(p.out, 2048 * 1024 * 2);
    unsigned vo = (w * 64 + r8) * 1024 + ct * 128 + ((ch ^ (r8 & 6)) << 4);
    const u32x4 tw = tag_bits(ep, w >> 1);
    unsigned t2 = (w & 1) ? ((lane & 8) ? tw[3] : tw[2]) : ((lane & 8) ? tw[1] : tw[0]);
    PIN(t2);
    PIN(a1.re); PIN(a1.im); PIN(a2.re); PIN(a2.im); PIN(bz.re); PIN(bz.im);
    PIN(F1.re); PIN(F1.im); PIN(F2.re); PIN(F2.im); PIN(G.re); PIN(G.im); PIN(twa.re); PIN(twa.im); PIN(twb.re); PIN(twb.im);
    PIN(ta); PIN(tb3); PIN(vo);
    i0 ^= tl; i1 ^= tl;
#pragma unroll 1
    for (int it = 0; it < SPIN_CAP && (stale(or4(or4(0, i0), i1)) || __any(isp[3] != ep)); ++it) {
        __builtin_amdgcn_s_sleep(2);
        FENCE();
        i0 = ld128(rws, tb2) ^ tl; i1 = ld128s(rws, tb2, 64 * TB) ^ tl;
        if (kb == 0) isp = ld128(rws, tb4);
    }
    STAMP(10);
    lw32(sb, i0[0]); lw32(sb + 128, i0[1]); lw32(sb + 8320, i0[2]); lw32(sb + 8320 + 128, i0[3]);
    lw32(sb + 512, i1[0]); lw32(sb + 512 + 128, i1[1]); lw32(sb + 512 + 8320, i1[2]); lw32(sb + 512 + 8320 + 128, i1[3]);
    if (kb == 0) { if (g == 0) lw32(L_SP + ((t16 >> 5) * 8 + (t16 & 7)) * 16 + ((t16 >> 3) & 3) * 4, isp[0]); lw128(L_PW + t16 * 32 + (g & 1) * 16, isp); }
    BAR();
    STAMP(4);
    {
        const u32x4 zi = lr128(ta), vi = lr128(ta + 1040);
#pragma unroll 1
        for (int q = 0; q < 8; ++q) dma(ro, vo, q * 8192, w * 8192 + q * 1024);
        const Cx Z = dftB(twpk(dftA(zi, F1), twa), G), V = dftB(twpk(dftA(vi, F1), twa), G);
        Cx Vz = V;
        bv = bz;
        if (special) {
            u32x4 v0 = lr128(L_SP + (g * 8 + (c16 & 7)) * 16) ^ 0x80000000u;
            if (c16 >= 8) v0 = vi;
            Vz = dftB(twpk(dftA(v0, F1), twa), G);
            if (g < 2) {
                const float hs = 0.5f / 1048576;
#pragma unroll
                for (int r = 0; r < 4; ++r) {
                    const u32x4 e = lr128(L_PW + (c16 + 64 * g + 16 * r) * 32);
                    const unsigned er = lr32(L_PW + (c16 + 64 * g + 16 * r) * 32 + 16);
                    const float ar = bf(e[1]), ai = bf(e[1] >> 16), br = bf(e[2]), bi = bf(e[2] >> 16);
                    a1.re[r] = 0.5f * a1.re[r] + hs * (ar + br); a1.im[r] = 0.5f * a1.im[r] + hs * (bi - ai);
                    bz.re[r] = 0.5f * bz.re[r] + hs * (ar - br); bz.im[r] = 0.5f * bz.im[r] - hs * (ai + bi);
                    a2.re[r] = 4.f * hs * bf(er); a2.im[r] = 4.f * hs * bf(er >> 16);
                    bv.re[r] = 0.f; bv.im[r] = 0.f;
                }
            }
        }
        const u32x4 zc = zip(a1.re * Z.re - a1.im * Z.im - bz.re * Vz.im - bz.im * Vz.re, bz.im * Vz.im - a1.re * Z.im - a1.im * Z.re - bz.re * Vz.re);
        const u32x4 vc = zip(a2.re * V.re - a2.im * V.im + bv.re * Z.im + bv.im * Z.re, bv.re * Z.re - a2.re * V.im - a2.im * V.re - bv.im * Z.im);
        const Cx zo = dftB(twpk(dftA(zc, G), twb), F2), vo2 = dftB(twpk(dftA(vc, G), twb), F2);
        lw128(ta, zipt(zo.re, zo.im, u32x4(t2))); lw128(ta + 1040, zipt(vo2.re, -vo2.im, u32x4(t2)));
        BAR();
        const u32x4 o0 = {lr32(sb), lr32(sb + 128), lr32(sb + 8320), lr32(sb + 8320 + 128)};
        const u32x4 o1 = {lr32(sb + 512), lr32(sb + 512 + 128), lr32(sb + 512 + 8320), lr32(sb + 512 + 8320 + 128)};
        st128(rws, tb3, o0); st128(rws, tb3 + 64 * 128, o1);
    }
    STAMP(5);
    unsigned ra[4];
#pragma unroll
    for (int i = 0; i < 4; ++i) {
        const int km = 64 * g + 16 * i + c16, kq = km < 128 ? km : (256 - km) & 127;
        ra[i] = L_U + ((km >> 7) * 16 + 2 * w) * 592 + (kq + 16 * (kq >> 6)) * 4;
        PIN(ra[i]);
    }
    const int set = g & 1, k0 = 16 * (2 * w + (g >> 1));
    unsigned oo = (256 * w + me) * 2048 + 1024 + (64 * g + c16) * 4, sa = WS_O + (rt * 64 + lane) * 1024 + 4;
    unsigned u2 = L_U + (set * 16 + (set ? jm : c16)) * 592 + (k0 + 16 * (k0 >> 6)) * 4;
    unsigned tb = WS_T2 + ((2 * w + (c16 >> 3)) * 16 + 4 * g) * TB + me * 128 + ch * 16;
    PIN(oo); PIN(sa); PIN(u2); PIN(tb);
    VMWAIT(0);
    BAR();
    STAMP(6);
    if (w < 4) {
        u32x4 l0, l1, l2, l3;
#pragma unroll 1
        for (int it = 0; it < SPIN_CAP; ++it) {
            FENCE();
            l0 = ld128(rws, tb) ^ tl; l1 = ld128s(rws, tb, TB) ^ tl; l2 = ld128s(rws, tb, 2 * TB) ^ tl; l3 = ld128s(rws, tb, 3 * TB) ^ tl;
            if (!stale(or4(or4(or4(or4(0, l0), l1), l2), l3))) break;
            __builtin_amdgcn_s_sleep(2);
        }
        STAMP(12);
        u32x4 t[4];
#pragma unroll
        for (int e = 0; e < 4; ++e) t[e] = pk4(dftA(u32x4{l0[e], l1[e], l2[e], l3[e]}, F));
#pragma unroll
        for (int r = 0; r < 4; ++r) lw128(u2 + r * 16, u32x4{t[0][r], t[1][r], t[2][r], t[3][r]});
    }
    BAR();
    STAMP(11);
    {
        const unsigned ts = tag_bits(ep, 0)[1];
        unsigned sen = ts;
        if (w == 0) sen = ld32(rws, sa);
        const u32x4 d0 = {lr32(ra[0]), lr32(ra[1]), lr32(ra[2]), lr32(ra[3])};
        const u32x4 d1 = {lr32(ra[0] + 592), lr32(ra[1] + 592), lr32(ra[2] + 592), lr32(ra[3] + 592)};
        const u32x4 y0 = pk4c(dftB(twpk(dftA(d0, F), tw1), F)), y1 = pk4c(dftB(twpk(dftA(d1, F), tw1), F));
#pragma unroll
        for (int r = 0; r < 4; ++r) {
            __builtin_amdgcn_raw_buffer_store_b32(y0[r], rout, oo + 64 * r, 0, OUT_AUX);
            __builtin_amdgcn_raw_buffer_store_b32(y1[r], rout, oo + 64 * r, 128 * 2048, OUT_AUX);
        }
        STAMP(13);
#pragma unroll 1
        for (int it = 0; it < SPIN_CAP && stale(sen ^ ts); ++it) { __builtin_amdgcn_s_sleep(8); FENCE(); sen = ld32(rws, sa); }
    }
    return ep;
}

__global__ void __launch_bounds__(512, 2) fused_kernel(Params p) {
    const char* code;
    asm volatile("s_getpc_b64 %0" : "=s"(code));
    code = (const char*)((size_t)code & ~(size_t)255);
    const int b = blockIdx.x, xcd = b & 7, i = b >> 3, li = i & 15;
    const int tid = threadIdx.x, lane = tid & 63, w = __builtin_amdgcn_readfirstlane(tid >> 6);
    const int rt = xcd * 4 + (i & 3), ct = i >> 2;
    __shared__ __attribute__((aligned(16))) char smem[LDS_BYTES];
    asm volatile("" :: "s"((unsigned)(size_t)(LAS char*)smem));
    const rsrc_t rws = mkrsrc(p.ws, WS_END);
    Stamps stp; stp.t = 0;
    STAMP(0);
    unsigned ep;
    if (i < 16) ep = role_a(p, rws, xcd, li, tid, lane, w, rt, ct, code STP_ARG);
    else        ep = role_b(p, rws, xcd, li, tid, lane, w, rt, ct, code STP_ARG);
    const int g = lane >> 4, c16 = lane & 15, q = c16 >> 2, kh = w >> 2;
    unsigned ob = WS_O + (rt * 4 + (w & 3)) * 16384 + kh * 8192 + lane * 16;
    unsigned wa = kh * 32768 + (16 * (g >> 1) + 4 * (g & 1) + q) * 128 + 8 * (lane & 3) + (((2 * g + (q >> 1)) & 3) << 5);
    const unsigned scr = 81920;
    unsigned rw = scr + kh * 17408 + ((w & 3) * 16 + c16) * 272 + g * 16, rr = scr + (tid >> 3) * 272 + (tid & 7) * 32;
    unsigned oo = (rt * 64 + (tid >> 3)) * 2048 + ct * 128 + (tid & 7) * 16;
    u32x4 tl = tag_bits(ep, lane & 3);
    PIN(ob); PIN(wa); PIN(rw); PIN(rr); PIN(oo); PIN(tl);
    BAR();
    STAMP(7);
    u32x4 o[8], wf[8][4];
#pragma unroll
    for (int k = 0; k < 8; ++k) o[k] = ld128s(rws, ob, k * 1024);
    FENCE();
#pragma unroll
    for (int k = 0; k < 8; ++k)
#pragma unroll
        for (int t = 0; t < 4; ++t) { const unsigned a = (wa ^ (t << 5)) + k * 4096; wf[k][t] = ltr2(a, a + 1024); }
    SCHED_FENCE();
    f32x4 acc[4];
#pragma unroll 1
    for (int it = 0; it < SPIN_CAP; ++it) {
        unsigned m = 0;
#pragma unroll
        for (int t = 0; t < 4; ++t) acc[t] = Z4;
#pragma unroll
        for (int k = 0; k < 8; ++k) {
            const u32x4 ok = o[k] ^ tl;
#pragma unroll
            for (int t = 0; t < 4; ++t) acc[t] = mma(wf[k][t], ok, acc[t]);
            m = or4(m, ok);
        }
        if (!stale(m)) break;
        __builtin_amdgcn_s_sleep(4);
        FENCE();
#pragma unroll
        for (int k = 0; k < 8; ++k) o[k] = ld128s(rws, ob, k * 1024);
    }
    STAMP(8);
#pragma unroll
    for (int t = 0; t < 4; ++t) lwf128(rw + t * 64, acc[t]);
    BAR();
    const f32x4 y0 = lrf128(rr) + lrf128(rr + 17408), y1 = lrf128(rr + 16) + lrf128(rr + 17408 + 16);
    __builtin_amdgcn_raw_buffer_store_b128(cat(pkv(y0), pkv(y1)), mkrsrc(p.out, 2048 * 1024 * 2), oo, 0, OUT_AUX);
    if (b == 128 && tid == 0) st32(rws, WS_EPOCH, ep);
    STAMP(9);
    if ((i >= 16) == CALROLE && stp.t) while (__builtin_amdgcn_s_memrealtime() - stp.t < 4000) __builtin_amdgcn_s_sleep(1);
}

extern "C" void kernel_launch(void* const* d_in, const int* in_sizes, int n_in,
                              void* d_out, int out_size, void* d_ws, size_t ws_size,
                              hipStream_t stream) {
    Params p;
    p.ax = (const unsigned short*)d_in[0]; p.wqkv = (const unsigned short*)d_in[1]; p.wo = (const unsigned short*)d_in[2];
    p.bx = (const unsigned short*)d_in[3]; p.wr = (const unsigned short*)d_in[4]; p.wi = (const unsigned short*)d_in[5];
    p.out = (unsigned short*)d_out; p.ws = (unsigned char*)d_ws;
    fused_kernel<<<256, 512, 0, stream>>>(p);
}
```

```cpp
#include <hip/hip_runtime.h>
#include <stdint.h>

typedef short    s16x4 __attribute__((ext_vector_type(4)));
typedef __bf16   bf16x8 __attribute__((ext_vector_type(8)));
typedef __bf16   bf16x2 __attribute__((ext_vector_type(2)));
typedef float    f32x2 __attribute__((ext_vector_type(2)));
typedef float    f32x4 __attribute__((ext_vector_type(4)));
typedef float    f32x16 __attribute__((ext_vector_type(16)));
typedef unsigned u32x2 __attribute__((ext_vector_type(2)));
typedef unsigned u32x4 __attribute__((ext_vector_type(4)));
typedef __amdgpu_buffer_rsrc_t rsrc_t;
#define LAS __attribute__((address_space(3)))
#define DI __device__ __forceinline__

constexpr unsigned WS_EPOCH = 0;
constexpr unsigned PART_STRIDE = 2688, WS_PART = 4096;
constexpr unsigned TB = 17024;
constexpr unsigned WS_T1 = 1u << 19, WS_T2 = WS_T1 + 128 * TB;
constexpr unsigned WS_O = WS_T2 + 128 * TB, WS_END = WS_O + (1u << 21);
constexpr int SPIN_CAP = 1 << 15;
constexpr unsigned LDS_BYTES = 163840;
#define OUT_AUX 16
#define STAMP(i) do {} while (0)
#define STP_PARAM
#define STP_ARG

struct Params {
    const unsigned short *ax, *wqkv, *wo, *bx, *wr, *wi;
    unsigned short* out;
    unsigned char* ws;
};

DI rsrc_t mkrsrc(const void* ptr, unsigned bytes) { return __builtin_amdgcn_make_buffer_rsrc((void*)ptr, 0, bytes, 0x00020000); }
DI u32x4 ld128(rsrc_t r, unsigned off)  { return __builtin_amdgcn_raw_buffer_load_b128(r, off, 0, 16); }
DI u32x2 ld64(rsrc_t r, unsigned off)   { return __builtin_amdgcn_raw_buffer_load_b64(r, off, 0, 16); }
DI unsigned ld32(rsrc_t r, unsigned off){ return __builtin_amdgcn_raw_buffer_load_b32(r, off, 0, 16); }
DI u32x2 ld64s(rsrc_t r, unsigned off, unsigned so)   { return __builtin_amdgcn_raw_buffer_load_b64(r, off, so, 16); }
DI u32x4 ld128s(rsrc_t r, unsigned off, unsigned so)  { return __builtin_amdgcn_raw_buffer_load_b128(r, off, so, 16); }
DI unsigned ld32s(rsrc_t r, unsigned off, unsigned so){ return __builtin_amdgcn_raw_buffer_load_b32(r, off, so, 16); }
DI void st128(rsrc_t r, unsigned off, u32x4 v) { __builtin_amdgcn_raw_buffer_store_b128(v, r, off, 0, 16); }
DI void st64(rsrc_t r, unsigned off, u32x2 v) { __builtin_amdgcn_raw_buffer_store_b64(v, r, off, 0, 16); }
DI void st32(rsrc_t r, unsigned off, unsigned v) { __builtin_amdgcn_raw_buffer_store_b32(v, r, off, 0, 16); }
#define FENCE() asm volatile("" ::: "memory")
#define BAR() asm volatile("s_waitcnt lgkmcnt(0)\n\ts_barrier" ::: "memory")
#define VMWAIT(n) asm volatile("s_waitcnt vmcnt(" #n ")" ::: "memory")
#define PIN(x) asm volatile("" : "+v"(x))
#define SCHED_FENCE() __builtin_amdgcn_sched_barrier(0)
DI void dma(rsrc_t r, unsigned voff, unsigned soff, unsigned lds) {
    asm volatile("s_mov_b32 m0, %0\n\ts_nop 0\n\tbuffer_load_dwordx4 %1, %2, %3 offen lds"
                 :: "s"(lds), "v"(voff), "s"(r), "s"(soff) : "memory");
}
DI unsigned pk(float lo, float hi) { f32x2 v = {lo, hi}; return __builtin_bit_cast(unsigned, __builtin_convertvector(v, bf16x2)); }
DI u32x2 pkv(f32x4 v) { return u32x2{pk(v[0], v[1]), pk(v[2], v[3])}; }
DI u32x4 cat(u32x2 a, u32x2 b) { return u32x4{a[0], a[1], b[0], b[1]}; }
DI f32x4 mma(u32x4 a, u32x4 b, f32x4 c) {
    return __builtin_amdgcn_mfma_f32_16x16x32_bf16(__builtin_bit_cast(bf16x8, a), __builtin_bit_cast(bf16x8, b), c, 0, 0, 0); }
DI f32x16 mma32(u32x4 a, u32x4 b, f32x16 c) {
    return __builtin_amdgcn_mfma_f32_32x32x16_bf16(__builtin_bit_cast(bf16x8, a), __builtin_bit_cast(bf16x8, b), c, 0, 0, 0); }
DI f32x4 mma16(u32x2 a, u32x2 b, f32x4 c) {
    return __builtin_amdgcn_mfma_f32_16x16x16bf16_1k(__builtin_bit_cast(s16x4, a), __builtin_bit_cast(s16x4, b), c, 0, 0, 0); }
#define LP(T, a) ((LAS T*)(size_t)(a))
DI u32x4 lr128(unsigned a) { return *LP(const u32x4, a); }
DI f32x4 lrf128(unsigned a) { return *LP(const f32x4, a); }
DI u32x2 lr64(unsigned a) { return *LP(const u32x2, a); }
DI unsigned lr32(unsigned a) { return *LP(const unsigned, a); }
DI float bf(unsigned short v) { return __builtin_bit_cast(float, (unsigned)v << 16); }
DI float lrbf(unsigned a) { return bf(*LP(const unsigned short, a)); }
DI void lw128(unsigned a, u32x4 v) { *LP(u32x4, a) = v; }
DI void lwf128(unsigned a, f32x4 v) { *LP(f32x4, a) = v; }
DI void lw64(unsigned a, u32x2 v) { *LP(u32x2, a) = v; }
DI void lw32(unsigned a, unsigned v) { *LP(unsigned, a) = v; }
DI u32x2 ltr(unsigned a) { return __builtin_bit_cast(u32x2, __builtin_amdgcn_ds_read_tr16_b64_v4i16(LP(s16x4, a))); }
DI u32x4 ltr2(unsigned a, unsigned b) { return cat(ltr(a), ltr(b)); }
constexpr unsigned TM = 0x00010001u;
DI u32x4 tag_bits(unsigned ep, int unit) {
    const unsigned t = ((ep << 2) | 2u) >> (8 * unit);
    u32x4 m;
#pragma unroll
    for (int k = 0; k < 4; ++k) m[k] = ((t >> (2 * k)) & 1u) | (((t >> (2 * k + 1)) & 1u) << 16);
    return m;
}
constexpr float TAG_GAIN = 1.0027f;
DI unsigned pkt1(float a, float b, unsigned bits) { return __builtin_amdgcn_bitop3_b32(pk(a, b), TM, bits, 0xBA); }
DI u32x4 zipt(f32x4 a, f32x4 b, u32x4 t) { return u32x4{pkt1(a[0], b[0], t[0]), pkt1(a[1], b[1], t[1]), pkt1(a[2], b[2], t[2]), pkt1(a[3], b[3], t[3])}; }
DI u32x4 catt(f32x4 a, f32x4 b, u32x4 t) { return u32x4{pkt1(a[0], a[1], t[0]), pkt1(a[2], a[3], t[1]), pkt1(b[0], b[1], t[2]), pkt1(b[2], b[3], t[3])}; }
DI unsigned or4(unsigned m, u32x4 v) { return m | v[0] | v[1] | v[2] | v[3]; }
DI bool stale(unsigned m) { return __any(m & TM); }
DI u32x2 phi4(f32x4 t) {
    f32x4 e, m;
#pragma unroll
    for (int r = 0; r < 4; ++r) {
        e[r] = __builtin_amdgcn_fmed3f(__builtin_amdgcn_exp2f(t[r] * 1.44269504f), 0.f, 1.f);
        m[r] = __builtin_elementwise_maximum(t[r], 0.f);
    }
    return pkv((m + e) + 1e-6f);
}
constexpr f32x4 Z4 = {0.f, 0.f, 0.f, 0.f};

struct Mat { u32x4 re, im; };
struct Cx  { f32x4 re, im; };
DI Cx angles(int n0, int dn, int mask, float inv) {
    Cx t;
#pragma unroll
    for (int r = 0; r < 4; ++r) { float a = (float)((n0 + r * dn) & mask) * inv;
        t.re[r] = __builtin_amdgcn_cosf(a); t.im[r] = __builtin_amdgcn_sinf(a); }
    return t;
}
DI u32x4 zip(f32x4 a, f32x4 b) { return u32x4{pk(a[0], b[0]), pk(a[1], b[1]), pk(a[2], b[2]), pk(a[3], b[3])}; }
DI Mat tomat(Cx t) { return Mat{zip(t.re, t.im), zip(-t.im, t.re)}; }
DI Cx dftA(u32x4 d, const Mat& f) { return Cx{mma(d, f.re, Z4), mma(d, f.im, Z4)}; }
DI Cx dftB(u32x4 d, const Mat& f) { return Cx{mma(f.re, d, Z4), mma(f.im, d, Z4)}; }
DI u32x4 twpk(Cx v, const Cx& w) { return zip(v.re * w.re + v.im * w.im, v.im * w.re - v.re * w.im); }
DI u32x4 pk4(Cx v)  { return zip(v.re, v.im); }
DI u32x4 pk4c(Cx v) { return zip(v.re, v.im) ^ 0x80000000u; }

DI unsigned read_epoch(const unsigned char* ws) {
    unsigned e;
    asm volatile("s_load_dword %0, %1, 0x0\n\ts_waitcnt lgkmcnt(0)" : "=s"(e) : "s"(ws) : "memory");
    return e + 1;
}
DI f32x4 q4(const f32x16& v, int i) { return f32x4{v[4 * i], v[4 * i + 1], v[4 * i + 2], v[4 * i + 3]}; }
struct Frag { u32x4 a[2][2], b[2][3]; };
DI void frag_read(Frag& f, unsigned xa, unsigned wa) {
#pragma unroll
    for (int k = 0; k < 2; ++k) {
        f.a[k][0] = lr128(xa ^ (k * 32)); f.b[k][0] = ltr2(wa + k * 2048, wa + k * 2048 + 512);
        f.a[k][1] = lr128((xa ^ (k * 32)) + 4096);
        f.b[k][1] = ltr2(wa + k * 2048 + 8192, wa + k * 2048 + 8704); f.b[k][2] = ltr2(wa + k * 2048 + 16384, wa + k * 2048 + 16896);
    }
}
DI void frag_mma(const Frag& f, f32x16 (&acc)[2][3]) {
#pragma unroll
    for (int k = 0; k < 2; ++k)
#pragma unroll
        for (int j = 0; j < 3; ++j)
#pragma unroll
            for (int i = 0; i < 2; ++i) acc[i][j] = (j < 2) ? mma32(f.a[k][i], f.b[k][j], acc[i][j]) : mma32(f.b[k][j], f.a[k][i], acc[i][j]);
}
DI void interleave() {
#pragma unroll
    for (int i = 0; i < 8; ++i) { __builtin_amdgcn_sched_group_barrier(0x8, 1, 0); __builtin_amdgcn_sched_group_barrier(0x100, 2, 0); }
    __builtin_amdgcn_sched_group_barrier(0x8, 4, 0);
}
#define DEPTH 2
#define ROTK 0
constexpr unsigned CODE_BYTES = 19456;

constexpr unsigned SLOT = 40960;
constexpr unsigned A_RED = 65536;
DI void a_loader(const Params& p, int rp, int hg, int li, int lane, int lw, int ct STP_PARAM) {
    const int rot = ROTK == 0 ? 0 : ROTK == 1 ? 2 * (li >> 2) + 2 * (li & 3) : (li & 1) * 4;
    const int r8 = lane >> 3, ch = lane & 7;
    const rsrc_t rx = mkrsrc(p.ax, 2048 * 512 * 2), rw = mkrsrc(p.wqkv, 512 * 1536 * 2);
    const unsigned vx0 = (rp * 128 + lw * 32 + r8) * 1024 + ((ch ^ (r8 >> 1)) << 4), vx1 = vx0 ^ 64;
    const unsigned vw = (lw * 16 + r8) * 3072 + hg * 128 + ((ch ^ ((r8 & 2) << 1)) << 4);
#pragma unroll 1
    for (int s = 0; s < 8; ++s) {
        if (s == 0) STAMP(6);
        if (s >= 2) { VMWAIT(10); BAR(); }
        if (s == 2) STAMP(11);
        const unsigned kb = (s + rot) & 7, so = kb * (64 * 3072), xo = kb * 128;
        const unsigned lx = (s & 3) * SLOT + lw * 4096, lk = (s & 3) * SLOT + 16384 + lw * 2048;
        dma(rx, vx0, xo, lx); dma(rx, vx1, xo + 8192, lx + 1024);
        dma(rw, vw, so + 1024, lk); dma(rw, vw, so + 25600, lk + 1024);
        dma(rx, vx0, xo + 16384, lx + 2048); dma(rx, vx1, xo + 24576, lx + 3072);
        dma(rw, vw, so + 2048, lk + 8192); dma(rw, vw, so + 26624, lk + 9216);
        dma(rw, vw, so, lk + 16384); dma(rw, vw, so + 24576, lk + 17408);
    }
    const rsrc_t ro = mkrsrc(p.wo, 512 * 512 * 2);
    const unsigned vo = (lw * 64 + r8) * 1024 + ct * 128 + ((ch ^ (r8 & 6)) << 4);
    VMWAIT(10);
#pragma unroll 1
    for (int u = 0; u < 2; ++u) {
        BAR();
#pragma unroll 1
        for (int q = 0; q < 8; ++q) dma(ro, vo, u * 262144 + q * 8192, u * 32768 + lw * 8192 + q * 1024);
        VMWAIT(8);
    }
    STAMP(1);
    BAR(); VMWAIT(0); BAR(); BAR();
}
DI void a_tile(rsrc_t rws, unsigned ep, int rp, int hg, int lane, int w STP_PARAM) {
    const int g = lane >> 4, c16 = lane & 15, h = lane >> 5, r32 = lane & 31, rh = w >> 1, hp = w & 1;
    const unsigned xa = (rh * 64 + r32) * 128 + ((h ^ ((r32 >> 1) & 7)) << 4);
    const unsigned wa = 16384 + (8 * h + (c16 >> 2)) * 128 + ((hp ^ (c16 >> 3)) << 6) + (g & 1) * 32 + 8 * (lane & 3);
    f32x16 acc[2][3];
#pragma unroll
    for (int i = 0; i < 2; ++i)
#pragma unroll
        for (int j = 0; j < 3; ++j) { acc[i][j] = 0.f; PIN(acc[i][j]); }
    const unsigned pg = WS_PART + hg * PART_STRIDE + hp * 1024;
    unsigned red = A_RED + hp * 4096 + lane * 16;
    unsigned pst = pg + rp * 8 * PART_STRIDE + lane * 16;
    unsigned pz = c16 < 4 ? pg + rp * 8 * PART_STRIDE + 2048 - hp * 768 + g * 64 + c16 * 16 : WS_END;
    unsigned pk0 = pg + rh * 64 * PART_STRIDE + lane * 16, pz0 = pg + rh * 64 * PART_STRIDE + 2048 - hp * 768 + (g ^ 1) * 64 + (lane & 3) * 16;
    unsigned ob = WS_O + ((rp * 8 + rh * 4 + (g & 1)) * 16 + hg * 2 + hp) * 1024 + (h * 16 + c16) * 16;
    u32x4 tg = tag_bits(ep, lane & 3), sel;
#pragma unroll
    for (int k = 0; k < 4; ++k)
        sel[k] = (r32 < 16 && ((r32 >> 2) & 1) == h && ((r32 >> 3) * 2 + ((r32 >> 1) & 1)) == k) ? ((r32 & 1) ? 0x3F800000u : 0x00003F80u) : 0u;
    PIN(red); PIN(pst); PIN(pz); PIN(pk0); PIN(pz0); PIN(ob); PIN(tg); PIN(sel);
    Frag f0, f1;
    BAR();
    frag_read(f0, xa, wa);
#pragma unroll 1
    for (int s = 0; s < 7; ++s) {
        const unsigned slot = (s & 3) * SLOT, nslot = ((s + 1) & 3) * SLOT;
        SCHED_FENCE();
        frag_read(f1, slot + (xa ^ 64), slot + wa + 4096); frag_mma(f0, acc); interleave();
        SCHED_FENCE(); BAR();
        frag_read(f0, nslot + xa, nslot + wa); frag_mma(f1, acc); interleave();
    }
    SCHED_FENCE();
    frag_read(f1, 3 * SLOT + (xa ^ 64), 3 * SLOT + wa + 4096); frag_mma(f0, acc); interleave();
    SCHED_FENCE();
    frag_mma(f1, acc);
    STAMP(1);
    {
        f32x16 kv = 0.f, zz = 0.f;
        const u32x4 ones = {0x3F803F80u, 0x3F803F80u, 0x3F803F80u, 0x3F803F80u};
#pragma unroll
        for (int i = 0; i < 2; ++i)
#pragma unroll
            for (int u = 0; u < 2; ++u) {
                const u32x4 A = cat(phi4(q4(acc[i][0], 2 * u)), phi4(q4(acc[i][0], 2 * u + 1))), B = cat(pkv(q4(acc[i][1], 2 * u)), pkv(q4(acc[i][1], 2 * u + 1)));
                kv = mma32(A, B, kv); zz = mma32(A, ones, zz);
            }
        f32x4 k0 = (g & 1) ? q4(kv, 2) : q4(kv, 0), k1 = (g & 1) ? q4(kv, 3) : q4(kv, 1);
        f32x4 z0 = (g & 1) ? q4(zz, 2) : q4(zz, 0), z1 = (g & 1) ? q4(zz, 3) : q4(zz, 1);
        if (rh) { lwf128(red, k0); lwf128(red + 1024, k1); lwf128(red + 2048, z0); lwf128(red + 3072, z1); }
        BAR();
        if (!rh) {
            k0 += lrf128(red); k1 += lrf128(red + 1024); z0 += lrf128(red + 2048); z1 += lrf128(red + 3072);
            st128(rws, pst, catt(k0, k1, tg)); st128(rws, pz, catt(z0, z1, tg));
        }
    }
    STAMP(2);
    SCHED_FENCE();
    u32x4 pq[2][2];
#pragma unroll
    for (int i = 0; i < 2; ++i)
#pragma unroll
        for (int b = 0; b < 2; ++b) { pq[i][b] = cat(phi4(q4(acc[i][2], 2 * b)), phi4(q4(acc[i][2], 2 * b + 1))); PIN(pq[i][b]); }
    VMWAIT(0);
    BAR();
    STAMP(3);
    f32x16 sk, sz;
#pragma unroll 1
    for (int it = 0; it < SPIN_CAP; ++it) {
        FENCE();
        u32x4 dk[8], dz[8];
#pragma unroll
        for (int j = 0; j < 8; ++j) { dk[j] = ld128s(rws, pk0, j * 8 * PART_STRIDE); dz[j] = ld128s(rws, pz0, j * 8 * PART_STRIDE); }
        unsigned m = 0;
        sk = 0.f; sz = 0.f;
#pragma unroll
        for (int j = 0; j < 8; ++j) {
            const u32x4 a = dk[j] ^ tg, c = dz[j] ^ tg;
            sk = mma32(sel, a, sk); sz = mma32(sel, c, sz);
            m = or4(or4(m, a), c);
        }
        if (!stale(m)) break;
        __builtin_amdgcn_s_sleep(1);
    }
    const unsigned ex = red + 8192 * rh, ey = red + 8192 * (rh ^ 1);
    lwf128(ex, q4(sk, 0)); lwf128(ex + 1024, q4(sk, 1)); lwf128(ex + 2048, q4(sz, 0)); lwf128(ex + 3072, q4(sz, 1));
    BAR();
    STAMP(4);
    const u32x4 KV = cat(pkv(q4(sk, 0) + lrf128(ey)), pkv(q4(sk, 1) + lrf128(ey + 1024)));
    const u32x4 ZZ = cat(pkv(q4(sz, 0) + lrf128(ey + 2048)), pkv(q4(sz, 1) + lrf128(ey + 3072)));
#pragma unroll
    for (int b = 0; b < 2; ++b) {
        const u32x4 A = ((g & 1) == b) ? KV : ZZ;
#pragma unroll
        for (int i = 0; i < 2; ++i) {
            const f32x16 nd = mma32(A, pq[i][b], 0.f);
            const float ri = TAG_GAIN * __builtin_amdgcn_rcpf(nd[b ? 0 : 8] + 1e-6f);
            st128(rws, ob + i * 32768 + b * 512, catt(q4(nd, 2 * b) * ri, q4(nd, 2 * b + 1) * ri, tg));
        }
    }
    STAMP(5);
}

constexpr unsigned L_U = 98304;
constexpr unsigned L_S = 65536;
constexpr unsigned L_W = L_U + 19200;
constexpr unsigned L_SP = L_W + 20480;
constexpr unsigned L_C = L_SP + 512;
constexpr unsigned L_PW = L_C + 10240;
constexpr unsigned L_PX = L_PW + 4096;
#define TOUCH 1
struct BIn { u32x4 x0, x1; const char* pf; unsigned pd, sink, spv, csink; };
DI void touch_stage(BIn& in, int lane) {
    if (TOUCH && lane < 10) asm volatile("global_load_dword %0, %1, off" : "+v"(in.sink) : "v"(in.pf) : "memory");
    in.pf += in.pd;
}
DI void b_loads(const Params& p, BIn& in, int xcd, int li, int tid, int lane, int w) {
    const int g = lane >> 4, c16 = lane & 15, me = xcd * 16 + li;
    const rsrc_t rb = mkrsrc(p.bx, 2048 * 512 * 2);
    const unsigned vo = (256 * w + me) * 1024 + g * 256 + c16 * 4;
    asm volatile("buffer_load_dword %0, %8, %9, 0 offen nt\n\tbuffer_load_dword %1, %8, %9, 0 offen offset:64 nt\n\t"
                 "buffer_load_dword %2, %8, %9, 0 offen offset:128 nt\n\tbuffer_load_dword %3, %8, %9, 0 offen offset:192 nt\n\t"
                 "buffer_load_dword %4, %8, %9, %10 offen nt\n\tbuffer_load_dword %5, %8, %9, %10 offen offset:64 nt\n\t"
                 "buffer_load_dword %6, %8, %9, %10 offen offset:128 nt\n\tbuffer_load_dword %7, %8, %9, %10 offen offset:192 nt"
                 : "=&v"(in.x0[0]), "=&v"(in.x0[1]), "=&v"(in.x0[2]), "=&v"(in.x0[3]), "=&v"(in.x1[0]), "=&v"(in.x1[1]), "=&v"(in.x1[2]), "=&v"(in.x1[3])
                 : "v"(vo), "s"(rb), "s"(131072u) : "memory");
    const int n = w * 10 + min(lane, 9), j = max(n - 32, 0), kr = j / 12, cc = j - 12 * kr;
    in.pf = n < 32 ? (const char*)p.ax + (((xcd >> 1) * 512 + li * 32 + n) * 1024 + 256)
                   : (const char*)p.wqkv + ((128 + li * 4 + kr) * 3072 + (cc >> 2) * 1024 + ((xcd & 1) * 4 + (cc & 3)) * 128);
    in.pd = n < 32 ? 128 : 64 * 3072;
    in.sink = 0;
    touch_stage(in, lane); touch_stage(in, lane);
    const int v = min(tid >> 4, 5), ks = c16 + 16 * me;
    const unsigned short* sp = ((v & 1) ? p.wi : p.wr) + (v < 2 ? ((2048 - ks) & 2047) * 257 : ks * 257 + (v < 4 ? 256 : 128));
    if (w < 2) asm volatile("global_load_ushort %0, %1, off" : "=v"(in.spv) : "v"(sp) : "memory");
}
struct BMid { Mat F; Cx tw1; u32x4 tl; };
DI void b_first(const Params& p, rsrc_t rws, unsigned ep, BIn& in, BMid& mid, int xcd, int li, int tid, int lane, int w, int ct STP_PARAM) {
    const int g = lane >> 4, c16 = lane & 15;
    const int me = xcd * 16 + li;
    const int j1 = (li >> 3) ? (xcd ? 16 - xcd : 8) : xcd, kb = li & 7;
    const int r8 = lane >> 3, ch = lane & 7, jm = (16 - c16) & 15;
    u32x4 &x0 = in.x0, &x1 = in.x1, wl0, wl1;
    unsigned tb1 = WS_T1 + me * TB + w * 2048 + lane * 16;
    const bool special = (kb == 0 && w == 0);
    if (w < 5) {
        const int k = w, k4 = 4 * k, sU = (0x04004 >> k4) & 15, sQ = (0x40040 >> k4) & 15;
        const int P = (((c16 & ((0x7F7FF >> k4) & 15)) << sU) + (sU ? j1 : 0)) << ((0x00833 >> k4) & 15);
        const int n0 = P * (((4 * (g & ((0x31133 >> k4) & 15))) << sQ) + (sQ ? j1 : 0)), dn = P << sQ;
        const Cx t = angles(n0, dn, 2047, 1.f / 2048);
        Mat m = tomat(t);
        if (k > 2) m = Mat{__builtin_bit_cast(u32x4, t.re), __builtin_bit_cast(u32x4, t.im)};
        if (k == 2 && (g >> 1) != (c16 >> 3)) m = Mat{u32x4{0, 0, 0, 0}, u32x4{0, 0, 0, 0}};
        lw128(L_C + k * 2048 + lane * 16, m.re); lw128(L_C + k * 2048 + 1024 + lane * 16, m.im);
    }
    touch_stage(in, lane);
    Mat F = tomat(angles(4 * g * c16, c16, 15, 1.f / 16));
    Cx tw1 = angles(4 * g * c16, c16, 255, 1.f / 256);
    const int km0 = 16 * w + c16, km1 = km0 ? 256 - km0 : 128;
    unsigned ua = L_U + (2 * w) * 1072 + g * 272 + c16 * 16;
    unsigned a0 = L_U + 4 * g * 1072 + ((km0 >> 2) & 3) * 272 + (km0 >> 4) * 16 + (km0 & 3) * 4;
    unsigned a1s = L_U + 4 * g * 1072 + ((km1 >> 2) & 3) * 272 + (km1 >> 4) * 16 + (km1 & 3) * 4;
    unsigned sw0 = L_S + w * 2304 + c16 * 144 + g * 16, sw1 = L_S + w * 2304 + jm * 144 + 64 + g * 16;
    unsigned sr0 = L_S + w * 2304 + r8 * 144 + ch * 16;
    unsigned wst = L_W + (w >> 2) * 5120 + ((tid >> 1) & 127) * 40 + (tid & 1) * 16;
    const int lam = tid >> 3, s2a = (lam >> 5) * 32 + (lam & 3) * 8 + ((lam >> 2) & 7);
    const int t16 = (w >> 2) * 32 + (lane & 8) * 8 + (lane & 3) * 8 + (w & 3) * 2 + ((lane >> 2) & 1);
    unsigned sb = L_U + ((ch & 3) * 2 + (ch >> 2)) * 1040 + ((lam >> 5) * 16 + ((lam >> 2) & 7)) * 16 + (lam & 3) * 4;
    unsigned tb2 = WS_T1 + s2a * TB + (kb * 16 + j1) * 128 + ch * 16;
    PIN(F.re); PIN(F.im); PIN(tw1.re); PIN(tw1.im); PIN(ua); PIN(a0); PIN(a1s); PIN(sw0); PIN(sw1); PIN(sr0); PIN(tb1); PIN(wst); PIN(sb); PIN(tb2);
    u32x4 tg = tag_bits(ep, g), tl = tag_bits(ep, lane & 3);
    PIN(tg); PIN(tl);
    const rsrc_t ro = mkrsrc(p.wo, 512 * 512 * 2);
    const int ks = j1 + 16 * ((tid >> 1) & 127), hf = (tid & 1) * 8;
    unsigned vo = (w * 64 + r8) * 1024 + ct * 128 + ((ch ^ (r8 & 6)) << 4);
    unsigned wo0 = (ks * 257 + 16 * kb + hf) * 2, wo1 = (((2048 - ks) & 2047) * 257 + 241 - 16 * kb + hf) * 2;
    PIN(vo); PIN(wo0); PIN(wo1);
    touch_stage(in, lane);
    asm volatile("global_load_dwordx4 %0, %2, %4 nt\n\tglobal_load_dwordx4 %1, %3, %4 nt" : "=&v"(wl0), "=&v"(wl1)
                 : "v"(wo0), "v"(wo1), "s"(w >= 4 ? p.wi : p.wr) : "memory");
    asm volatile("s_waitcnt vmcnt(%c4)" : "+v"(x0), "+v"(x1), "+v"(in.spv), "+v"(in.csink) : "i"(TOUCH ? 4 : 2) : "memory");
    STAMP(1);
    if (w < 2) { const int v = min(tid >> 4, 5); *LP(unsigned short, L_PX + c16 * 32 + 4 + 2 * v + (v & 4)) = (unsigned short)in.spv; }
    lw128(ua, pk4(dftA(twpk(dftA(x0, F), tw1), F))); lw128(ua + 1072, pk4(dftA(twpk(dftA(x1, F), tw1), F)));
    touch_stage(in, lane);
    BAR();
    touch_stage(in, lane);
    {
        const u32x4 d0 = {lr32(a0), lr32(a0 + 1072), lr32(a0 + 2144), lr32(a0 + 3216)};
        const u32x4 d1 = {lr32(a1s), lr32(a1s + 1072), lr32(a1s + 2144), lr32(a1s + 3216)};
        u32x4 pkt = lr128(L_PX + jm * 32 + (g & 1) * 16);
        const Cx c0 = dftA(d0, F), c1 = dftA(d1, F);
        lw128(sw0, zipt(c0.re, c0.im, tg)); lw128(sw1, zipt(c1.re, -c1.im, tg));
        if (g == 0) pkt[0] = pk(c0.re[0], c0.im[0]) & ~TM;
        pkt[3] = ep;
        const u32x4 s0 = lr128(sr0), s1 = lr128(sr0 + 8 * 144);
        st128(rws, tb1, s0); st128(rws, tb1 + 1024, s1);
        if (tid < 32) st128(rws, WS_T1 + me * TB + 16384 + jm * 32 + g * 16, pkt);
    }
    STAMP(2);
    asm volatile("s_waitcnt vmcnt(0)" : "+v"(wl0), "+v"(wl1), "+v"(in.sink) :: "memory");
    lw64(wst, u32x2{wl0[0], wl0[1]}); lw64(wst + 8, u32x2{wl0[2], wl0[3]});
    lw64(wst + 10240, u32x2{wl1[0], wl1[1]}); lw64(wst + 10248, u32x2{wl1[2], wl1[3]});
    BAR();
    STAMP(3);
    u32x4 i0 = ld128(rws, tb2), i1 = ld128s(rws, tb2, 64 * TB), isp = {0, 0, 0, ep};
    const unsigned tb4 = WS_T1 + t16 * TB + 16384 + j1 * 32 + (g & 1) * 16;
    if (kb == 0) isp = ld128(rws, tb4);
    FENCE();
    Cx a1, bz, a2, bv;
    {
        const int col = 2 * w + (g >> 1);
        const float th = (float)(16 * kb + col) * (1.f / 512), sn = __builtin_amdgcn_sinf(th), cs = __builtin_amdgcn_cosf(th);
        unsigned wp = L_W + (c16 + 64 * (g & 1)) * 40 + col * 2, wq = wp + 10240 + 30 - col * 4;
        PIN(wp); PIN(wq);
        f32x4 pr, pi, qr, qi;
#pragma unroll
        for (int r = 0; r < 4; ++r) { pr[r] = lrbf(wp + r * 640); pi[r] = lrbf(wp + r * 640 + 5120); qr[r] = lrbf(wq + r * 640); qi[r] = lrbf(wq + r * 640 + 5120); }
        const float sc = TAG_GAIN * TAG_GAIN / 1048576;
        const f32x4 sr = (pr + qr) * sc, si = (pi - qi) * sc, dr = (pr - qr) * sc, di = (pi + qi) * sc;
        a1.re = sr - dr * sn; a1.im = si - di * sn; a2.re = sr + dr * sn; a2.im = si + di * sn;
        bz.re = dr * cs; bz.im = di * cs;
    }
    unsigned ca = L_C + lane * 16;
    PIN(ca);
    Mat F1 = {lr128(ca), lr128(ca + 1024)}, F2 = {lr128(ca + 2048), lr128(ca + 3072)}, G = {lr128(ca + 4096), lr128(ca + 5120)};
    Cx twa = {lrf128(ca + 6144), lrf128(ca + 7168)}, twb = {lrf128(ca + 8192), lrf128(ca + 9216)};
    unsigned ta = L_U + ((w >> 1) * 2 + 8 * (w & 1)) * 1040 + lane * 16;
    unsigned tb3 = WS_T2 + (kb * 16 + j1) * TB + s2a * 128 + ch * 16;
    const u32x4 tw = tag_bits(ep, w >> 1);
    unsigned t2 = (w & 1) ? ((lane & 8) ? tw[3] : tw[2]) : ((lane & 8) ? tw[1] : tw[0]);
    PIN(t2);
    PIN(a1.re); PIN(a1.im); PIN(a2.re); PIN(a2.im); PIN(bz.re); PIN(bz.im);
    PIN(F1.re); PIN(F1.im); PIN(F2.re); PIN(F2.im); PIN(G.re); PIN(G.im); PIN(twa.re); PIN(twa.im); PIN(twb.re); PIN(twb.im);
    PIN(ta); PIN(tb3);
    i0 ^= tl; i1 ^= tl;
#pragma unroll 1
    for (int it = 0; it < SPIN_CAP && (stale(or4(or4(0, i0), i1)) || __any(isp[3] != ep)); ++it) {
        __builtin_amdgcn_s_sleep(2);
        FENCE();
        i0 = ld128(rws, tb2) ^ tl; i1 = ld128s(rws, tb2, 64 * TB) ^ tl;
        if (kb == 0) isp = ld128(rws, tb4);
    }
    STAMP(10);
    lw32(sb, i0[0]); lw32(sb + 128, i0[1]); lw32(sb + 8320, i0[2]); lw32(sb + 8320 + 128, i0[3]);
    lw32(sb + 512, i1[0]); lw32(sb + 512 + 128, i1[1]); lw32(sb + 512 + 8320, i1[2]); lw32(sb + 512 + 8320 + 128, i1[3]);
    if (kb == 0) { if (g == 0) lw32(L_SP + ((t16 >> 5) * 8 + (t16 & 7)) * 16 + ((t16 >> 3) & 3) * 4, isp[0]); lw128(L_PW + t16 * 32 + (g & 1) * 16, isp); }
    BAR();
    STAMP(4);
    {
        const u32x4 zi = lr128(ta), vi = lr128(ta + 1040);
        auto wo2 = [&](int q) { dma(ro, vo, q * 8192, w * 8192 + q * 1024); dma(ro, vo, q * 8192 + 8192, w * 8192 + q * 1024 + 1024); };
        wo2(0);
        u32x4 zt = twpk(dftA(zi, F1), twa), vt = twpk(dftA(vi, F1), twa);
        PIN(zt); PIN(vt);
        wo2(2);
        const Cx Z = dftB(zt, G), V = dftB(vt, G);
        Cx Vz = V;
        bv = bz;
        if (special) {
            u32x4 v0 = lr128(L_SP + (g * 8 + (c16 & 7)) * 16) ^ 0x80000000u;
            if (c16 >= 8) v0 = vi;
            Vz = dftB(twpk(dftA(v0, F1), twa), G);
            if (g < 2) {
                const float hs = 0.5f * TAG_GAIN * TAG_GAIN / 1048576;
#pragma unroll
                for (int r = 0; r < 4; ++r) {
                    const u32x4 e = lr128(L_PW + (c16 + 64 * g + 16 * r) * 32);
                    const unsigned er = lr32(L_PW + (c16 + 64 * g + 16 * r) * 32 + 16);
                    const float ar = bf(e[1]), ai = bf(e[1] >> 16), br = bf(e[2]), bi = bf(e[2] >> 16);
                    a1.re[r] = 0.5f * a1.re[r] + hs * (ar + br); a1.im[r] = 0.5f * a1.im[r] + hs * (bi - ai);
                    bz.re[r] = 0.5f * bz.re[r] + hs * (ar - br); bz.im[r] = 0.5f * bz.im[r] - hs * (ai + bi);
                    a2.re[r] = 4.f * hs * bf(er); a2.im[r] = 4.f * hs * bf(er >> 16);
                    bv.re[r] = 0.f; bv.im[r] = 0.f;
                }
            }
        }
        u32x4 zc = zip(a1.re * Z.re - a1.im * Z.im - bz.re * Vz.im - bz.im * Vz.re, bz.im * Vz.im - a1.re * Z.im - a1.im * Z.re - bz.re * Vz.re);
        u32x4 vc = zip(a2.re * V.re - a2.im * V.im + bv.re * Z.im + bv.im * Z.re, bv.re * Z.re - a2.re * V.im - a2.im * V.re - bv.im * Z.im);
        PIN(zc); PIN(vc);
        wo2(4);
        zt = twpk(dftA(zc, G), twb); vt = twpk(dftA(vc, G), twb);
        PIN(zt); PIN(vt);
        wo2(6);
        const Cx zo = dftB(zt, F2), vo2 = dftB(vt, F2);
        lw128(ta, zipt(zo.re, zo.im, u32x4(t2))); lw128(ta + 1040, zipt(vo2.re, -vo2.im, u32x4(t2)));
        BAR();
        const u32x4 o0 = {lr32(sb), lr32(sb + 128), lr32(sb + 8320), lr32(sb + 8320 + 128)};
        const u32x4 o1 = {lr32(sb + 512), lr32(sb + 512 + 128), lr32(sb + 512 + 8320), lr32(sb + 512 + 8320 + 128)};
        st128(rws, tb3, o0); st128(rws, tb3 + 64 * 128, o1);
    }
    STAMP(5);
    VMWAIT(2);
    BAR();
    mid.F = F; mid.tw1 = tw1; mid.tl = tl;
}
DI void b_second(const Params& p, rsrc_t rws, unsigned ep, const BMid& mid, int xcd, int li, int lane, int w, int rt STP_PARAM) {
    const int g = lane >> 4, c16 = lane & 15, ch = lane & 7, jm = (16 - c16) & 15, me = xcd * 16 + li;
    const Mat& F = mid.F; const Cx& tw1 = mid.tw1; const u32x4 tl = mid.tl;
    const rsrc_t rout = mkrsrc(p.out, 2048 * 1024 * 2);
    unsigned ra[4];
#pragma unroll
    for (int i = 0; i < 4; ++i) {
        const int km = 64 * g + 16 * i + c16, kq = km < 128 ? km : (256 - km) & 127;
        ra[i] = L_U + ((km >> 7) * 16 + 2 * w) * 592 + (kq + 16 * (kq >> 6)) * 4;
        PIN(ra[i]);
    }
    const int set = g & 1, k0 = 16 * (2 * w + (g >> 1));
    unsigned oo = (256 * w + me) * 2048 + 1024 + (64 * g + c16) * 4;
    unsigned u2 = L_U + (set * 16 + (set ? jm : c16)) * 592 + (k0 + 16 * (k0 >> 6)) * 4;
    unsigned tb = WS_T2 + ((2 * w + (c16 >> 3)) * 16 + 4 * g) * TB + me * 128 + ch * 16;
    PIN(oo); PIN(u2); PIN(tb);
    VMWAIT(0);
    BAR();
    STAMP(6);
    if (w < 4) {
        u32x4 l0, l1, l2, l3;
#pragma unroll 1
        for (int it = 0; it < SPIN_CAP; ++it) {
            FENCE();
            l0 = ld128(rws, tb) ^ tl; l1 = ld128s(rws, tb, TB) ^ tl; l2 = ld128s(rws, tb, 2 * TB) ^ tl; l3 = ld128s(rws, tb, 3 * TB) ^ tl;
            if (!stale(or4(or4(or4(or4(0, l0), l1), l2), l3))) break;
            __builtin_amdgcn_s_sleep(2);
        }
        STAMP(12);
        u32x4 t[4];
#pragma unroll
        for (int e = 0; e < 4; ++e) t[e] = pk4(dftA(u32x4{l0[e], l1[e], l2[e], l3[e]}, F));
#pragma unroll
        for (int r = 0; r < 4; ++r) lw128(u2 + r * 16, u32x4{t[0][r], t[1][r], t[2][r], t[3][r]});
    }
    BAR();
    STAMP(11);
    {
        const unsigned ts = tag_bits(ep, 0)[1], sa = WS_O + (rt * 64 + lane) * 1024 + 4;
        unsigned sen = ts;
        if (w == 0) sen = ld32(rws, sa);
        const u32x4 d0 = {lr32(ra[0]), lr32(ra[1]), lr32(ra[2]), lr32(ra[3])};
        const u32x4 d1 = {lr32(ra[0] + 592), lr32(ra[1] + 592), lr32(ra[2] + 592), lr32(ra[3] + 592)};
        const u32x4 y0 = pk4c(dftB(twpk(dftA(d0, F), tw1), F)), y1 = pk4c(dftB(twpk(dftA(d1, F), tw1), F));
#pragma unroll
        for (int r = 0; r < 4; ++r) {
            __builtin_amdgcn_raw_buffer_store_b32(y0[r], rout, oo + 64 * r, 0, OUT_AUX);
            __builtin_amdgcn_raw_buffer_store_b32(y1[r], rout, oo + 64 * r, 128 * 2048, OUT_AUX);
        }
        STAMP(13);
#pragma unroll 1
        for (int it = 0; it < SPIN_CAP && stale(sen ^ ts); ++it) { __builtin_amdgcn_s_sleep(6); FENCE(); sen = ld32(rws, sa); }
    }
}

__global__ void __launch_bounds__(512, 2) fused_kernel(Params p) {
    const int b = blockIdx.x, xcd = b & 7, i = b >> 3, li = i & 15;
    const int tid = threadIdx.x, lane = tid & 63, w = __builtin_amdgcn_readfirstlane(tid >> 6);
    BIn in;
    unsigned& csink = in.csink;
    {
        size_t code;
        asm volatile("s_getpc_b64 %0" : "=s"(code));
        asm volatile("global_load_dword %0, %1, %2" : "=v"(csink) : "v"(min((unsigned)(w * 32 + i) * 128u, CODE_BYTES - 128u)), "s"(code & ~(size_t)255) : "memory");
    }
    __shared__ __attribute__((aligned(16))) char smem[LDS_BYTES];
    asm volatile("" :: "s"((unsigned)(size_t)(LAS char*)smem));
    STAMP(0);
    const int rp = (xcd >> 1) * 4 + (li & 3), hg = (xcd & 1) * 4 + (li >> 2), rt = xcd * 4 + (i & 3), ct = i >> 2;
    if (__builtin_expect(i < 16 && w >= 4, 1)) { a_loader(p, rp, hg, li, lane, w - 4, ct STP_ARG); PIN(csink); }
    if (__builtin_expect(i >= 16, 1)) b_loads(p, in, xcd, li, tid, lane, w);
    const rsrc_t rws = mkrsrc(p.ws, WS_END);
    const unsigned ep = read_epoch(p.ws);
    BMid mid;
    if (i >= 16) b_first(p, rws, ep, in, mid, xcd, li, tid, lane, w, ct STP_ARG);
    else if (w < 4) { asm volatile("s_waitcnt vmcnt(0)" : "+v"(csink) :: "memory"); a_tile(rws, ep, rp, hg, lane, w STP_ARG); }
    const int g = lane >> 4, c16 = lane & 15, q = c16 >> 2, kh = w >> 2;
    unsigned ob = WS_O + (rt * 4 + (w & 3)) * 16384 + kh * 8192 + lane * 16;
    unsigned wa = kh * 32768 + (16 * (g >> 1) + 4 * (g & 1) + q) * 128 + 8 * (lane & 3) + (((2 * g + (q >> 1)) & 3) << 5);
    const unsigned scr = 81920;
    unsigned rw = scr + kh * 17408 + ((w & 3) * 16 + c16) * 272 + g * 16, rr = scr + (tid >> 3) * 272 + (tid & 7) * 32;
    unsigned oo = (rt * 64 + (tid >> 3)) * 2048 + ct * 128 + (tid & 7) * 16;
    u32x4 tl = tag_bits(ep, lane & 3);
    u32x4 o[8], wf[8][4];
#pragma unroll
    for (int k = 0; k < 8; ++k)
#pragma unroll
        for (int t = 0; t < 4; ++t) { const unsigned a = (wa ^ (t << 5)) + k * 4096; wf[k][t] = ltr2(a, a + 1024); }
    PIN(ob); PIN(rw); PIN(rr); PIN(oo); PIN(tl);
    SCHED_FENCE();
    if (i < 16) VMWAIT(0);
    else b_second(p, rws, ep, mid, xcd, li, lane, w, rt STP_ARG);
    BAR();
    STAMP(7);
#pragma unroll
    for (int k = 0; k < 8; ++k) o[k] = ld128s(rws, ob, k * 1024);
    f32x4 acc[4];
#pragma unroll 1
    for (int it = 0; it < SPIN_CAP; ++it) {
        unsigned m = 0;
#pragma unroll
        for (int t = 0; t < 4; ++t) acc[t] = Z4;
#pragma unroll
        for (int k = 0; k < 8; ++k) {
            const u32x4 ok = o[k] ^ tl;
#pragma unroll
            for (int t = 0; t < 4; ++t) acc[t] = mma(wf[k][t], ok, acc[t]);
            m = or4(m, ok);
        }
        if (!stale(m)) break;
        __builtin_amdgcn_s_sleep(4);
        FENCE();
#pragma unroll
        for (int k = 0; k < 8; ++k) o[k] = ld128s(rws, ob, k * 1024);
    }
    STAMP(8);
#pragma unroll
    for (int t = 0; t < 4; ++t) lwf128(rw + t * 64, acc[t]);
    BAR();
    const f32x4 y0 = lrf128(rr) + lrf128(rr + 17408), y1 = lrf128(rr + 16) + lrf128(rr + 17408 + 16);
    __builtin_amdgcn_raw_buffer_store_b128(cat(pkv(y0), pkv(y1)), mkrsrc(p.out, 2048 * 1024 * 2), oo, 0, OUT_AUX);
    if (b == 128 && tid == 0) st32(rws, WS_EPOCH, ep);
    STAMP(9);
}

extern "C" void kernel_launch(void* const* d_in, const int* in_sizes, int n_in,
                              void* d_out, int out_size, void* d_ws, size_t ws_size,
                              hipStream_t stream) {
    Params p;
    p.ax = (const unsigned short*)d_in[0]; p.wqkv = (const unsigned short*)d_in[1]; p.wo = (const unsigned short*)d_in[2];
    p.bx = (const unsigned short*)d_in[3]; p.wr = (const unsigned short*)d_in[4]; p.wi = (const unsigned short*)d_in[5];
    p.out = (unsigned short*)d_out; p.ws = (unsigned char*)d_ws;
    fused_kernel<<<256, 512, 0, stream>>>(p);
}
```

```cpp
#include <hip/hip_runtime.h>
#include <stdint.h>

typedef short    s16x4 __attribute__((ext_vector_type(4)));
typedef __bf16   bf16x8 __attribute__((ext_vector_type(8)));
typedef __bf16   bf16x2 __attribute__((ext_vector_type(2)));
typedef float    f32x2 __attribute__((ext_vector_type(2)));
typedef float    f32x4 __attribute__((ext_vector_type(4)));
typedef float    f32x16 __attribute__((ext_vector_type(16)));
typedef unsigned u32x2 __attribute__((ext_vector_type(2)));
typedef unsigned u32x4 __attribute__((ext_vector_type(4)));
typedef __amdgpu_buffer_rsrc_t rsrc_t;
#define LAS __attribute__((address_space(3)))
#define DI __device__ __forceinline__

constexpr unsigned WS_EPOCH = 0;
constexpr unsigned PART_STRIDE = 2688, WS_PART = 4096;
constexpr unsigned TB = 17024;
constexpr unsigned WS_T1 = 1u << 19, WS_T2 = WS_T1 + 128 * TB;
constexpr unsigned WS_O = WS_T2 + 128 * TB, WS_END = WS_O + (1u << 21);
constexpr int SPIN_CAP = 1 << 15;
constexpr unsigned LDS_BYTES = 163840;
#define OUT_AUX 16
#define STAMP(i) do {} while (0)
#define STP_PARAM
#define STP_ARG

struct Params {
    const unsigned short *ax, *wqkv, *wo, *bx, *wr, *wi;
    unsigned short* out;
    unsigned char* ws;
};

DI rsrc_t mkrsrc(const void* ptr, unsigned bytes) { return __builtin_amdgcn_make_buffer_rsrc((void*)ptr, 0, bytes, 0x00020000); }
DI u32x4 ld128(rsrc_t r, unsigned off)  { return __builtin_amdgcn_raw_buffer_load_b128(r, off, 0, 16); }
DI u32x2 ld64(rsrc_t r, unsigned off)   { return __builtin_amdgcn_raw_buffer_load_b64(r, off, 0, 16); }
DI unsigned ld32(rsrc_t r, unsigned off){ return __builtin_amdgcn_raw_buffer_load_b32(r, off, 0, 16); }
DI u32x2 ld64s(rsrc_t r, unsigned off, unsigned so)   { return __builtin_amdgcn_raw_buffer_load_b64(r, off, so, 16); }
DI u32x4 ld128s(rsrc_t r, unsigned off, unsigned so)  { return __builtin_amdgcn_raw_buffer_load_b128(r, off, so, 16); }
DI unsigned ld32s(rsrc_t r, unsigned off, unsigned so){ return __builtin_amdgcn_raw_buffer_load_b32(r, off, so, 16); }
DI void st128(rsrc_t r, unsigned off, u32x4 v) { __builtin_amdgcn_raw_buffer_store_b128(v, r, off, 0, 16); }
DI void st64(rsrc_t r, unsigned off, u32x2 v) { __builtin_amdgcn_raw_buffer_store_b64(v, r, off, 0, 16); }
DI void st32(rsrc_t r, unsigned off, unsigned v) { __builtin_amdgcn_raw_buffer_store_b32(v, r, off, 0, 16); }
#define FENCE() asm volatile("" ::: "memory")
#define BAR() asm volatile("s_waitcnt lgkmcnt(0)\n\ts_barrier" ::: "memory")
#define VMWAIT(n) asm volatile("s_waitcnt vmcnt(" #n ")" ::: "memory")
#define PIN(x) asm volatile("" : "+v"(x))
#define SCHED_FENCE() __builtin_amdgcn_sched_barrier(0)
DI void dma(rsrc_t r, unsigned voff, unsigned soff, unsigned lds) {
    asm volatile("s_mov_b32 m0, %0\n\ts_nop 0\n\tbuffer_load_dwordx4 %1, %2, %3 offen lds"
                 :: "s"(lds), "v"(voff), "s"(r), "s"(soff) : "memory");
}
DI void dma4(rsrc_t r, unsigned v0, unsigned v1, unsigned v2, unsigned v3, unsigned soff, unsigned lds) {
    asm volatile("s_mov_b32 m0, %0\n\ts_nop 0\n\tbuffer_load_dwordx4 %1, %5, %6 offen lds\n\tbuffer_load_dwordx4 %2, %5, %6 offen offset:1024 lds\n\t"
                 "buffer_load_dwordx4 %3, %5, %6 offen offset:2048 lds\n\tbuffer_load_dwordx4 %4, %5, %6 offen offset:3072 lds"
                 :: "s"(lds), "v"(v0), "v"(v1), "v"(v2), "v"(v3), "s"(r), "s"(soff) : "memory");
}
DI void dma2(rsrc_t r, unsigned v0, unsigned v1, unsigned soff, unsigned lds) {
    asm volatile("s_mov_b32 m0, %0\n\ts_nop 0\n\tbuffer_load_dwordx4 %1, %3, %4 offen lds\n\tbuffer_load_dwordx4 %2, %3, %4 offen offset:1024 lds"
                 :: "s"(lds), "v"(v0), "v"(v1), "s"(r), "s"(soff) : "memory");
}
DI unsigned pk(float lo, float hi) { f32x2 v = {lo, hi}; return __builtin_bit_cast(unsigned, __builtin_convertvector(v, bf16x2)); }
DI u32x2 pkv(f32x4 v) { return u32x2{pk(v[0], v[1]), pk(v[2], v[3])}; }
DI u32x4 cat(u32x2 a, u32x2 b) { return u32x4{a[0], a[1], b[0], b[1]}; }
DI f32x4 mma(u32x4 a, u32x4 b, f32x4 c) {
    return __builtin_amdgcn_mfma_f32_16x16x32_bf16(__builtin_bit_cast(bf16x8, a), __builtin_bit_cast(bf16x8, b), c, 0, 0, 0); }
DI f32x16 mma32(u32x4 a, u32x4 b, f32x16 c) {
    return __builtin_amdgcn_mfma_f32_32x32x16_bf16(__builtin_bit_cast(bf16x8, a), __builtin_bit_cast(bf16x8, b), c, 0, 0, 0); }
DI f32x4 mma16(u32x2 a, u32x2 b, f32x4 c) {
    return __builtin_amdgcn_mfma_f32_16x16x16bf16_1k(__builtin_bit_cast(s16x4, a), __builtin_bit_cast(s16x4, b), c, 0, 0, 0); }
#define LP(T, a) ((LAS T*)(size_t)(a))
DI u32x4 lr128(unsigned a) { return *LP(const u32x4, a); }
DI f32x4 lrf128(unsigned a) { return *LP(const f32x4, a); }
DI u32x2 lr64(unsigned a) { return *LP(const u32x2, a); }
DI unsigned lr32(unsigned a) { return *LP(const unsigned, a); }
DI float bf(unsigned short v) { return __builtin_bit_cast(float, (unsigned)v << 16); }
DI float lrbf(unsigned a) { return bf(*LP(const unsigned short, a)); }
DI void lw128(unsigned a, u32x4 v) { *LP(u32x4, a) = v; }
DI void lwf128(unsigned a, f32x4 v) { *LP(f32x4, a) = v; }
DI void lw64(unsigned a, u32x2 v) { *LP(u32x2, a) = v; }
DI void lw32(unsigned a, unsigned v) { *LP(unsigned, a) = v; }
DI u32x2 ltr(unsigned a) { return __builtin_bit_cast(u32x2, __builtin_amdgcn_ds_read_tr16_b64_v4i16(LP(s16x4, a))); }
DI u32x4 ltr2(unsigned a, unsigned b) { return cat(ltr(a), ltr(b)); }
constexpr unsigned TM = 0x00010001u;
DI u32x4 tag_bits(unsigned ep, int unit) {
    const unsigned t = ((ep << 2) | 2u) >> (8 * unit);
    u32x4 m;
#pragma unroll
    for (int k = 0; k < 4; ++k) m[k] = ((t >> (2 * k)) & 1u) | (((t >> (2 * k + 1)) & 1u) << 16);
    return m;
}
constexpr float TAG_GAIN = 1.0027f;
DI unsigned pkt1(float a, float b, unsigned bits) { return __builtin_amdgcn_bitop3_b32(pk(a, b), TM, bits, 0xBA); }
DI u32x4 zipt(f32x4 a, f32x4 b, u32x4 t) { return u32x4{pkt1(a[0], b[0], t[0]), pkt1(a[1], b[1], t[1]), pkt1(a[2], b[2], t[2]), pkt1(a[3], b[3], t[3])}; }
DI u32x4 catt(f32x4 a, f32x4 b, u32x4 t) { return u32x4{pkt1(a[0], a[1], t[0]), pkt1(a[2], a[3], t[1]), pkt1(b[0], b[1], t[2]), pkt1(b[2], b[3], t[3])}; }
DI unsigned or4(unsigned m, u32x4 v) { return m | v[0] | v[1] | v[2] | v[3]; }
DI bool stale(unsigned m) { return __any(m & TM); }
DI u32x2 phi4(f32x4 t) {
    f32x4 e, m;
#pragma unroll
    for (int r = 0; r < 4; ++r) {
        e[r] = __builtin_amdgcn_fmed3f(__builtin_amdgcn_exp2f(t[r] * 1.44269504f), 0.f, 1.f);
        m[r] = __builtin_elementwise_maximum(t[r], 0.f);
    }
    return pkv((m + e) + 1e-6f);
}
constexpr f32x4 Z4 = {0.f, 0.f, 0.f, 0.f};

struct Mat { u32x4 re, im; };
struct Cx  { f32x4 re, im; };
DI Cx angles(int n0, int dn, int mask, float inv) {
    Cx t;
#pragma unroll
    for (int r = 0; r < 4; ++r) { float a = (float)((n0 + r * dn) & mask) * inv;
        t.re[r] = __builtin_amdgcn_cosf(a); t.im[r] = __builtin_amdgcn_sinf(a); }
    return t;
}
DI u32x4 zip(f32x4 a, f32x4 b) { return u32x4{pk(a[0], b[0]), pk(a[1], b[1]), pk(a[2], b[2]), pk(a[3], b[3])}; }
DI Mat tomat(Cx t) { return Mat{zip(t.re, t.im), zip(-t.im, t.re)}; }
DI Cx dftA(u32x4 d, const Mat& f) { return Cx{mma(d, f.re, Z4), mma(d, f.im, Z4)}; }
DI Cx dftB(u32x4 d, const Mat& f) { return Cx{mma(f.re, d, Z4), mma(f.im, d, Z4)}; }
DI u32x4 twpk(Cx v, const Cx& w) { return zip(v.re * w.re + v.im * w.im, v.im * w.re - v.re * w.im); }
DI u32x4 pk4(Cx v)  { return zip(v.re, v.im); }
DI u32x4 pk4c(Cx v) { return zip(v.re, v.im) ^ 0x80000000u; }

DI void epoch_ask(unsigned& e, const unsigned char* ws) { asm volatile("s_load_dword %0, %1, 0x0" : "=s"(e) : "s"(ws) : "memory"); }
DI void epoch_get(unsigned& e) {
    unsigned r;
    asm volatile("s_waitcnt lgkmcnt(0)\n\ts_add_u32 %0, %1, 1" : "=s"(r) : "s"(e) : "memory", "scc");
    e = r;
}
DI f32x4 q4(const f32x16& v, int i) { return f32x4{v[4 * i], v[4 * i + 1], v[4 * i + 2], v[4 * i + 3]}; }
struct Frag { u32x4 a[2][2], b[2][3]; };
DI void frag_read(Frag& f, unsigned xa, unsigned wa) {
#pragma unroll
    for (int k = 0; k < 2; ++k) {
        f.a[k][0] = lr128(xa ^ (k * 32)); f.b[k][0] = ltr2(wa + k * 10240, wa + k * 10240 + 512);
        f.a[k][1] = lr128((xa ^ (k * 32)) + 10240);
        f.b[k][1] = ltr2(wa + k * 10240 + 2048, wa + k * 10240 + 2560); f.b[k][2] = ltr2(wa + k * 10240 + 4096, wa + k * 10240 + 4608);
    }
}
DI void frag_mma(const Frag& f, f32x16 (&acc)[2][3]) {
#pragma unroll
    for (int k = 0; k < 2; ++k)
#pragma unroll
        for (int j = 0; j < 3; ++j)
#pragma unroll
            for (int i = 0; i < 2; ++i) acc[i][j] = (j < 2) ? mma32(f.a[k][i], f.b[k][j], acc[i][j]) : mma32(f.b[k][j], f.a[k][i], acc[i][j]);
}
DI void interleave() {
#pragma unroll
    for (int i = 0; i < 8; ++i) { __builtin_amdgcn_sched_group_barrier(0x8, 1, 0); __builtin_amdgcn_sched_group_barrier(0x100, 2, 0); }
    __builtin_amdgcn_sched_group_barrier(0x8, 4, 0);
}
#define DEPTH 2
#define ROTK 0
constexpr unsigned CODE_BYTES = 19456;

constexpr unsigned SLOT = 40960;
constexpr unsigned A_RED = 65536;
DI void a_loader(const Params& p, int rp, int hg, int li, int lane, int lw STP_PARAM) {
    const int rot = ROTK == 0 ? 0 : ROTK == 1 ? 2 * (li >> 2) + 2 * (li & 3) : (li & 1) * 4;
    const int r8 = lane >> 3, ch = lane & 7;
    const rsrc_t rx = mkrsrc(p.ax, 2048 * 512 * 2), rw = mkrsrc(p.wqkv, 512 * 1536 * 2);
    const unsigned vx = (rp * 128 + lw * 32 + r8) * 1024 + ((ch ^ (r8 >> 1)) << 4);
    const unsigned vw = (lw * 16 + r8) * 3072 + hg * 128 + ((ch ^ ((r8 & 2) << 1)) << 4);
    const unsigned vx1 = (vx ^ 64) + 7168, vx2 = vx + 14336, vx3 = (vx ^ 64) + 21504, vk = vw + 1024, vw1 = vw + 24576 - 1024;
#pragma unroll 1
    for (int s = 0; s < 8; ++s) {
        if (s == 0) STAMP(6);
        if (s >= 2) { VMWAIT(10); BAR(); }
        if (s == 2) STAMP(11);
        const unsigned kb = (s + rot) & 7, so = kb * (64 * 3072), l = (s & 3) * SLOT + lw * 10240;
        dma4(rx, vx, vx1, vx2, vx3, kb * 128, l);
        dma4(rw, vk, vw + 24576, vw, vw1, so, l + 4096);
        dma2(rw, vw, vw1, so, l + 8192);
    }
}
DI void a_loader_tail(const Params& p, int lane, int lw, int ct STP_PARAM) {
    const int r8 = lane >> 3, ch = lane & 7;
    const rsrc_t ro = mkrsrc(p.wo, 512 * 512 * 2);
    const unsigned vo = (lw * 64 + r8) * 1024 + ct * 128 + ((ch ^ (r8 & 6)) << 4);
    VMWAIT(10);
#pragma unroll 1
    for (int u = 0; u < 2; ++u) {
        BAR();
#pragma unroll 1
        for (int q = 0; q < 8; ++q) dma(ro, vo, u * 262144 + q * 8192, u * 32768 + lw * 8192 + q * 1024);
        VMWAIT(8);
    }
    STAMP(1);
    BAR(); VMWAIT(0); BAR(); BAR();
}
DI void a_tile(rsrc_t rws, unsigned ep, int rp, int hg, int lane, int w STP_PARAM) {
    const int g = lane >> 4, c16 = lane & 15, h = lane >> 5, r32 = lane & 31, rh = w >> 1, hp = w & 1;
    const unsigned xa = rh * 20480 + r32 * 128 + ((h ^ ((r32 >> 1) & 7)) << 4);
    const unsigned wa = 4096 + (8 * h + (c16 >> 2)) * 128 + ((hp ^ (c16 >> 3)) << 6) + (g & 1) * 32 + 8 * (lane & 3);
    f32x16 acc[2][3];
#pragma unroll
    for (int i = 0; i < 2; ++i)
#pragma unroll
        for (int j = 0; j < 3; ++j) { acc[i][j] = 0.f; PIN(acc[i][j]); }
    const unsigned pg = WS_PART + hg * PART_STRIDE + hp * 1024;
    unsigned red = A_RED + hp * 4096 + lane * 16;
    unsigned pst = pg + rp * 8 * PART_STRIDE + lane * 16;
    unsigned pz = c16 < 4 ? pg + rp * 8 * PART_STRIDE + 2048 - hp * 768 + g * 64 + c16 * 16 : WS_END;
    unsigned pk0 = pg + rh * 64 * PART_STRIDE + lane * 16, pz0 = pg + rh * 64 * PART_STRIDE + 2048 - hp * 768 + (g ^ 1) * 64 + (lane & 3) * 16;
    unsigned ob = WS_O + ((rp * 8 + rh * 4 + (g & 1)) * 16 + hg * 2 + hp) * 1024 + (h * 16 + c16) * 16;
    u32x4 tg = tag_bits(ep, lane & 3), sel;
#pragma unroll
    for (int k = 0; k < 4; ++k)
        sel[k] = (r32 < 16 && ((r32 >> 2) & 1) == h && ((r32 >> 3) * 2 + ((r32 >> 1) & 1)) == k) ? ((r32 & 1) ? 0x3F800000u : 0x00003F80u) : 0u;
    PIN(red); PIN(pst); PIN(pz); PIN(pk0); PIN(pz0); PIN(ob); PIN(tg); PIN(sel);
    Frag f0, f1;
    BAR();
    frag_read(f0, xa, wa);
#pragma unroll 1
    for (int s = 0; s < 7; ++s) {
        const unsigned slot = (s & 3) * SLOT, nslot = ((s + 1) & 3) * SLOT;
        SCHED_FENCE();
        frag_read(f1, slot + (xa ^ 64), slot + wa + 20480); frag_mma(f0, acc); interleave();
        SCHED_FENCE(); BAR();
        frag_read(f0, nslot + xa, nslot + wa); frag_mma(f1, acc); interleave();
    }
    SCHED_FENCE();
    frag_read(f1, 3 * SLOT + (xa ^ 64), 3 * SLOT + wa + 20480); frag_mma(f0, acc); interleave();
    SCHED_FENCE();
    frag_mma(f1, acc);
    STAMP(1);
    {
        f32x16 kv = 0.f, zz = 0.f;
        const u32x4 ones = {0x3F803F80u, 0x3F803F80u, 0x3F803F80u, 0x3F803F80u};
#pragma unroll
        for (int i = 0; i < 2; ++i)
#pragma unroll
            for (int u = 0; u < 2; ++u) {
                const u32x4 A = cat(phi4(q4(acc[i][0], 2 * u)), phi4(q4(acc[i][0], 2 * u + 1))), B = cat(pkv(q4(acc[i][1], 2 * u)), pkv(q4(acc[i][1], 2 * u + 1)));
                kv = mma32(A, B, kv); zz = mma32(A, ones, zz);
            }
        f32x4 k0 = (g & 1) ? q4(kv, 2) : q4(kv, 0), k1 = (g & 1) ? q4(kv, 3) : q4(kv, 1);
        f32x4 z0 = (g & 1) ? q4(zz, 2) : q4(zz, 0), z1 = (g & 1) ? q4(zz, 3) : q4(zz, 1);
        if (rh) { lwf128(red, k0); lwf128(red + 1024, k1); lwf128(red + 2048, z0); lwf128(red + 3072, z1); }
        BAR();
        if (!rh) {
            k0 += lrf128(red); k1 += lrf128(red + 1024); z0 += lrf128(red + 2048); z1 += lrf128(red + 3072);
            st128(rws, pst, catt(k0, k1, tg)); st128(rws, pz, catt(z0, z1, tg));
        }
    }
    STAMP(2);
    SCHED_FENCE();
    u32x4 pq[2][2];
#pragma unroll
    for (int i = 0; i < 2; ++i)
#pragma unroll
        for (int b = 0; b < 2; ++b) { pq[i][b] = cat(phi4(q4(acc[i][2], 2 * b)), phi4(q4(acc[i][2], 2 * b + 1))); PIN(pq[i][b]); }
    VMWAIT(0);
    BAR();
    STAMP(3);
    f32x16 sk, sz;
#pragma unroll 1
    for (int it = 0; it < SPIN_CAP; ++it) {
        FENCE();
        u32x4 dk[8], dz[8];
#pragma unroll
        for (int j = 0; j < 8; ++j) { dk[j] = ld128s(rws, pk0, j * 8 * PART_STRIDE); dz[j] = ld128s(rws, pz0, j * 8 * PART_STRIDE); }
        unsigned m = 0;
        sk = 0.f; sz = 0.f;
#pragma unroll
        for (int j = 0; j < 8; ++j) {
            const u32x4 a = dk[j] ^ tg, c = dz[j] ^ tg;
            sk = mma32(sel, a, sk); sz = mma32(sel, c, sz);
            m = or4(or4(m, a), c);
        }
        if (!stale(m)) break;
        __builtin_amdgcn_s_sleep(1);
    }
    const unsigned ex = red + 8192 * rh, ey = red + 8192 * (rh ^ 1);
    lwf128(ex, q4(sk, 0)); lwf128(ex + 1024, q4(sk, 1)); lwf128(ex + 2048, q4(sz, 0)); lwf128(ex + 3072, q4(sz, 1));
    BAR();
    STAMP(4);
    const u32x4 KV = cat(pkv(q4(sk, 0) + lrf128(ey)), pkv(q4(sk, 1) + lrf128(ey + 1024)));
    const u32x4 ZZ = cat(pkv(q4(sz, 0) + lrf128(ey + 2048)), pkv(q4(sz, 1) + lrf128(ey + 3072)));
#pragma unroll
    for (int b = 0; b < 2; ++b) {
        const u32x4 A = ((g & 1) == b) ? KV : ZZ;
#pragma unroll
        for (int i = 0; i < 2; ++i) {
            const f32x16 nd = mma32(A, pq[i][b], 0.f);
            const float ri = TAG_GAIN * __builtin_amdgcn_rcpf(nd[b ? 0 : 8] + 1e-6f);
            st128(rws, ob + i * 32768 + b * 512, catt(q4(nd, 2 * b) * ri, q4(nd, 2 * b + 1) * ri, tg));
        }
    }
    STAMP(5);
}

constexpr unsigned L_U = 98304;
constexpr unsigned L_S = 65536;
constexpr unsigned L_W = L_U + 19200;
constexpr unsigned L_SP = L_W + 20480;
constexpr unsigned L_C = L_SP + 512;
constexpr unsigned L_PW = L_C + 10240;
constexpr unsigned L_PX = L_PW + 4096;
#define TOUCH 1
struct BIn { u32x4 x0, x1; const char* pf; unsigned po, pd, sink, spv, csink; };
DI void touch_stage(BIn& in) {
    if (TOUCH) asm volatile("global_load_dword %0, %1, %2" : "+v"(in.sink) : "v"(in.po), "s"(in.pf) : "memory");
    in.pf += in.pd;
}
DI void b_loads(const Params& p, BIn& in, int xcd, int li, int tid, int lane, int w) {
    const int g = lane >> 4, c16 = lane & 15, me = xcd * 16 + li, c12 = min(c16, 11);
    in.po = w < 4 ? ((xcd >> 1) * 512 + li * 32 + w * 8 + (lane & 7)) * 1024 + 128
                  : (60 + li * 4 + w) * 3072 + (xcd & 1) * 512 + (c12 >> 2) * 1024 + (c12 & 3) * 128;
    in.pf = w < 4 ? (const char*)p.ax : (const char*)p.wqkv;
    in.pd = w < 4 ? 128 : 64 * 3072;
    in.sink = 0;
    touch_stage(in); touch_stage(in);
    const rsrc_t rb = mkrsrc(p.bx, 2048 * 512 * 2);
    const unsigned vo = (256 * w + me) * 1024 + g * 256 + c16 * 4;
    asm volatile("buffer_load_dword %0, %8, %9, 0 offen nt\n\tbuffer_load_dword %1, %8, %9, 0 offen offset:64 nt\n\t"
                 "buffer_load_dword %2, %8, %9, 0 offen offset:128 nt\n\tbuffer_load_dword %3, %8, %9, 0 offen offset:192 nt\n\t"
                 "buffer_load_dword %4, %8, %9, %10 offen nt\n\tbuffer_load_dword %5, %8, %9, %10 offen offset:64 nt\n\t"
                 "buffer_load_dword %6, %8, %9, %10 offen offset:128 nt\n\tbuffer_load_dword %7, %8, %9, %10 offen offset:192 nt"
                 : "=&v"(in.x0[0]), "=&v"(in.x0[1]), "=&v"(in.x0[2]), "=&v"(in.x0[3]), "=&v"(in.x1[0]), "=&v"(in.x1[1]), "=&v"(in.x1[2]), "=&v"(in.x1[3])
                 : "v"(vo), "s"(rb), "s"(131072u) : "memory");
    touch_stage(in);
}
struct BMid { Mat F; Cx tw1; u32x4 tl; };
DI void b_first(const Params& p, rsrc_t rws, unsigned& ep, BIn& in, BMid& mid, int xcd, int li, int tid, int lane, int w, int ct STP_PARAM) {
    const int g = lane >> 4, c16 = lane & 15;
    const int me = xcd * 16 + li;
    const int j1 = (li >> 3) ? (xcd ? 16 - xcd : 8) : xcd, kb = li & 7;
    const int r8 = lane >> 3, ch = lane & 7, jm = (16 - c16) & 15;
    u32x4 &x0 = in.x0, &x1 = in.x1, wl0, wl1;
    unsigned tb1 = WS_T1 + me * TB + w * 2048 + lane * 16;
    {
    const int v = min(tid >> 4, 5), ks = c16 + 16 * me;
    const unsigned short* sp = ((v & 1) ? p.wi : p.wr) + (v < 2 ? ((2048 - ks) & 2047) * 257 : ks * 257 + (v < 4 ? 256 : 128));
    if (w < 2) asm volatile("global_load_ushort %0, %1, off" : "=v"(in.spv) : "v"(sp) : "memory");
    }
    touch_stage(in); touch_stage(in);
    const bool special = (kb == 0 && w == 0);
    if (w < 5) {
        const int k = w, k4 = 4 * k, sU = (0x04004 >> k4) & 15, sQ = (0x40040 >> k4) & 15;
        const int P = (((c16 & ((0x7F7FF >> k4) & 15)) << sU) + (sU ? j1 : 0)) << ((0x00833 >> k4) & 15);
        const int n0 = P * (((4 * (g & ((0x31133 >> k4) & 15))) << sQ) + (sQ ? j1 : 0)), dn = P << sQ;
        const Cx t = angles(n0, dn, 2047, 1.f / 2048);
        Mat m = tomat(t);
        if (k > 2) m = Mat{__builtin_bit_cast(u32x4, t.re), __builtin_bit_cast(u32x4, t.im)};
        if (k == 2 && (g >> 1) != (c16 >> 3)) m = Mat{u32x4{0, 0, 0, 0}, u32x4{0, 0, 0, 0}};
        lw128(L_C + k * 2048 + lane * 16, m.re); lw128(L_C + k * 2048 + 1024 + lane * 16, m.im);
    }
    Mat F = tomat(angles(4 * g * c16, c16, 15, 1.f / 16));
    Cx tw1 = angles(4 * g * c16, c16, 255, 1.f / 256);
    const int km0 = 16 * w + c16, km1 = km0 ? 256 - km0 : 128;
    unsigned ua = L_U + (2 * w) * 1072 + g * 272 + c16 * 16;
    unsigned a0 = L_U + 4 * g * 1072 + ((km0 >> 2) & 3) * 272 + (km0 >> 4) * 16 + (km0 & 3) * 4;
    unsigned a1s = L_U + 4 * g * 1072 + ((km1 >> 2) & 3) * 272 + (km1 >> 4) * 16 + (km1 & 3) * 4;
    unsigned sw0 = L_S + w * 2304 + c16 * 144 + g * 16, sw1 = L_S + w * 2304 + jm * 144 + 64 + g * 16;
    unsigned sr0 = L_S + w * 2304 + r8 * 144 + ch * 16;
    unsigned wst = L_W + (w >> 2) * 5120 + ((tid >> 1) & 127) * 40 + (tid & 1) * 16;
    const int lam = tid >> 3, s2a = (lam >> 5) * 32 + (lam & 3) * 8 + ((lam >> 2) & 7);
    const int t16 = (w >> 2) * 32 + (lane & 8) * 8 + (lane & 3) * 8 + (w & 3) * 2 + ((lane >> 2) & 1);
    unsigned sb = L_U + ((ch & 3) * 2 + (ch >> 2)) * 1040 + ((lam >> 5) * 16 + ((lam >> 2) & 7)) * 16 + (lam & 3) * 4;
    unsigned tb2 = WS_T1 + s2a * TB + (kb * 16 + j1) * 128 + ch * 16;
    PIN(F.re); PIN(F.im); PIN(tw1.re); PIN(tw1.im); PIN(ua); PIN(a0); PIN(a1s); PIN(sw0); PIN(sw1); PIN(sr0); PIN(tb1); PIN(wst); PIN(sb); PIN(tb2);
    epoch_get(ep);
    u32x4 tg = tag_bits(ep, g), tl = tag_bits(ep, lane & 3);
    PIN(tg); PIN(tl);
    const rsrc_t ro = mkrsrc(p.wo, 512 * 512 * 2);
    const int ks = j1 + 16 * ((tid >> 1) & 127), hf = (tid & 1) * 8;
    unsigned vo = (w * 64 + r8) * 1024 + ct * 128 + ((ch ^ (r8 & 6)) << 4);
    unsigned wo0 = (ks * 257 + 16 * kb + hf) * 2, wo1 = (((2048 - ks) & 2047) * 257 + 241 - 16 * kb + hf) * 2;
    PIN(vo); PIN(wo0); PIN(wo1);
    touch_stage(in); touch_stage(in);
    asm volatile("global_load_dwordx4 %0, %2, %4 nt\n\tglobal_load_dwordx4 %1, %3, %4 nt" : "=&v"(wl0), "=&v"(wl1)
                 : "v"(wo0), "v"(wo1), "s"(w >= 4 ? p.wi : p.wr) : "memory");
    asm volatile("s_waitcnt vmcnt(%c4)" : "+v"(x0), "+v"(x1), "+v"(in.spv), "+v"(in.csink) : "i"(TOUCH ? 6 : 2) : "memory");
    STAMP(1);
    if (w < 2) { const int v = min(tid >> 4, 5); *LP(unsigned short, L_PX + c16 * 32 + 4 + 2 * v + (v & 4)) = (unsigned short)in.spv; }
    lw128(ua, pk4(dftA(twpk(dftA(x0, F), tw1), F))); lw128(ua + 1072, pk4(dftA(twpk(dftA(x1, F), tw1), F)));
    BAR();
    {
        const u32x4 d0 = {lr32(a0), lr32(a0 + 1072), lr32(a0 + 2144), lr32(a0 + 3216)};
        const u32x4 d1 = {lr32(a1s), lr32(a1s + 1072), lr32(a1s + 2144), lr32(a1s + 3216)};
        u32x4 pkt = lr128(L_PX + jm * 32 + (g & 1) * 16);
        const Cx c0 = dftA(d0, F), c1 = dftA(d1, F);
        lw128(sw0, zipt(c0.re, c0.im, tg)); lw128(sw1, zipt(c1.re, -c1.im, tg));
        if (g == 0) pkt[0] = pk(c0.re[0], c0.im[0]) & ~TM;
        pkt[3] = ep;
        const u32x4 s0 = lr128(sr0), s1 = lr128(sr0 + 8 * 144);
        st128(rws, tb1, s0); st128(rws, tb1 + 1024, s1);
        if (tid < 32) st128(rws, WS_T1 + me * TB + 16384 + jm * 32 + g * 16, pkt);
    }
    STAMP(2);
    asm volatile("s_waitcnt vmcnt(0)" : "+v"(wl0), "+v"(wl1), "+v"(in.sink) :: "memory");
    lw64(wst, u32x2{wl0[0], wl0[1]}); lw64(wst + 8, u32x2{wl0[2], wl0[3]});
    lw64(wst + 10240, u32x2{wl1[0], wl1[1]}); lw64(wst + 10248, u32x2{wl1[2], wl1[3]});
    BAR();
    STAMP(3);
    u32x4 i0 = ld128(rws, tb2), i1 = ld128s(rws, tb2, 64 * TB), isp = {0, 0, 0, ep};
    const unsigned tb4 = WS_T1 + t16 * TB + 16384 + j1 * 32 + (g & 1) * 16;
    if (kb == 0) isp = ld128(rws, tb4);
    FENCE();
    Cx a1, bz, a2, bv;
    {
        const int col = 2 * w + (g >> 1);
        const float th = (float)(16 * kb + col) * (1.f / 512), sn = __builtin_amdgcn_sinf(th), cs = __builtin_amdgcn_cosf(th);
        unsigned wp = L_W + (c16 + 64 * (g & 1)) * 40 + col * 2, wq = wp + 10240 + 30 - col * 4;
        PIN(wp); PIN(wq);
        f32x4 pr, pi, qr, qi;
#pragma unroll
        for (int r = 0; r < 4; ++r) { pr[r] = lrbf(wp + r * 640); pi[r] = lrbf(wp + r * 640 + 5120); qr[r] = lrbf(wq + r * 640); qi[r] = lrbf(wq + r * 640 + 5120); }
        const float sc = TAG_GAIN * TAG_GAIN / 1048576;
        const f32x4 sr = (pr + qr) * sc, si = (pi - qi) * sc, dr = (pr - qr) * sc, di = (pi + qi) * sc;
        a1.re = sr - dr * sn; a1.im = si - di * sn; a2.re = sr + dr * sn; a2.im = si + di * sn;
        bz.re = dr * cs; bz.im = di * cs;
    }
    unsigned ca = L_C + lane * 16;
    PIN(ca);
    Mat F1 = {lr128(ca), lr128(ca + 1024)}, F2 = {lr128(ca + 2048), lr128(ca + 3072)}, G = {lr128(ca + 4096), lr128(ca + 5120)};
    Cx twa = {lrf128(ca + 6144), lrf128(ca + 7168)}, twb = {lrf128(ca + 8192), lrf128(ca + 9216)};
    unsigned ta = L_U + ((w >> 1) * 2 + 8 * (w & 1)) * 1040 + lane * 16;
    unsigned tb3 = WS_T2 + (kb * 16 + j1) * TB + s2a * 128 + ch * 16;
    const u32x4 tw = tag_bits(ep, w >> 1);
    unsigned t2 = (w & 1) ? ((lane & 8) ? tw[3] : tw[2]) : ((lane & 8) ? tw[1] : tw[0]);
    PIN(t2);
    PIN(a1.re); PIN(a1.im); PIN(a2.re); PIN(a2.im); PIN(bz.re); PIN(bz.im);
    PIN(F1.re); PIN(F1.im); PIN(F2.re); PIN(F2.im); PIN(G.re); PIN(G.im); PIN(twa.re); PIN(twa.im); PIN(twb.re); PIN(twb.im);
    PIN(ta); PIN(tb3);
    i0 ^= tl; i1 ^= tl;
#pragma unroll 1
    for (int it = 0; it < SPIN_CAP && (stale(or4(or4(0, i0), i1)) || __any(isp[3] != ep)); ++it) {
        __builtin_amdgcn_s_sleep(2);
        FENCE();
        i0 = ld128(rws, tb2) ^ tl; i1 = ld128s(rws, tb2, 64 * TB) ^ tl;
        if (kb == 0) isp = ld128(rws, tb4);
    }
    STAMP(10);
    lw32(sb, i0[0]); lw32(sb + 128, i0[1]); lw32(sb + 8320, i0[2]); lw32(sb + 8320 + 128, i0[3]);
    lw32(sb + 512, i1[0]); lw32(sb + 512 + 128, i1[1]); lw32(sb + 512 + 8320, i1[2]); lw32(sb + 512 + 8320 + 128, i1[3]);
    if (kb == 0) { if (g == 0) lw32(L_SP + ((t16 >> 5) * 8 + (t16 & 7)) * 16 + ((t16 >> 3) & 3) * 4, isp[0]); lw128(L_PW + t16 * 32 + (g & 1) * 16, isp); }
    BAR();
    STAMP(4);
    {
        const u32x4 zi = lr128(ta), vi = lr128(ta + 1040);
        auto wo2 = [&](int q) { dma(ro, vo, q * 8192, w * 8192 + q * 1024); dma(ro, vo, q * 8192 + 8192, w * 8192 + q * 1024 + 1024); };
        wo2(0);
        u32x4 zt = twpk(dftA(zi, F1), twa), vt = twpk(dftA(vi, F1), twa);
        PIN(zt); PIN(vt);
        wo2(2);
        const Cx Z = dftB(zt, G), V = dftB(vt, G);
        Cx Vz = V;
        bv = bz;
        if (special) {
            u32x4 v0 = lr128(L_SP + (g * 8 + (c16 & 7)) * 16) ^ 0x80000000u;
            if (c16 >= 8) v0 = vi;
            Vz = dftB(twpk(dftA(v0, F1), twa), G);
            if (g < 2) {
                const float hs = 0.5f * TAG_GAIN * TAG_GAIN / 1048576;
#pragma unroll
                for (int r = 0; r < 4; ++r) {
                    const u32x4 e = lr128(L_PW + (c16 + 64 * g + 16 * r) * 32);
                    const unsigned er = lr32(L_PW + (c16 + 64 * g + 16 * r) * 32 + 16);
                    const float ar = bf(e[1]), ai = bf(e[1] >> 16), br = bf(e[2]), bi = bf(e[2] >> 16);
                    a1.re[r] = 0.5f * a1.re[r] + hs * (ar + br); a1.im[r] = 0.5f * a1.im[r] + hs * (bi - ai);
                    bz.re[r] = 0.5f * bz.re[r] + hs * (ar - br); bz.im[r] = 0.5f * bz.im[r] - hs * (ai + bi);
                    a2.re[r] = 4.f * hs * bf(er); a2.im[r] = 4.f * hs * bf(er >> 16);
                    bv.re[r] = 0.f; bv.im[r] = 0.f;
                }
            }
        }
        u32x4 zc = zip(a1.re * Z.re - a1.im * Z.im - bz.re * Vz.im - bz.im * Vz.re, bz.im * Vz.im - a1.re * Z.im - a1.im * Z.re - bz.re * Vz.re);
        u32x4 vc = zip(a2.re * V.re - a2.im * V.im + bv.re * Z.im + bv.im * Z.re, bv.re * Z.re - a2.re * V.im - a2.im * V.re - bv.im * Z.im);
        PIN(zc); PIN(vc);
        wo2(4);
        zt = twpk(dftA(zc, G), twb); vt = twpk(dftA(vc, G), twb);
        PIN(zt); PIN(vt);
        wo2(6);
        const Cx zo = dftB(zt, F2), vo2 = dftB(vt, F2);
        lw128(ta, zipt(zo.re, zo.im, u32x4(t2))); lw128(ta + 1040, zipt(vo2.re, -vo2.im, u32x4(t2)));
        BAR();
        const u32x4 o0 = {lr32(sb), lr32(sb + 128), lr32(sb + 8320), lr32(sb + 8320 + 128)};
        const u32x4 o1 = {lr32(sb + 512), lr32(sb + 512 + 128), lr32(sb + 512 + 8320), lr32(sb + 512 + 8320 + 128)};
        st128(rws, tb3, o0); st128(rws, tb3 + 64 * 128, o1);
    }
    STAMP(5);
    VMWAIT(2);
    BAR();
    mid.F = F; mid.tw1 = tw1; mid.tl = tl;
}
DI void b_second(const Params& p, rsrc_t rws, unsigned ep, const BMid& mid, int xcd, int li, int lane, int w, int rt STP_PARAM) {
    const int g = lane >> 4, c16 = lane & 15, ch = lane & 7, jm = (16 - c16) & 15, me = xcd * 16 + li;
    const Mat& F = mid.F; const Cx& tw1 = mid.tw1; const u32x4 tl = mid.tl;
    const rsrc_t rout = mkrsrc(p.out, 2048 * 1024 * 2);
    unsigned ra[4];
#pragma unroll
    for (int i = 0; i < 4; ++i) {
        const int km = 64 * g + 16 * i + c16, kq = km < 128 ? km : (256 - km) & 127;
        ra[i] = L_U + ((km >> 7) * 16 + 2 * w) * 592 + (kq + 16 * (kq >> 6)) * 4;
        PIN(ra[i]);
    }
    const int set = g & 1, k0 = 16 * (2 * w + (g >> 1));
    unsigned oo = (256 * w + me) * 2048 + 1024 + (64 * g + c16) * 4;
    unsigned u2 = L_U + (set * 16 + (set ? jm : c16)) * 592 + (k0 + 16 * (k0 >> 6)) * 4;
    unsigned tb = WS_T2 + ((2 * w + (c16 >> 3)) * 16 + 4 * g) * TB + me * 128 + ch * 16;
    PIN(oo); PIN(u2); PIN(tb);
    VMWAIT(0);
    BAR();
    STAMP(6);
    if (w < 4) {
        u32x4 l0, l1, l2, l3;
#pragma unroll 1
        for (int it = 0; it < SPIN_CAP; ++it) {
            FENCE();
            l0 = ld128(rws, tb) ^ tl; l1 = ld128s(rws, tb, TB) ^ tl; l2 = ld128s(rws, tb, 2 * TB) ^ tl; l3 = ld128s(rws, tb, 3 * TB) ^ tl;
            if (!stale(or4(or4(or4(or4(0, l0), l1), l2), l3))) break;
            __builtin_amdgcn_s_sleep(2);
        }
        STAMP(12);
        u32x4 t[4];
#pragma unroll
        for (int e = 0; e < 4; ++e) t[e] = pk4(dftA(u32x4{l0[e], l1[e], l2[e], l3[e]}, F));
#pragma unroll
        for (int r = 0; r < 4; ++r) lw128(u2 + r * 16, u32x4{t[0][r], t[1][r], t[2][r], t[3][r]});
    }
    BAR();
    STAMP(11);
    {
        const unsigned ts = tag_bits(ep, 0)[1], sa = WS_O + (rt * 64 + lane) * 1024 + 4;
        unsigned sen = ts;
        if (w == 0) sen = ld32(rws, sa);
        const u32x4 d0 = {lr32(ra[0]), lr32(ra[1]), lr32(ra[2]), lr32(ra[3])};
        const u32x4 d1 = {lr32(ra[0] + 592), lr32(ra[1] + 592), lr32(ra[2] + 592), lr32(ra[3] + 592)};
        const u32x4 y0 = pk4c(dftB(twpk(dftA(d0, F), tw1), F)), y1 = pk4c(dftB(twpk(dftA(d1, F), tw1), F));
#pragma unroll
        for (int r = 0; r < 4; ++r) {
            __builtin_amdgcn_raw_buffer_store_b32(y0[r], rout, oo + 64 * r, 0, OUT_AUX);
            __builtin_amdgcn_raw_buffer_store_b32(y1[r], rout, oo + 64 * r, 128 * 2048, OUT_AUX);
        }
        STAMP(13);
#pragma unroll 1
        for (int it = 0; it < SPIN_CAP && stale(sen ^ ts); ++it) { __builtin_amdgcn_s_sleep(6); FENCE(); sen = ld32(rws, sa); }
    }
}

__global__ void __launch_bounds__(512, 2) fused_kernel(Params p) {
    const int b = blockIdx.x, xcd = b & 7, i = b >> 3, li = i & 15;
    const int tid = threadIdx.x, lane = tid & 63, w = __builtin_amdgcn_readfirstlane(tid >> 6);
    BIn in;
    unsigned& csink = in.csink;
    {
        size_t code;
        asm volatile("s_getpc_b64 %0" : "=s"(code));
        asm volatile("global_load_dword %0, %1, %2" : "=v"(csink) : "v"(min((unsigned)(w * 32 + i) * 128u, CODE_BYTES - 128u)), "s"(code & ~(size_t)255) : "memory");
    }
    __shared__ __attribute__((aligned(16))) char smem[LDS_BYTES];
    asm volatile("" :: "s"((unsigned)(size_t)(LAS char*)smem));
    STAMP(0);
    const int rp = (xcd >> 1) * 4 + (li & 3), hg = (xcd & 1) * 4 + (li >> 2), rt = xcd * 4 + (i & 3), ct = i >> 2;
    if (i < 16 && w >= 4) a_loader(p, rp, hg, li, lane, w - 4 STP_ARG);
    FENCE();
    if (i >= 16) b_loads(p, in, xcd, li, tid, lane, w);
    FENCE();
    const rsrc_t rws = mkrsrc(p.ws, WS_END);
    unsigned ep;
    epoch_ask(ep, p.ws);
    BMid mid;
    if (i >= 16) b_first(p, rws, ep, in, mid, xcd, li, tid, lane, w, ct STP_ARG);
    else if (w < 4) { asm volatile("s_waitcnt vmcnt(0)" : "+v"(csink) :: "memory"); epoch_get(ep); a_tile(rws, ep, rp, hg, lane, w STP_ARG); }
    else { PIN(csink); a_loader_tail(p, lane, w - 4, ct STP_ARG); epoch_get(ep); }
    const int g = lane >> 4, c16 = lane & 15, q = c16 >> 2, kh = w >> 2;
    unsigned ob = WS_O + (rt * 4 + (w & 3)) * 16384 + kh * 8192 + lane * 16;
    unsigned wa = kh * 32768 + (16 * (g >> 1) + 4 * (g & 1) + q) * 128 + 8 * (lane & 3) + (((2 * g + (q >> 1)) & 3) << 5);
    const unsigned scr = 81920;
    unsigned rw = scr + kh * 17408 + ((w & 3) * 16 + c16) * 272 + g * 16, rr = scr + (tid >> 3) * 272 + (tid & 7) * 32;
    unsigned oo = (rt * 64 + (tid >> 3)) * 2048 + ct * 128 + (tid & 7) * 16;
    u32x4 tl = tag_bits(ep, lane & 3);
    u32x4 o[8], wf[8][4];
#pragma unroll
    for (int k = 0; k < 8; ++k)
#pragma unroll
        for (int t = 0; t < 4; ++t) { const unsigned a = (wa ^ (t << 5)) + k * 4096; wf[k][t] = ltr2(a, a + 1024); }
    PIN(ob); PIN(rw); PIN(rr); PIN(oo); PIN(tl);
    SCHED_FENCE();
    if (i < 16) VMWAIT(0);
    else b_second(p, rws, ep, mid, xcd, li, lane, w, rt STP_ARG);
    BAR();
    STAMP(7);
#pragma unroll
    for (int k = 0; k < 8; ++k) o[k] = ld128s(rws, ob, k * 1024);
    f32x4 acc[4];
#pragma unroll 1
    for (int it = 0; it < SPIN_CAP; ++it) {
        unsigned m = 0;
#pragma unroll
        for (int t = 0; t < 4; ++t) acc[t] = Z4;
#pragma unroll
        for (int k = 0; k < 8; ++k) {
            const u32x4 ok = o[k] ^ tl;
#pragma unroll
            for (int t = 0; t < 4; ++t) acc[t] = mma(wf[k][t], ok, acc[t]);
            m = or4(m, ok);
        }
        if (!stale(m)) break;
        __builtin_amdgcn_s_sleep(4);
        FENCE();
#pragma unroll
        for (int k = 0; k < 8; ++k) o[k] = ld128s(rws, ob, k * 1024);
    }
    STAMP(8);
#pragma unroll
    for (int t = 0; t < 4; ++t) lwf128(rw + t * 64, acc[t]);
    BAR();
    const f32x4 y0 = lrf128(rr) + lrf128(rr + 17408), y1 = lrf128(rr + 16) + lrf128(rr + 17408 + 16);
    __builtin_amdgcn_raw_buffer_store_b128(cat(pkv(y0), pkv(y1)), mkrsrc(p.out, 2048 * 1024 * 2), oo, 0, OUT_AUX);
    if (b == 128 && tid == 0) st32(rws, WS_EPOCH, ep);
    STAMP(9);
}

extern "C" void kernel_launch(void* const* d_in, const int* in_sizes, int n_in,
                              void* d_out, int out_size, void* d_ws, size_t ws_size,
                              hipStream_t stream) {
    Params p;
    p.ax = (const unsigned short*)d_in[0]; p.wqkv = (const unsigned short*)d_in[1]; p.wo = (const unsigned short*)d_in[2];
    p.bx = (const unsigned short*)d_in[3]; p.wr = (const unsigned short*)d_in[4]; p.wi = (const unsigned short*)d_in[5];
    p.out = (unsigned short*)d_out; p.ws = (unsigned char*)d_ws;
    fused_kernel<<<256, 512, 0, stream>>>(p);
}
```

```cpp
#include <hip/hip_runtime.h>
#include <stdint.h>

typedef short    s16x4 __attribute__((ext_vector_type(4)));
typedef __bf16   bf16x8 __attribute__((ext_vector_type(8)));
typedef __bf16   bf16x2 __attribute__((ext_vector_type(2)));
typedef float    f32x2 __attribute__((ext_vector_type(2)));
typedef float    f32x4 __attribute__((ext_vector_type(4)));
typedef float    f32x16 __attribute__((ext_vector_type(16)));
typedef unsigned u32x2 __attribute__((ext_vector_type(2)));
typedef unsigned u32x4 __attribute__((ext_vector_type(4)));
typedef __amdgpu_buffer_rsrc_t rsrc_t;
#define LAS __attribute__((address_space(3)))
#define DI __device__ __forceinline__

constexpr unsigned WS_EPOCH = 0;
constexpr unsigned PART_STRIDE = 2688, WS_PART = 4096;
constexpr unsigned TB = 17024;
constexpr unsigned WS_T1 = 1u << 19, WS_T2 = WS_T1 + 128 * TB;
constexpr unsigned WS_O = WS_T2 + 128 * TB, WS_END = WS_O + (1u << 21);
constexpr int SPIN_CAP = 1 << 15;
constexpr unsigned LDS_BYTES = 163840;
#define OUT_AUX 16
#define STAMP(i) do {} while (0)
#define STP_PARAM
#define STP_ARG

struct Params {
    const unsigned short *ax, *wqkv, *wo, *bx, *wr, *wi;
    unsigned short* out;
    unsigned char* ws;
};

DI rsrc_t mkrsrc(const void* ptr, unsigned bytes) { return __builtin_amdgcn_make_buffer_rsrc((void*)ptr, 0, bytes, 0x00020000); }
DI u32x4 ld128(rsrc_t r, unsigned off)  { return __builtin_amdgcn_raw_buffer_load_b128(r, off, 0, 16); }
DI u32x2 ld64(rsrc_t r, unsigned off)   { return __builtin_amdgcn_raw_buffer_load_b64(r, off, 0, 16); }
DI unsigned ld32(rsrc_t r, unsigned off){ return __builtin_amdgcn_raw_buffer_load_b32(r, off, 0, 16); }
DI u32x2 ld64s(rsrc_t r, unsigned off, unsigned so)   { return __builtin_amdgcn_raw_buffer_load_b64(r, off, so, 16); }
DI u32x4 ld128s(rsrc_t r, unsigned off, unsigned so)  { return __builtin_amdgcn_raw_buffer_load_b128(r, off, so, 16); }
DI unsigned ld32s(rsrc_t r, unsigned off, unsigned so){ return __builtin_amdgcn_raw_buffer_load_b32(r, off, so, 16); }
DI void st128(rsrc_t r, unsigned off, u32x4 v) { __builtin_amdgcn_raw_buffer_store_b128(v, r, off, 0, 16); }
DI void st64(rsrc_t r, unsigned off, u32x2 v) { __builtin_amdgcn_raw_buffer_store_b64(v, r, off, 0, 16); }
DI void st32(rsrc_t r, unsigned off, unsigned v) { __builtin_amdgcn_raw_buffer_store_b32(v, r, off, 0, 16); }
#define FENCE() asm volatile("" ::: "memory")
#define BAR() asm volatile("s_waitcnt lgkmcnt(0)\n\ts_barrier" ::: "memory")
#define VMWAIT(n) asm volatile("s_waitcnt vmcnt(" #n ")" ::: "memory")
#define PIN(x) asm volatile("" : "+v"(x))
#define SCHED_FENCE() __builtin_amdgcn_sched_barrier(0)
DI void dma(rsrc_t r, unsigned voff, unsigned soff, unsigned lds) {
    asm volatile("s_mov_b32 m0, %0\n\ts_nop 0\n\tbuffer_load_dwordx4 %1, %2, %3 offen lds"
                 :: "s"(lds), "v"(voff), "s"(r), "s"(soff) : "memory");
}
DI void dma4(rsrc_t r, unsigned v0, unsigned v1, unsigned v2, unsigned v3, unsigned soff, unsigned lds) {
    asm volatile("s_mov_b32 m0, %0\n\ts_nop 0\n\tbuffer_load_dwordx4 %1, %5, %6 offen lds\n\tbuffer_load_dwordx4 %2, %5, %6 offen offset:1024 lds\n\t"
                 "buffer_load_dwordx4 %3, %5, %6 offen offset:2048 lds\n\tbuffer_load_dwordx4 %4, %5, %6 offen offset:3072 lds"
                 :: "s"(lds), "v"(v0), "v"(v1), "v"(v2), "v"(v3), "s"(r), "s"(soff) : "memory");
}
DI void dma2(rsrc_t r, unsigned v0, unsigned v1, unsigned soff, unsigned lds) {
    asm volatile("s_mov_b32 m0, %0\n\ts_nop 0\n\tbuffer_load_dwordx4 %1, %3, %4 offen lds\n\tbuffer_load_dwordx4 %2, %3, %4 offen offset:1024 lds"
                 :: "s"(lds), "v"(v0), "v"(v1), "s"(r), "s"(soff) : "memory");
}
DI unsigned pk(float lo, float hi) { f32x2 v = {lo, hi}; return __builtin_bit_cast(unsigned, __builtin_convertvector(v, bf16x2)); }
DI u32x2 pkv(f32x4 v) { return u32x2{pk(v[0], v[1]), pk(v[2], v[3])}; }
DI u32x4 cat(u32x2 a, u32x2 b) { return u32x4{a[0], a[1], b[0], b[1]}; }
DI f32x4 mma(u32x4 a, u32x4 b, f32x4 c) {
    return __builtin_amdgcn_mfma_f32_16x16x32_bf16(__builtin_bit_cast(bf16x8, a), __builtin_bit_cast(bf16x8, b), c, 0, 0, 0); }
DI f32x16 mma32(u32x4 a, u32x4 b, f32x16 c) {
    return __builtin_amdgcn_mfma_f32_32x32x16_bf16(__builtin_bit_cast(bf16x8, a), __builtin_bit_cast(bf16x8, b), c, 0, 0, 0); }
DI f32x4 mma16(u32x2 a, u32x2 b, f32x4 c) {
    return __builtin_amdgcn_mfma_f32_16x16x16bf16_1k(__builtin_bit_cast(s16x4, a), __builtin_bit_cast(s16x4, b), c, 0, 0, 0); }
#define LP(T, a) ((LAS T*)(size_t)(a))
DI u32x4 lr128(unsigned a) { return *LP(const u32x4, a); }
DI f32x4 lrf128(unsigned a) { return *LP(const f32x4, a); }
DI u32x2 lr64(unsigned a) { return *LP(const u32x2, a); }
DI unsigned lr32(unsigned a) { return *LP(const unsigned, a); }
DI float bf(unsigned short v) { return __builtin_bit_cast(float, (unsigned)v << 16); }
DI float lrbf(unsigned a) { return bf(*LP(const unsigned short, a)); }
DI void lw128(unsigned a, u32x4 v) { *LP(u32x4, a) = v; }
DI void lwf128(unsigned a, f32x4 v) { *LP(f32x4, a) = v; }
DI void lw64(unsigned a, u32x2 v) { *LP(u32x2, a) = v; }
DI void lw32(unsigned a, unsigned v) { *LP(unsigned, a) = v; }
DI u32x2 ltr(unsigned a) { return __builtin_bit_cast(u32x2, __builtin_amdgcn_ds_read_tr16_b64_v4i16(LP(s16x4, a))); }
DI u32x4 ltr2(unsigned a, unsigned b) { return cat(ltr(a), ltr(b)); }
constexpr unsigned TM = 0x00010001u;
DI u32x4 tag_bits(unsigned ep, int unit) {
    const unsigned t = ((ep << 2) | 2u) >> (8 * unit);
    u32x4 m;
#pragma unroll
    for (int k = 0; k < 4; ++k) m[k] = ((t >> (2 * k)) & 1u) | (((t >> (2 * k + 1)) & 1u) << 16);
    return m;
}
constexpr float TAG_GAIN = 1.0027f;
DI unsigned pkt1(float a, float b, unsigned bits) { return __builtin_amdgcn_bitop3_b32(pk(a, b), TM, bits, 0xBA); }
DI u32x4 zipt(f32x4 a, f32x4 b, u32x4 t) { return u32x4{pkt1(a[0], b[0], t[0]), pkt1(a[1], b[1], t[1]), pkt1(a[2], b[2], t[2]), pkt1(a[3], b[3], t[3])}; }
DI u32x4 catt(f32x4 a, f32x4 b, u32x4 t) { return u32x4{pkt1(a[0], a[1], t[0]), pkt1(a[2], a[3], t[1]), pkt1(b[0], b[1], t[2]), pkt1(b[2], b[3], t[3])}; }
DI unsigned or4(unsigned m, u32x4 v) { return m | v[0] | v[1] | v[2] | v[3]; }
DI bool stale(unsigned m) { return __any(m & TM); }
DI u32x2 phi4(f32x4 t) {
    f32x4 e, m;
#pragma unroll
    for (int r = 0; r < 4; ++r) {
        e[r] = __builtin_amdgcn_fmed3f(__builtin_amdgcn_exp2f(t[r] * 1.44269504f), 0.f, 1.f);
        m[r] = __builtin_elementwise_maximum(t[r], 0.f);
    }
    return pkv((m + e) + 1e-6f);
}
constexpr f32x4 Z4 = {0.f, 0.f, 0.f, 0.f};

struct Mat { u32x4 re, im; };
struct Cx  { f32x4 re, im; };
DI Cx angles(int n0, int dn, int mask, float inv) {
    Cx t;
#pragma unroll
    for (int r = 0; r < 4; ++r) { float a = (float)((n0 + r * dn) & mask) * inv;
        t.re[r] = __builtin_amdgcn_cosf(a); t.im[r] = __builtin_amdgcn_sinf(a); }
    return t;
}
DI u32x4 zip(f32x4 a, f32x4 b) { return u32x4{pk(a[0], b[0]), pk(a[1], b[1]), pk(a[2], b[2]), pk(a[3], b[3])}; }
DI Mat tomat(Cx t) { return Mat{zip(t.re, t.im), zip(-t.im, t.re)}; }
DI Cx dftA(u32x4 d, const Mat& f) { return Cx{mma(d, f.re, Z4), mma(d, f.im, Z4)}; }
DI Cx dftB(u32x4 d, const Mat& f) { return Cx{mma(f.re, d, Z4), mma(f.im, d, Z4)}; }
DI u32x4 twpk(Cx v, const Cx& w) { return zip(v.re * w.re + v.im * w.im, v.im * w.re - v.re * w.im); }
DI u32x4 pk4(Cx v)  { return zip(v.re, v.im); }
DI u32x4 pk4c(Cx v) { return zip(v.re, v.im) ^ 0x80000000u; }

DI void epoch_ask(unsigned& e, const unsigned char* ws) { asm volatile("s_load_dword %0, %1, 0x0" : "=s"(e) : "s"(ws) : "memory"); }
DI void epoch_get(unsigned& e) {
    unsigned r;
    asm volatile("s_waitcnt lgkmcnt(0)\n\ts_add_u32 %0, %1, 1" : "=s"(r) : "s"(e) : "memory", "scc");
    e = r;
}
DI f32x4 q4(const f32x16& v, int i) { return f32x4{v[4 * i], v[4 * i + 1], v[4 * i + 2], v[4 * i + 3]}; }
struct Frag { u32x4 a[2][2], b[2][3]; };
DI void frag_read(Frag& f, unsigned xa, unsigned wa) {
#pragma unroll
    for (int k = 0; k < 2; ++k) {
        f.a[k][0] = lr128(xa ^ (k * 32)); f.b[k][0] = ltr2(wa + k * 10240, wa + k * 10240 + 512);
        f.a[k][1] = lr128((xa ^ (k * 32)) + 10240);
        f.b[k][1] = ltr2(wa + k * 10240 + 2048, wa + k * 10240 + 2560); f.b[k][2] = ltr2(wa + k * 10240 + 4096, wa + k * 10240 + 4608);
    }
}
DI void frag_mma(const Frag& f, f32x16 (&acc)[2][3]) {
#pragma unroll
    for (int k = 0; k < 2; ++k)
#pragma unroll
        for (int j = 0; j < 3; ++j)
#pragma unroll
            for (int i = 0; i < 2; ++i) acc[i][j] = (j < 2) ? mma32(f.a[k][i], f.b[k][j], acc[i][j]) : mma32(f.b[k][j], f.a[k][i], acc[i][j]);
}
DI void interleave() {
#pragma unroll
    for (int i = 0; i < 8; ++i) { __builtin_amdgcn_sched_group_barrier(0x8, 1, 0); __builtin_amdgcn_sched_group_barrier(0x100, 2, 0); }
    __builtin_amdgcn_sched_group_barrier(0x8, 4, 0);
}
#define DEPTH 2
#define ROTK 0
constexpr unsigned CODE_BYTES = 19456;

constexpr unsigned SLOT = 40960;
constexpr unsigned A_RED = 65536;
DI void a_loader(const Params& p, int rp, int hg, int li, int lane, int lw STP_PARAM) {
    const int rot = ROTK == 0 ? 0 : ROTK == 1 ? 2 * (li >> 2) + 2 * (li & 3) : (li & 1) * 4;
    const int r8 = lane >> 3, ch = lane & 7;
    const rsrc_t rx = mkrsrc(p.ax, 2048 * 512 * 2), rw = mkrsrc(p.wqkv, 512 * 1536 * 2);
    const unsigned vx = (rp * 128 + lw * 32 + r8) * 1024 + ((ch ^ (r8 >> 1)) << 4);
    const unsigned vw = (lw * 16 + r8) * 3072 + hg * 128 + ((ch ^ ((r8 & 2) << 1)) << 4);
    const unsigned vx1 = (vx ^ 64) + 7168, vx2 = vx + 14336, vx3 = (vx ^ 64) + 21504, vk = vw + 1024, vw1 = vw + 24576 - 1024;
#pragma unroll 1
    for (int s = 0; s < 8; ++s) {
        if (s == 0) STAMP(6);
        if (s >= 2) { VMWAIT(10); BAR(); }
        if (s == 2) STAMP(11);
        const unsigned kb = (s + rot) & 7, so = kb * (64 * 3072), l = (s & 3) * SLOT + lw * 10240;
        dma4(rx, vx, vx1, vx2, vx3, kb * 128, l);
        dma4(rw, vk, vw + 24576, vw, vw1, so, l + 4096);
        dma2(rw, vw, vw1, so, l + 8192);
    }
}
DI void a_loader_tail(const Params& p, int lane, int lw, int ct STP_PARAM) {
    const int r8 = lane >> 3, ch = lane & 7;
    const rsrc_t ro = mkrsrc(p.wo, 512 * 512 * 2);
    const unsigned vo = (lw * 64 + r8) * 1024 + ct * 128 + ((ch ^ (r8 & 6)) << 4);
    VMWAIT(10);
#pragma unroll 1
    for (int u = 0; u < 2; ++u) {
        BAR();
#pragma unroll 1
        for (int q = 0; q < 8; ++q) dma(ro, vo, u * 262144 + q * 8192, u * 32768 + lw * 8192 + q * 1024);
        VMWAIT(8);
    }
    STAMP(1);
    BAR(); VMWAIT(0); BAR(); BAR();
}
DI void a_tile(rsrc_t rws, unsigned ep, int rp, int hg, int lane, int w STP_PARAM) {
    const int g = lane >> 4, c16 = lane & 15, h = lane >> 5, r32 = lane & 31, rh = w >> 1, hp = w & 1;
    const unsigned xa = rh * 20480 + r32 * 128 + ((h ^ ((r32 >> 1) & 7)) << 4);
    const unsigned wa = 4096 + (8 * h + (c16 >> 2)) * 128 + ((hp ^ (c16 >> 3)) << 6) + (g & 1) * 32 + 8 * (lane & 3);
    f32x16 acc[2][3];
#pragma unroll
    for (int i = 0; i < 2; ++i)
#pragma unroll
        for (int j = 0; j < 3; ++j) { acc[i][j] = 0.f; PIN(acc[i][j]); }
    const unsigned pg = WS_PART + hg * PART_STRIDE + hp * 1024;
    unsigned red = A_RED + hp * 4096 + lane * 16;
    unsigned pst = pg + rp * 8 * PART_STRIDE + lane * 16;
    unsigned pz = c16 < 4 ? pg + rp * 8 * PART_STRIDE + 2048 - hp * 768 + g * 64 + c16 * 16 : WS_END;
    unsigned pk0 = pg + rh * 64 * PART_STRIDE + lane * 16, pz0 = pg + rh * 64 * PART_STRIDE + 2048 - hp * 768 + (g ^ 1) * 64 + (lane & 3) * 16;
    unsigned ob = WS_O + ((rp * 8 + rh * 4 + (g & 1)) * 16 + hg * 2 + hp) * 1024 + (h * 16 + c16) * 16;
    u32x4 tg = tag_bits(ep, lane & 3), sel;
#pragma unroll
    for (int k = 0; k < 4; ++k)
        sel[k] = (r32 < 16 && ((r32 >> 2) & 1) == h && ((r32 >> 3) * 2 + ((r32 >> 1) & 1)) == k) ? ((r32 & 1) ? 0x3F800000u : 0x00003F80u) : 0u;
    PIN(red); PIN(pst); PIN(pz); PIN(pk0); PIN(pz0); PIN(ob); PIN(tg); PIN(sel);
    Frag f0, f1;
    BAR();
    frag_read(f0, xa, wa);
#pragma unroll 1
    for (int s = 0; s < 7; ++s) {
        const unsigned slot = (s & 3) * SLOT, nslot = ((s + 1) & 3) * SLOT;
        SCHED_FENCE();
        frag_read(f1, slot + (xa ^ 64), slot + wa + 20480); frag_mma(f0, acc); interleave();
        SCHED_FENCE(); BAR();
        frag_read(f0, nslot + xa, nslot + wa); frag_mma(f1, acc); interleave();
    }
    SCHED_FENCE();
    frag_read(f1, 3 * SLOT + (xa ^ 64), 3 * SLOT + wa + 20480); frag_mma(f0, acc); interleave();
    SCHED_FENCE();
    frag_mma(f1, acc);
    STAMP(1);
    {
        f32x16 kv = 0.f, zz = 0.f;
        const u32x4 ones = {0x3F803F80u, 0x3F803F80u, 0x3F803F80u, 0x3F803F80u};
#pragma unroll
        for (int i = 0; i < 2; ++i)
#pragma unroll
            for (int u = 0; u < 2; ++u) {
                const u32x4 A = cat(phi4(q4(acc[i][0], 2 * u)), phi4(q4(acc[i][0], 2 * u + 1))), B = cat(pkv(q4(acc[i][1], 2 * u)), pkv(q4(acc[i][1], 2 * u + 1)));
                kv = mma32(A, B, kv); zz = mma32(A, ones, zz);
            }
        f32x4 k0 = (g & 1) ? q4(kv, 2) : q4(kv, 0), k1 = (g & 1) ? q4(kv, 3) : q4(kv, 1);
        f32x4 z0 = (g & 1) ? q4(zz, 2) : q4(zz, 0), z1 = (g & 1) ? q4(zz, 3) : q4(zz, 1);
        if (rh) { lwf128(red, k0); lwf128(red + 1024, k1); lwf128(red + 2048, z0); lwf128(red + 3072, z1); }
        BAR();
        if (!rh) {
            k0 += lrf128(red); k1 += lrf128(red + 1024); z0 += lrf128(red + 2048); z1 += lrf128(red + 3072);
            st128(rws, pst, catt(k0, k1, tg)); st128(rws, pz, catt(z0, z1, tg));
        }
    }
    STAMP(2);
    SCHED_FENCE();
    u32x4 pq[2][2];
#pragma unroll
    for (int i = 0; i < 2; ++i)
#pragma unroll
        for (int b = 0; b < 2; ++b) { pq[i][b] = cat(phi4(q4(acc[i][2], 2 * b)), phi4(q4(acc[i][2], 2 * b + 1))); PIN(pq[i][b]); }
    VMWAIT(0);
    BAR();
    STAMP(3);
    f32x16 sk, sz;
#pragma unroll 1
    for (int it = 0; it < SPIN_CAP; ++it) {
        FENCE();
        u32x4 dk[8], dz[8];
#pragma unroll
        for (int j = 0; j < 8; ++j) { dk[j] = ld128s(rws, pk0, j * 8 * PART_STRIDE); dz[j] = ld128s(rws, pz0, j * 8 * PART_STRIDE); }
        unsigned m = 0;
        sk = 0.f; sz = 0.f;
#pragma unroll
        for (int j = 0; j < 8; ++j) {
            const u32x4 a = dk[j] ^ tg, c = dz[j] ^ tg;
            sk = mma32(sel, a, sk); sz = mma32(sel, c, sz);
            m = or4(or4(m, a), c);
        }
        if (!stale(m)) break;
        __builtin_amdgcn_s_sleep(1);
    }
    const unsigned ex = red + 8192 * rh, ey = red + 8192 * (rh ^ 1);
    lwf128(ex, q4(sk, 0)); lwf128(ex + 1024, q4(sk, 1)); lwf128(ex + 2048, q4(sz, 0)); lwf128(ex + 3072, q4(sz, 1));
    BAR();
    STAMP(4);
    const u32x4 KV = cat(pkv(q4(sk, 0) + lrf128(ey)), pkv(q4(sk, 1) + lrf128(ey + 1024)));
    const u32x4 ZZ = cat(pkv(q4(sz, 0) + lrf128(ey + 2048)), pkv(q4(sz, 1) + lrf128(ey + 3072)));
#pragma unroll
    for (int b = 0; b < 2; ++b) {
        const u32x4 A = ((g & 1) == b) ? KV : ZZ;
#pragma unroll
        for (int i = 0; i < 2; ++i) {
            const f32x16 nd = mma32(A, pq[i][b], 0.f);
            const float ri = TAG_GAIN * __builtin_amdgcn_rcpf(nd[b ? 0 : 8] + 1e-6f);
            st128(rws, ob + i * 32768 + b * 512, catt(q4(nd, 2 * b) * ri, q4(nd, 2 * b + 1) * ri, tg));
        }
    }
    STAMP(5);
}

constexpr unsigned L_U = 98304;
constexpr unsigned L_S = 65536;
constexpr unsigned L_W = L_U + 19200;
constexpr unsigned L_SP = L_W + 20480;
constexpr unsigned L_C = L_SP + 512;
constexpr unsigned L_PW = L_C + 14336;
constexpr unsigned L_PX = L_PW + 4096;
struct BIn { const char* pf; size_t pm; unsigned po, spv; };
DI void touch(BIn& in, unsigned add) {
    asm volatile("s_mov_b64 exec, %2\n\tglobal_load_dword v255, %0, %1\n\ts_mov_b64 exec, -1" :: "v"(in.po), "s"(in.pf + add), "s"(in.pm) : "memory", "v255");
}
DI void b_loads(const Params& p, BIn& in, int xcd, int li, int tid, int lane, int w) {
    const int g = lane >> 4, c16 = lane & 15, me = xcd * 16 + li;
    asm volatile("s_mov_b32 m0, %0\n\ts_nop 0\n\tbuffer_load_dwordx4 %1, %3, 0 offen nt lds\n\tbuffer_load_dwordx4 %2, %3, 0 offen offset:1024 nt lds"
                 :: "s"(w * 2048), "v"((256 * w + me) * 1024 + lane * 16), "v"((256 * w + me + 127) * 1024 + lane * 16), "s"(mkrsrc(p.bx, 2048 * 512 * 2)) : "memory");
    if (w < 2) {
        const int v = min(tid >> 4, 5), ks = c16 + 16 * me;
        const unsigned short* sp = ((v & 1) ? p.wi : p.wr) + (v < 2 ? ((2048 - ks) & 2047) * 257 : ks * 257 + (v < 4 ? 256 : 128));
        asm volatile("global_load_ushort %0, %1, off" : "=v"(in.spv) : "v"(sp) : "memory");
    }
    in.pf = (w & 1) ? (const char*)p.ax : (const char*)p.wqkv;
    in.pm = (w & 1) ? 0xFFFFFFFFull : 0x0FFF0FFF0FFF0FFFull;
    in.po = (w & 1) ? ((xcd >> 1) * 512 + li * 32 + (lane & 31)) * 1024 + (1 + (w >> 1)) * 128
                    : (64 + 32 * w + li * 4 + g) * 3072 + (xcd & 1) * 512 + (c16 >> 2) * 1024 + (c16 & 3) * 128;
    if (w < 2) touch(in, 0);
}
struct BMid { Mat F; Cx tw1; u32x4 tl, l[4]; unsigned tb; };
DI void b_first(const Params& p, rsrc_t rws, unsigned& ep, BIn& in, BMid& mid, int xcd, int li, int tid, int lane, int w, int ct STP_PARAM) {
    const int g = lane >> 4, c16 = lane & 15;
    const int me = xcd * 16 + li;
    const int j1 = (li >> 3) ? (xcd ? 16 - xcd : 8) : xcd, kb = li & 7;
    const int r8 = lane >> 3, ch = lane & 7, jm = (16 - c16) & 15;
    const bool special = (kb == 0 && w == 0);
    if ((w >> 1) == 1) touch(in, 0);
    if (w < 7) {
        const int k4 = 4 * w, sU = (0x0040004 >> k4) & 15, sQ = (0x0400040 >> k4) & 15;
        const int P = (((c16 & ((0xF7FF7FF >> k4) & 15)) << sU) + (sU ? j1 : 0)) << ((0x3007833 >> k4) & 15);
        const int n0 = P * (((4 * (g & ((0x3313133 >> k4) & 15))) << sQ) + (sQ ? j1 : 0)), dn = P << sQ;
        const Cx t = angles(n0, dn, 2047, 1.f / 2048);
        Mat m = Mat{__builtin_bit_cast(u32x4, t.re), __builtin_bit_cast(u32x4, t.im)};
        if (w < 4) m = tomat(t);
        if (w == 2 && (g >> 1) != (c16 >> 3)) m = Mat{u32x4{0, 0, 0, 0}, u32x4{0, 0, 0, 0}};
        lw128(L_C + w * 2048 + lane * 16, m.re); lw128(L_C + w * 2048 + 1024 + lane * 16, m.im);
    }
    const int km0 = 16 * w + c16, km1 = km0 ? 256 - km0 : 128, ks = j1 + 16 * ((tid >> 1) & 127), hf = (tid & 1) * 8;
    unsigned ca = L_C + lane * 16, xa = w * 2048 + g * 256 + c16 * 4;
    unsigned ua = L_U + (2 * w) * 1072 + g * 272 + c16 * 16;
    unsigned wo0 = (ks * 257 + 16 * kb + hf) * 2, wo1 = (((2048 - ks) & 2047) * 257 + 241 - 16 * kb + hf) * 2;
    PIN(ca); PIN(xa); PIN(ua); PIN(wo0); PIN(wo1);
    if (w >= 4) touch(in, 0);
    epoch_get(ep);
    BAR();
    Mat F = {lr128(ca + 6144), lr128(ca + 7168)};
    Cx tw1 = {lrf128(ca + 12288), lrf128(ca + 13312)};
    unsigned a0 = L_U + 4 * g * 1072 + ((km0 >> 2) & 3) * 272 + (km0 >> 4) * 16 + (km0 & 3) * 4;
    unsigned a1s = L_U + 4 * g * 1072 + ((km1 >> 2) & 3) * 272 + (km1 >> 4) * 16 + (km1 & 3) * 4;
    unsigned sw0 = L_S + w * 2304 + c16 * 144 + g * 16, sw1 = L_S + w * 2304 + jm * 144 + 64 + g * 16;
    unsigned sr0 = L_S + w * 2304 + r8 * 144 + ch * 16, tb1 = WS_T1 + me * TB + w * 2048 + lane * 16;
    u32x4 tg = tag_bits(ep, g), wl0, wl1;
    PIN(a0); PIN(a1s); PIN(sw0); PIN(sw1); PIN(sr0); PIN(tb1); PIN(tg);
    asm volatile("s_waitcnt vmcnt(1)" : "+v"(in.spv) :: "memory");
    STAMP(1);
    const u32x4 x0 = {lr32(xa), lr32(xa + 64), lr32(xa + 128), lr32(xa + 192)}, x1 = {lr32(xa + 1024), lr32(xa + 1088), lr32(xa + 1152), lr32(xa + 1216)};
    if (w < 2) { const int v = min(tid >> 4, 5); *LP(unsigned short, L_PX + c16 * 32 + 4 + 2 * v + (v & 4)) = (unsigned short)in.spv; }
    lw128(ua, pk4(dftA(twpk(dftA(x0, F), tw1), F))); lw128(ua + 1072, pk4(dftA(twpk(dftA(x1, F), tw1), F)));
    BAR();
    {
        const u32x4 d0 = {lr32(a0), lr32(a0 + 1072), lr32(a0 + 2144), lr32(a0 + 3216)};
        const u32x4 d1 = {lr32(a1s), lr32(a1s + 1072), lr32(a1s + 2144), lr32(a1s + 3216)};
        u32x4 pkt = lr128(L_PX + jm * 32 + (g & 1) * 16);
        const Cx c0 = dftA(d0, F), c1 = dftA(d1, F);
        lw128(sw0, zipt(c0.re, c0.im, tg)); lw128(sw1, zipt(c1.re, -c1.im, tg));
        if (g == 0) pkt[0] = pk(c0.re[0], c0.im[0]) & ~TM;
        pkt[3] = ep;
        const u32x4 s0 = lr128(sr0), s1 = lr128(sr0 + 8 * 144);
        st128(rws, tb1, s0); st128(rws, tb1 + 1024, s1);
        if (tid < 32) st128(rws, WS_T1 + me * TB + 16384 + jm * 32 + g * 16, pkt);
    }
    STAMP(2);
    if (w < 6) touch(in, (w & 1) ? 512 : 256 * 3072);
    else {
        const int kr = ((lane & 8) ? (xcd ? 16 - xcd : 8) : xcd) + 16 * (li * 8 + (lane >> 4));
        asm volatile("s_mov_b64 exec, %2\n\tbuffer_load_dword v255, %0, %1, 0 offen nt\n\tbuffer_load_dword v255, %0, %1, %3 offen nt\n\ts_mov_b64 exec, -1"
                     :: "v"((kr * 514 + (lane & 7) * 128) & ~3u), "s"(mkrsrc(w == 6 ? p.wr : p.wi, 2048 * 514)),
                        "s"(0x1F1F1F1F1F1F1F1Full), "s"(64 * 514) : "memory", "v255");
    }
    const rsrc_t ro = mkrsrc(p.wo, 512 * 512 * 2);
    const int lam = tid >> 3, s2a = (lam >> 5) * 32 + (lam & 3) * 8 + ((lam >> 2) & 7);
    const int t16 = (w >> 2) * 32 + (lane & 8) * 8 + (lane & 3) * 8 + (w & 3) * 2 + ((lane >> 2) & 1);
    unsigned wst = L_W + (w >> 2) * 5120 + ((tid >> 1) & 127) * 40 + (tid & 1) * 16;
    unsigned sb = L_U + ((ch & 3) * 2 + (ch >> 2)) * 1040 + ((lam >> 5) * 16 + ((lam >> 2) & 7)) * 16 + (lam & 3) * 4;
    unsigned tb2 = WS_T1 + s2a * TB + (kb * 16 + j1) * 128 + ch * 16;
    unsigned vo = (w * 64 + r8) * 1024 + ct * 128 + ((ch ^ (r8 & 6)) << 4);
    u32x4 tl = tag_bits(ep, lane & 3);
    PIN(wst); PIN(sb); PIN(tb2); PIN(vo); PIN(tl);
    const unsigned tb4 = WS_T1 + t16 * TB + 16384 + j1 * 32 + (g & 1) * 16;
    if (w < 6) VMWAIT(1); else VMWAIT(2);
    BAR();
    STAMP(3);
    u32x4 i0 = ld128(rws, tb2) ^ tl, i1 = ld128s(rws, tb2, 64 * TB) ^ tl, isp = {0, 0, 0, ep};
    if (kb == 0) isp = ld128(rws, tb4);
    PIN(i0); PIN(i1); PIN(isp);
    asm volatile("global_load_dwordx4 %0, %2, %4\n\tglobal_load_dwordx4 %1, %3, %4" : "=&v"(wl0), "=&v"(wl1)
                 : "v"(wo0), "v"(wo1), "s"(w >= 4 ? p.wi : p.wr) : "memory");
    asm volatile("s_mov_b64 exec, 0xFFFF\n\tglobal_load_dword v255, %0, %1\n\ts_mov_b64 exec, -1" :: "v"(((li & 3) * 128 + w * 16 + c16) * 1024 + ct * 128), "s"(p.wo) : "memory", "v255");
#pragma unroll 1
    for (int it = 0; it < SPIN_CAP && (stale(or4(or4(0, i0), i1)) || __any(isp[3] != ep)); ++it) {
        __builtin_amdgcn_s_sleep(2);
        FENCE();
        i0 = ld128(rws, tb2) ^ tl; i1 = ld128s(rws, tb2, 64 * TB) ^ tl;
        if (kb == 0) isp = ld128(rws, tb4);
    }
    STAMP(10);
    lw32(sb, i0[0]); lw32(sb + 128, i0[1]); lw32(sb + 8320, i0[2]); lw32(sb + 8320 + 128, i0[3]);
    lw32(sb + 512, i1[0]); lw32(sb + 512 + 128, i1[1]); lw32(sb + 512 + 8320, i1[2]); lw32(sb + 512 + 8320 + 128, i1[3]);
    if (kb == 0) { if (g == 0) lw32(L_SP + ((t16 >> 5) * 8 + (t16 & 7)) * 16 + ((t16 >> 3) & 3) * 4, isp[0]); lw128(L_PW + t16 * 32 + (g & 1) * 16, isp); }
    Mat F1 = {lr128(ca), lr128(ca + 1024)}, F2 = {lr128(ca + 2048), lr128(ca + 3072)}, G = {lr128(ca + 4096), lr128(ca + 5120)};
    Cx twa = {lrf128(ca + 8192), lrf128(ca + 9216)}, twb = {lrf128(ca + 10240), lrf128(ca + 11264)};
    unsigned ta = L_U + ((w >> 1) * 2 + 8 * (w & 1)) * 1040 + lane * 16;
    unsigned tb3 = WS_T2 + (kb * 16 + j1) * TB + s2a * 128 + ch * 16;
    const u32x4 tw = tag_bits(ep, w >> 1);
    unsigned t2 = (w & 1) ? ((lane & 8) ? tw[3] : tw[2]) : ((lane & 8) ? tw[1] : tw[0]);
    const int col = 2 * w + (g >> 1);
    const float th = (float)(16 * kb + col) * (1.f / 512);
    float sn = __builtin_amdgcn_sinf(th), cs = __builtin_amdgcn_cosf(th);
    unsigned wp = L_W + (c16 + 64 * (g & 1)) * 40 + col * 2, wq = wp + 10240 + 30 - col * 4;
    PIN(F1.re); PIN(F1.im); PIN(F2.re); PIN(F2.im); PIN(G.re); PIN(G.im); PIN(twa.re); PIN(twa.im); PIN(twb.re); PIN(twb.im);
    PIN(ta); PIN(tb3); PIN(t2); PIN(sn); PIN(cs); PIN(wp); PIN(wq);
    BAR();
    STAMP(4);
    auto wo2 = [&](int q) { dma(ro, vo, q * 8192, w * 8192 + q * 1024); dma(ro, vo, q * 8192 + 8192, w * 8192 + q * 1024 + 1024); };
    const u32x4 zi = lr128(ta), vi = lr128(ta + 1040);
    Cx Z = dftB(twpk(dftA(zi, F1), twa), G), V = dftB(twpk(dftA(vi, F1), twa), G);
    PIN(Z.re); PIN(Z.im); PIN(V.re); PIN(V.im);
    asm volatile("s_waitcnt vmcnt(1)" : "+v"(wl0), "+v"(wl1) :: "memory");
    lw64(wst, u32x2{wl0[0], wl0[1]}); lw64(wst + 8, u32x2{wl0[2], wl0[3]});
    lw64(wst + 10240, u32x2{wl1[0], wl1[1]}); lw64(wst + 10248, u32x2{wl1[2], wl1[3]});
    wo2(0);
    BAR();
    STAMP(12);
    Cx a1, bz, a2, bv;
    {
        f32x4 pr, pi, qr, qi;
#pragma unroll
        for (int r = 0; r < 4; ++r) { pr[r] = lrbf(wp + r * 640); pi[r] = lrbf(wp + r * 640 + 5120); qr[r] = lrbf(wq + r * 640); qi[r] = lrbf(wq + r * 640 + 5120); }
        const float sc = TAG_GAIN * TAG_GAIN / 1048576;
        const f32x4 sr = (pr + qr) * sc, si = (pi - qi) * sc, dr = (pr - qr) * sc, di = (pi + qi) * sc;
        a1.re = sr - dr * sn; a1.im = si - di * sn; a2.re = sr + dr * sn; a2.im = si + di * sn;
        bz.re = dr * cs; bz.im = di * cs;
    }
    wo2(2);
    {
        Cx Vz = V;
        bv = bz;
        if (special) {
            u32x4 v0 = lr128(L_SP + (g * 8 + (c16 & 7)) * 16) ^ 0x80000000u;
            if (c16 >= 8) v0 = vi;
            Vz = dftB(twpk(dftA(v0, F1), twa), G);
            if (g < 2) {
                const float hs = 0.5f * TAG_GAIN * TAG_GAIN / 1048576;
#pragma unroll
                for (int r = 0; r < 4; ++r) {
                    const u32x4 e = lr128(L_PW + (c16 + 64 * g + 16 * r) * 32);
                    const unsigned er = lr32(L_PW + (c16 + 64 * g + 16 * r) * 32 + 16);
                    const float ar = bf(e[1]), ai = bf(e[1] >> 16), br = bf(e[2]), bi = bf(e[2] >> 16);
                    a1.re[r] = 0.5f * a1.re[r] + hs * (ar + br); a1.im[r] = 0.5f * a1.im[r] + hs * (bi - ai);
                    bz.re[r] = 0.5f * bz.re[r] + hs * (ar - br); bz.im[r] = 0.5f * bz.im[r] - hs * (ai + bi);
                    a2.re[r] = 4.f * hs * bf(er); a2.im[r] = 4.f * hs * bf(er >> 16);
                    bv.re[r] = 0.f; bv.im[r] = 0.f;
                }
            }
        }
        u32x4 zc = zip(a1.re * Z.re - a1.im * Z.im - bz.re * Vz.im - bz.im * Vz.re, bz.im * Vz.im - a1.re * Z.im - a1.im * Z.re - bz.re * Vz.re);
        u32x4 vc = zip(a2.re * V.re - a2.im * V.im + bv.re * Z.im + bv.im * Z.re, bv.re * Z.re - a2.re * V.im - a2.im * V.re - bv.im * Z.im);
        PIN(zc); PIN(vc);
        wo2(4);
        u32x4 zt = twpk(dftA(zc, G), twb), vt = twpk(dftA(vc, G), twb);
        PIN(zt); PIN(vt);
        wo2(6);
        const Cx zo = dftB(zt, F2), vo2 = dftB(vt, F2);
        lw128(ta, zipt(zo.re, zo.im, u32x4(t2))); lw128(ta + 1040, zipt(vo2.re, -vo2.im, u32x4(t2)));
        BAR();
        const u32x4 o0 = {lr32(sb), lr32(sb + 128), lr32(sb + 8320), lr32(sb + 8320 + 128)};
        const u32x4 o1 = {lr32(sb + 512), lr32(sb + 512 + 128), lr32(sb + 512 + 8320), lr32(sb + 512 + 8320 + 128)};
        st128(rws, tb3, o0); st128(rws, tb3 + 64 * 128, o1);
    }
    STAMP(5);
    unsigned tb = WS_T2 + ((2 * w + (c16 >> 3)) * 16 + 4 * g) * TB + me * 128 + ch * 16;
    PIN(tb);
    VMWAIT(0);
    BAR();
    STAMP(6);
    if (w < 4) { mid.l[0] = ld128(rws, tb); mid.l[1] = ld128s(rws, tb, TB); mid.l[2] = ld128s(rws, tb, 2 * TB); mid.l[3] = ld128s(rws, tb, 3 * TB); }
    FENCE();
    mid.F = F; mid.tw1 = tw1; mid.tl = tl; mid.tb = tb;
}
DI void b_second(const Params& p, rsrc_t rws, unsigned ep, const BMid& mid, int xcd, int li, int lane, int w, int rt STP_PARAM) {
    const int g = lane >> 4, c16 = lane & 15, ch = lane & 7, jm = (16 - c16) & 15, me = xcd * 16 + li;
    const Mat& F = mid.F; const Cx& tw1 = mid.tw1; const u32x4 tl = mid.tl;
    const rsrc_t rout = mkrsrc(p.out, 2048 * 1024 * 2);
    unsigned ra[4];
#pragma unroll
    for (int i = 0; i < 4; ++i) {
        const int km = 64 * g + 16 * i + c16, kq = km < 128 ? km : (256 - km) & 127;
        ra[i] = L_U + ((km >> 7) * 16 + 2 * w) * 592 + (kq + 16 * (kq >> 6)) * 4;
        PIN(ra[i]);
    }
    const int set = g & 1, k0 = 16 * (2 * w + (g >> 1));
    unsigned oo = (256 * w + me) * 2048 + 1024 + (64 * g + c16) * 4;
    unsigned u2 = L_U + (set * 16 + (set ? jm : c16)) * 592 + (k0 + 16 * (k0 >> 6)) * 4;
    PIN(oo); PIN(u2);
    if (w < 4) {
        const unsigned tb = mid.tb;
        u32x4 l0 = mid.l[0] ^ tl, l1 = mid.l[1] ^ tl, l2 = mid.l[2] ^ tl, l3 = mid.l[3] ^ tl;
#pragma unroll 1
        for (int it = 0; it < SPIN_CAP && stale(or4(or4(or4(or4(0, l0), l1), l2), l3)); ++it) {
            __builtin_amdgcn_s_sleep(1);
            FENCE();
            l0 = ld128(rws, tb) ^ tl; l1 = ld128s(rws, tb, TB) ^ tl; l2 = ld128s(rws, tb, 2 * TB) ^ tl; l3 = ld128s(rws, tb, 3 * TB) ^ tl;
        }
        u32x4 t[4];
#pragma unroll
        for (int e = 0; e < 4; ++e) t[e] = pk4(dftA(u32x4{l0[e], l1[e], l2[e], l3[e]}, F));
#pragma unroll
        for (int r = 0; r < 4; ++r) lw128(u2 + r * 16, u32x4{t[0][r], t[1][r], t[2][r], t[3][r]});
    }
    BAR();
    STAMP(11);
    {
        const unsigned ts = tag_bits(ep, 0)[1], sa = WS_O + (rt * 64 + lane) * 1024 + 4;
        unsigned sen = ts;
        if (w == 0) sen = ld32(rws, sa);
        const u32x4 d0 = {lr32(ra[0]), lr32(ra[1]), lr32(ra[2]), lr32(ra[3])};
        const u32x4 d1 = {lr32(ra[0] + 592), lr32(ra[1] + 592), lr32(ra[2] + 592), lr32(ra[3] + 592)};
        const u32x4 y0 = pk4c(dftB(twpk(dftA(d0, F), tw1), F)), y1 = pk4c(dftB(twpk(dftA(d1, F), tw1), F));
#pragma unroll
        for (int r = 0; r < 4; ++r) {
            __builtin_amdgcn_raw_buffer_store_b32(y0[r], rout, oo + 64 * r, 0, OUT_AUX);
            __builtin_amdgcn_raw_buffer_store_b32(y1[r], rout, oo + 64 * r, 128 * 2048, OUT_AUX);
        }
        STAMP(13);
#pragma unroll 1
        for (int it = 0; it < SPIN_CAP && stale(sen ^ ts); ++it) { __builtin_amdgcn_s_sleep(6); FENCE(); sen = ld32(rws, sa); }
    }
}

__global__ void __launch_bounds__(512, 2) fused_kernel(Params p) {
    const int b = blockIdx.x, xcd = b & 7, i = b >> 3, li = i & 15;
    const int tid = threadIdx.x, lane = tid & 63, w = __builtin_amdgcn_readfirstlane(tid >> 6);
    BIn in;
    {
        size_t code;
        asm volatile("s_getpc_b64 %0" : "=s"(code));
        asm volatile("s_mov_b64 exec, 1\n\tglobal_load_dword v255, %0, %1\n\ts_mov_b64 exec, -1" :: "v"(min((unsigned)(w * 32 + i) * 128u, CODE_BYTES - 128u)), "s"(code & ~(size_t)255) : "memory", "v255");
    }
    __shared__ __attribute__((aligned(16))) char smem[LDS_BYTES];
    asm volatile("" :: "s"((unsigned)(size_t)(LAS char*)smem));
    STAMP(0);
    const int rp = (xcd >> 1) * 4 + (li & 3), hg = (xcd & 1) * 4 + (li >> 2), rt = xcd * 4 + (i & 3), ct = i >> 2;
    if (i < 16 && w >= 4) a_loader(p, rp, hg, li, lane, w - 4 STP_ARG);
    FENCE();
    if (i >= 16) b_loads(p, in, xcd, li, tid, lane, w);
    FENCE();
    const rsrc_t rws = mkrsrc(p.ws, WS_END);
    unsigned ep;
    epoch_ask(ep, p.ws);
    BMid mid;
    if (i >= 16) b_first(p, rws, ep, in, mid, xcd, li, tid, lane, w, ct STP_ARG);
    else if (w < 4) { epoch_get(ep); a_tile(rws, ep, rp, hg, lane, w STP_ARG); }
    else { a_loader_tail(p, lane, w - 4, ct STP_ARG); epoch_get(ep); }
    const int g = lane >> 4, c16 = lane & 15, q = c16 >> 2, kh = w >> 2;
    unsigned ob = WS_O + (rt * 4 + (w & 3)) * 16384 + kh * 8192 + lane * 16;
    unsigned wa = kh * 32768 + (16 * (g >> 1) + 4 * (g & 1) + q) * 128 + 8 * (lane & 3) + (((2 * g + (q >> 1)) & 3) << 5);
    const unsigned scr = 81920;
    unsigned rw = scr + kh * 17408 + ((w & 3) * 16 + c16) * 272 + g * 16, rr = scr + (tid >> 3) * 272 + (tid & 7) * 32;
    unsigned oo = (rt * 64 + (tid >> 3)) * 2048 + ct * 128 + (tid & 7) * 16;
    u32x4 tl = tag_bits(ep, lane & 3);
    u32x4 o[8], wf[8][4];
#pragma unroll
    for (int k = 0; k < 8; ++k)
#pragma unroll
        for (int t = 0; t < 4; ++t) { const unsigned a = (wa ^ (t << 5)) + k * 4096; wf[k][t] = ltr2(a, a + 1024); }
    PIN(ob); PIN(rw); PIN(rr); PIN(oo); PIN(tl);
    SCHED_FENCE();
    if (i < 16) VMWAIT(0);
    else b_second(p, rws, ep, mid, xcd, li, lane, w, rt STP_ARG);
    BAR();
    STAMP(7);
#pragma unroll
    for (int k = 0; k < 8; ++k) o[k] = ld128s(rws, ob, k * 1024);
    f32x4 acc[4];
#pragma unroll 1
    for (int it = 0; it < SPIN_CAP; ++it) {
        unsigned m = 0;
#pragma unroll
        for (int t = 0; t < 4; ++t) acc[t] = Z4;
#pragma unroll
        for (int k = 0; k < 8; ++k) {
            const u32x4 ok = o[k] ^ tl;
#pragma unroll
            for (int t = 0; t < 4; ++t) acc[t] = mma(wf[k][t], ok, acc[t]);
            m = or4(m, ok);
        }
        if (!stale(m)) break;
        __builtin_amdgcn_s_sleep(4);
        FENCE();
#pragma unroll
        for (int k = 0; k < 8; ++k) o[k] = ld128s(rws, ob, k * 1024);
    }
    STAMP(8);
#pragma unroll
    for (int t = 0; t < 4; ++t) lwf128(rw + t * 64, acc[t]);
    BAR();
    const f32x4 y0 = lrf128(rr) + lrf128(rr + 17408), y1 = lrf128(rr + 16) + lrf128(rr + 17408 + 16);
    __builtin_amdgcn_raw_buffer_store_b128(cat(pkv(y0), pkv(y1)), mkrsrc(p.out, 2048 * 1024 * 2), oo, 0, OUT_AUX);
    if (b == 128 && tid == 0) st32(rws, WS_EPOCH, ep);
    STAMP(9);
}

extern "C" void kernel_launch(void* const* d_in, const int* in_sizes, int n_in,
                              void* d_out, int out_size, void* d_ws, size_t ws_size,
                              hipStream_t stream) {
    Params p;
    p.ax = (const unsigned short*)d_in[0]; p.wqkv = (const unsigned short*)d_in[1]; p.wo = (const unsigned short*)d_in[2];
    p.bx = (const unsigned short*)d_in[3]; p.wr = (const unsigned short*)d_in[4]; p.wi = (const unsigned short*)d_in[5];
    p.out = (unsigned short*)d_out; p.ws = (unsigned char*)d_ws;
    fused_kernel<<<256, 512, 0, stream>>>(p);
}
```

```cpp
#include <hip/hip_runtime.h>
#include <stdint.h>

typedef short    s16x4 __attribute__((ext_vector_type(4)));
typedef __bf16   bf16x8 __attribute__((ext_vector_type(8)));
typedef __bf16   bf16x2 __attribute__((ext_vector_type(2)));
typedef float    f32x2 __attribute__((ext_vector_type(2)));
typedef float    f32x4 __attribute__((ext_vector_type(4)));
typedef float    f32x16 __attribute__((ext_vector_type(16)));
typedef unsigned u32x2 __attribute__((ext_vector_type(2)));
typedef unsigned u32x4 __attribute__((ext_vector_type(4)));
typedef __amdgpu_buffer_rsrc_t rsrc_t;
#define LAS __attribute__((address_space(3)))
#define DI __device__ __forceinline__

constexpr unsigned WS_EPOCH = 0;
constexpr unsigned PART_STRIDE = 2688, WS_PART = 4096;
constexpr unsigned TB = 17024;
constexpr unsigned WS_T1 = 1u << 19;
constexpr unsigned WS_O = WS_T1 + 128 * TB, WS_END = WS_O + (1u << 21);
constexpr int SPIN_CAP = 1 << 15;
#define RSLEEP 2
#define BACKOFF() do { __builtin_amdgcn_s_sleep(RSLEEP); FENCE(); } while (0)
constexpr unsigned LDS_BYTES = 163840;
#define OUTB_AUX 16
#define OUT_AUX 16
#define STAMP(i) do {} while (0)
#define STP_PARAM
#define STP_ARG

struct Params {
    const unsigned short *ax, *wqkv, *wo, *bx, *wr, *wi;
    unsigned short* out;
    unsigned char* ws;
};

DI rsrc_t mkrsrc(const void* ptr, unsigned bytes) { return __builtin_amdgcn_make_buffer_rsrc((void*)ptr, 0, bytes, 0x00020000); }
DI u32x4 ld128(rsrc_t r, unsigned off)  { return __builtin_amdgcn_raw_buffer_load_b128(r, off, 0, 16); }
DI u32x2 ld64(rsrc_t r, unsigned off)   { return __builtin_amdgcn_raw_buffer_load_b64(r, off, 0, 16); }
DI unsigned ld32(rsrc_t r, unsigned off){ return __builtin_amdgcn_raw_buffer_load_b32(r, off, 0, 16); }
DI u32x2 ld64s(rsrc_t r, unsigned off, unsigned so)   { return __builtin_amdgcn_raw_buffer_load_b64(r, off, so, 16); }
DI u32x4 ld128s(rsrc_t r, unsigned off, unsigned so)  { return __builtin_amdgcn_raw_buffer_load_b128(r, off, so, 16); }
DI unsigned ld32s(rsrc_t r, unsigned off, unsigned so){ return __builtin_amdgcn_raw_buffer_load_b32(r, off, so, 16); }
DI void st128(rsrc_t r, unsigned off, u32x4 v) { __builtin_amdgcn_raw_buffer_store_b128(v, r, off, 0, 16); }
DI void st64(rsrc_t r, unsigned off, u32x2 v) { __builtin_amdgcn_raw_buffer_store_b64(v, r, off, 0, 16); }
DI void st32(rsrc_t r, unsigned off, unsigned v) { __builtin_amdgcn_raw_buffer_store_b32(v, r, off, 0, 16); }
#define FENCE() asm volatile("" ::: "memory")
#define BAR() asm volatile("s_waitcnt lgkmcnt(0)\n\ts_barrier" ::: "memory")
#define VMWAIT(n) asm volatile("s_waitcnt vmcnt(" #n ")" ::: "memory")
#define PIN(x) asm volatile("" : "+v"(x))
#define SCHED_FENCE() __builtin_amdgcn_sched_barrier(0)
DI void dma(rsrc_t r, unsigned voff, unsigned soff, unsigned lds) {
    asm volatile("s_mov_b32 m0, %0\n\ts_nop 0\n\tbuffer_load_dwordx4 %1, %2, %3 offen lds"
                 :: "s"(lds), "v"(voff), "s"(r), "s"(soff) : "memory");
}
DI void dma4(rsrc_t r, unsigned v0, unsigned v1, unsigned v2, unsigned v3, unsigned soff, unsigned lds) {
    asm volatile("s_mov_b32 m0, %0\n\ts_nop 0\n\tbuffer_load_dwordx4 %1, %5, %6 offen lds\n\tbuffer_load_dwordx4 %2, %5, %6 offen offset:1024 lds\n\t"
                 "buffer_load_dwordx4 %3, %5, %6 offen offset:2048 lds\n\tbuffer_load_dwordx4 %4, %5, %6 offen offset:3072 lds"
                 :: "s"(lds), "v"(v0), "v"(v1), "v"(v2), "v"(v3), "s"(r), "s"(soff) : "memory");
}
DI void dma2(rsrc_t r, unsigned v0, unsigned v1, unsigned soff, unsigned lds) {
    asm volatile("s_mov_b32 m0, %0\n\ts_nop 0\n\tbuffer_load_dwordx4 %1, %3, %4 offen lds\n\tbuffer_load_dwordx4 %2, %3, %4 offen offset:1024 lds"
                 :: "s"(lds), "v"(v0), "v"(v1), "s"(r), "s"(soff) : "memory");
}
DI unsigned pk(float lo, float hi) { f32x2 v = {lo, hi}; return __builtin_bit_cast(unsigned, __builtin_convertvector(v, bf16x2)); }
DI u32x2 pkv(f32x4 v) { return u32x2{pk(v[0], v[1]), pk(v[2], v[3])}; }
DI u32x4 cat(u32x2 a, u32x2 b) { return u32x4{a[0], a[1], b[0], b[1]}; }
DI f32x4 mma(u32x4 a, u32x4 b, f32x4 c) {
    return __builtin_amdgcn_mfma_f32_16x16x32_bf16(__builtin_bit_cast(bf16x8, a), __builtin_bit_cast(bf16x8, b), c, 0, 0, 0); }
DI f32x16 mma32(u32x4 a, u32x4 b, f32x16 c) {
    return __builtin_amdgcn_mfma_f32_32x32x16_bf16(__builtin_bit_cast(bf16x8, a), __builtin_bit_cast(bf16x8, b), c, 0, 0, 0); }
DI f32x4 mma16(u32x2 a, u32x2 b, f32x4 c) {
    return __builtin_amdgcn_mfma_f32_16x16x16bf16_1k(__builtin_bit_cast(s16x4, a), __builtin_bit_cast(s16x4, b), c, 0, 0, 0); }
#define LP(T, a) ((LAS T*)(size_t)(a))
DI u32x4 lr128(unsigned a) { return *LP(const u32x4, a); }
DI f32x4 lrf128(unsigned a) { return *LP(const f32x4, a); }
DI u32x2 lr64(unsigned a) { return *LP(const u32x2, a); }
DI unsigned lr32(unsigned a) { return *LP(const unsigned, a); }
DI float bf(unsigned short v) { return __builtin_bit_cast(float, (unsigned)v << 16); }
DI float lrbf(unsigned a) { return bf(*LP(const unsigned short, a)); }
DI void lw128(unsigned a, u32x4 v) { *LP(u32x4, a) = v; }
DI void lwf128(unsigned a, f32x4 v) { *LP(f32x4, a) = v; }
DI void lw64(unsigned a, u32x2 v) { *LP(u32x2, a) = v; }
DI void lw32(unsigned a, unsigned v) { *LP(unsigned, a) = v; }
DI u32x2 ltr(unsigned a) { return __builtin_bit_cast(u32x2, __builtin_amdgcn_ds_read_tr16_b64_v4i16(LP(s16x4, a))); }
DI u32x4 ltr2(unsigned a, unsigned b) { return cat(ltr(a), ltr(b)); }
constexpr unsigned TM = 0x00010001u;
DI u32x4 tag_bits(unsigned ep, int unit, unsigned use = 2) {
    const unsigned t = ((ep << 2) | use) >> (8 * unit);
    u32x4 m;
#pragma unroll
    for (int k = 0; k < 4; ++k) m[k] = ((t >> (2 * k)) & 1u) | (((t >> (2 * k + 1)) & 1u) << 16);
    return m;
}
constexpr float TAG_GAIN = 1.0027f;
DI unsigned pkt1(float a, float b, unsigned bits) { return __builtin_amdgcn_bitop3_b32(pk(a, b), TM, bits, 0xBA); }
DI u32x4 zipt(f32x4 a, f32x4 b, u32x4 t) { return u32x4{pkt1(a[0], b[0], t[0]), pkt1(a[1], b[1], t[1]), pkt1(a[2], b[2], t[2]), pkt1(a[3], b[3], t[3])}; }
DI u32x4 catt(f32x4 a, f32x4 b, u32x4 t) { return u32x4{pkt1(a[0], a[1], t[0]), pkt1(a[2], a[3], t[1]), pkt1(b[0], b[1], t[2]), pkt1(b[2], b[3], t[3])}; }
DI unsigned or4(unsigned m, u32x4 v) { return m | v[0] | v[1] | v[2] | v[3]; }
DI bool stale(unsigned m) { return __any(m & TM); }
DI u32x2 phi4(f32x4 t) {
    f32x4 e, m;
#pragma unroll
    for (int r = 0; r < 4; ++r) {
        e[r] = __builtin_amdgcn_fmed3f(__builtin_amdgcn_exp2f(t[r] * 1.44269504f), 0.f, 1.f);
        m[r] = __builtin_elementwise_maximum(t[r], 0.f);
    }
    return pkv((m + e) + 1e-6f);
}
constexpr f32x4 Z4 = {0.f, 0.f, 0.f, 0.f};

struct Mat { u32x4 re, im; };
struct Cx  { f32x4 re, im; };
DI Cx angles(int n0, int dn, int mask, float inv) {
    Cx t;
#pragma unroll
    for (int r = 0; r < 4; ++r) { float a = (float)((n0 + r * dn) & mask) * inv;
        t.re[r] = __builtin_amdgcn_cosf(a); t.im[r] = __builtin_amdgcn_sinf(a); }
    return t;
}
DI u32x4 zip(f32x4 a, f32x4 b) { return u32x4{pk(a[0], b[0]), pk(a[1], b[1]), pk(a[2], b[2]), pk(a[3], b[3])}; }
DI Mat tomat(Cx t) { return Mat{zip(t.re, t.im), zip(-t.im, t.re)}; }
DI Cx dftA(u32x4 d, const Mat& f) { return Cx{mma(d, f.re, Z4), mma(d, f.im, Z4)}; }
DI Cx dftB(u32x4 d, const Mat& f) { return Cx{mma(f.re, d, Z4), mma(f.im, d, Z4)}; }
DI u32x4 twpk(Cx v, const Cx& w) { return zip(v.re * w.re + v.im * w.im, v.im * w.re - v.re * w.im); }
DI u32x4 pk4(Cx v)  { return zip(v.re, v.im); }
DI u32x4 pk4c(Cx v) { return zip(v.re, v.im) ^ 0x80000000u; }

DI void epoch_ask(unsigned& e, const unsigned char* ws) { asm volatile("s_load_dword %0, %1, 0x0" : "=s"(e) : "s"(ws) : "memory"); }
DI void epoch_get(unsigned& e) {
    unsigned r;
    asm volatile("s_waitcnt lgkmcnt(0)\n\ts_add_u32 %0, %1, 1" : "=s"(r) : "s"(e) : "memory", "scc");
    e = r;
}
DI f32x4 q4(const f32x16& v, int i) { return f32x4{v[4 * i], v[4 * i + 1], v[4 * i + 2], v[4 * i + 3]}; }
struct Frag { u32x4 a[2][2], b[2][3]; };
DI void frag_read(Frag& f, unsigned xa, unsigned wa) {
#pragma unroll
    for (int k = 0; k < 2; ++k) {
        f.a[k][0] = lr128(xa ^ (k * 32)); f.b[k][0] = ltr2(wa + k * 10240, wa + k * 10240 + 512);
        f.a[k][1] = lr128((xa ^ (k * 32)) + 10240);
        f.b[k][1] = ltr2(wa + k * 10240 + 2048, wa + k * 10240 + 2560); f.b[k][2] = ltr2(wa + k * 10240 + 4096, wa + k * 10240 + 4608);
    }
}
DI void frag_mma(const Frag& f, f32x16 (&acc)[2][3]) {
#pragma unroll
    for (int k = 0; k < 2; ++k)
#pragma unroll
        for (int j = 0; j < 3; ++j)
#pragma unroll
            for (int i = 0; i < 2; ++i) acc[i][j] = (j < 2) ? mma32(f.a[k][i], f.b[k][j], acc[i][j]) : mma32(f.b[k][j], f.a[k][i], acc[i][j]);
}
DI void interleave() {
#pragma unroll
    for (int i = 0; i < 8; ++i) { __builtin_amdgcn_sched_group_barrier(0x8, 1, 0); __builtin_amdgcn_sched_group_barrier(0x100, 2, 0); }
    __builtin_amdgcn_sched_group_barrier(0x8, 4, 0);
}
#define DEPTH 2
#define ROTK 0
constexpr unsigned CODE_BYTES = 21760;

constexpr unsigned SLOT = 40960;
constexpr unsigned A_RED = 65536;
DI void a_loader(const Params& p, int rp, int hg, int li, int lane, int lw STP_PARAM) {
    const int rot = ROTK == 0 ? 0 : ROTK == 1 ? 2 * (li >> 2) + 2 * (li & 3) : (li & 1) * 4;
    const int r8 = lane >> 3, ch = lane & 7;
    const rsrc_t rx = mkrsrc(p.ax, 2048 * 512 * 2), rw = mkrsrc(p.wqkv, 512 * 1536 * 2);
    const unsigned vx = (rp * 128 + lw * 32 + r8) * 1024 + ((ch ^ (r8 >> 1)) << 4);
    const unsigned vw = (lw * 16 + r8) * 3072 + hg * 128 + ((ch ^ ((r8 & 2) << 1)) << 4);
    const unsigned vx1 = (vx ^ 64) + 7168, vx2 = vx + 14336, vx3 = (vx ^ 64) + 21504, vk = vw + 1024, vw1 = vw + 24576 - 1024;
#pragma unroll 1
    for (int s = 0; s < 8; ++s) {
        if (s == 0) STAMP(6);
        if (s >= 2) { VMWAIT(10); BAR(); }
        if (s == 2) STAMP(11);
        const unsigned kb = (s + rot) & 7, so = kb * (64 * 3072), l = (s & 3) * SLOT + lw * 10240;
        dma4(rx, vx, vx1, vx2, vx3, kb * 128, l);
        dma4(rw, vk, vw + 24576, vw, vw1, so, l + 4096);
        dma2(rw, vw, vw1, so, l + 8192);
    }
}
DI void a_loader_tail(const Params& p, rsrc_t rws, unsigned& ep, int rp, int hg, int lane, int lw, int ct STP_PARAM) {
    const int r8 = lane >> 3, ch = lane & 7;
    const rsrc_t ro = mkrsrc(p.wo, 512 * 512 * 2);
    const unsigned vo = (lw * 64 + r8) * 1024 + ct * 128 + ((ch ^ (r8 & 6)) << 4);
    VMWAIT(10);
#pragma unroll 1
    for (int u = 0; u < 2; ++u) {
        BAR();
#pragma unroll 1
        for (int q = 0; q < 8; ++q) dma(ro, vo, u * 262144 + q * 8192, u * 32768 + lw * 8192 + q * 1024);
        VMWAIT(8);
    }
    STAMP(1);
    epoch_get(ep);
    {
        const u32x4 nt = tag_bits(ep, lane & 3, 1);
        const unsigned ob = WS_O + ((rp * 8 + lw * 2) * 16 + hg * 2) * 1024 + lane * 16;
        st128(rws, ob, nt); st128(rws, ob + 1024, nt); st128(rws, ob + 16384, nt); st128(rws, ob + 17408, nt);
    }
    BAR(); BAR(); VMWAIT(0); BAR();
}
DI void a_tile(rsrc_t rws, unsigned ep, int rp, int hg, int lane, int w STP_PARAM) {
    const int g = lane >> 4, c16 = lane & 15, h = lane >> 5, r32 = lane & 31, rh = w >> 1, hp = w & 1;
    const unsigned xa = rh * 20480 + r32 * 128 + ((h ^ ((r32 >> 1) & 7)) << 4);
    const unsigned wa = 4096 + (8 * h + (c16 >> 2)) * 128 + ((hp ^ (c16 >> 3)) << 6) + (g & 1) * 32 + 8 * (lane & 3);
    f32x16 acc[2][3];
#pragma unroll
    for (int i = 0; i < 2; ++i)
#pragma unroll
        for (int j = 0; j < 3; ++j) { acc[i][j] = 0.f; PIN(acc[i][j]); }
    const unsigned pg = WS_PART + hg * PART_STRIDE + hp * 1024;
    unsigned red = A_RED + hp * 4096 + lane * 16;
    unsigned pst = pg + rp * 8 * PART_STRIDE + lane * 16;
    unsigned pz = c16 < 4 ? pg + rp * 8 * PART_STRIDE + 2048 - hp * 768 + g * 64 + c16 * 16 : WS_END;
    unsigned pk0 = pg + rh * 64 * PART_STRIDE + lane * 16, pz0 = pg + rh * 64 * PART_STRIDE + 2048 - hp * 768 + (g ^ 1) * 64 + (lane & 3) * 16;
    unsigned ob = WS_O + ((rp * 8 + rh * 4 + (g & 1)) * 16 + hg * 2 + hp) * 1024 + (h * 16 + c16) * 16;
    u32x4 tg = tag_bits(ep, lane & 3), sel;
#pragma unroll
    for (int k = 0; k < 4; ++k)
        sel[k] = (r32 < 16 && ((r32 >> 2) & 1) == h && ((r32 >> 3) * 2 + ((r32 >> 1) & 1)) == k) ? ((r32 & 1) ? 0x3F800000u : 0x00003F80u) : 0u;
    PIN(red); PIN(pst); PIN(pz); PIN(pk0); PIN(pz0); PIN(ob); PIN(tg); PIN(sel);
    Frag f0, f1;
    BAR();
    frag_read(f0, xa, wa);
#pragma unroll 1
    for (int s = 0; s < 7; ++s) {
        const unsigned slot = (s & 3) * SLOT, nslot = ((s + 1) & 3) * SLOT;
        SCHED_FENCE();
        frag_read(f1, slot + (xa ^ 64), slot + wa + 20480); frag_mma(f0, acc); interleave();
        SCHED_FENCE(); BAR();
        frag_read(f0, nslot + xa, nslot + wa); frag_mma(f1, acc); interleave();
    }
    SCHED_FENCE();
    frag_read(f1, 3 * SLOT + (xa ^ 64), 3 * SLOT + wa + 20480); frag_mma(f0, acc); interleave();
    SCHED_FENCE();
    frag_mma(f1, acc);
    STAMP(1);
    {
        f32x16 kv = 0.f, zz = 0.f;
        const u32x4 ones = {0x3F803F80u, 0x3F803F80u, 0x3F803F80u, 0x3F803F80u};
#pragma unroll
        for (int i = 0; i < 2; ++i)
#pragma unroll
            for (int u = 0; u < 2; ++u) {
                const u32x4 A = cat(phi4(q4(acc[i][0], 2 * u)), phi4(q4(acc[i][0], 2 * u + 1))), B = cat(pkv(q4(acc[i][1], 2 * u)), pkv(q4(acc[i][1], 2 * u + 1)));
                kv = mma32(A, B, kv); zz = mma32(A, ones, zz);
            }
        f32x4 k0 = (g & 1) ? q4(kv, 2) : q4(kv, 0), k1 = (g & 1) ? q4(kv, 3) : q4(kv, 1);
        f32x4 z0 = (g & 1) ? q4(zz, 2) : q4(zz, 0), z1 = (g & 1) ? q4(zz, 3) : q4(zz, 1);
        if (rh) { lwf128(red, k0); lwf128(red + 1024, k1); lwf128(red + 2048, z0); lwf128(red + 3072, z1); }
        BAR();
        if (!rh) {
            k0 += lrf128(red); k1 += lrf128(red + 1024); z0 += lrf128(red + 2048); z1 += lrf128(red + 3072);
            st128(rws, pst, catt(k0, k1, tg)); st128(rws, pz, catt(z0, z1, tg));
        }
    }
    STAMP(2);
    SCHED_FENCE();
    u32x4 pq[2][2];
#pragma unroll
    for (int i = 0; i < 2; ++i)
#pragma unroll
        for (int b = 0; b < 2; ++b) { pq[i][b] = cat(phi4(q4(acc[i][2], 2 * b)), phi4(q4(acc[i][2], 2 * b + 1))); PIN(pq[i][b]); }
    VMWAIT(0);
    BAR();
    STAMP(3);
    f32x16 sk, sz;
    u32x4 dk[8], dz[8];
#pragma unroll
    for (int j = 0; j < 8; ++j) dz[j] = tg;
    if (c16 < 4) {
#pragma unroll
        for (int j = 0; j < 8; ++j) dz[j] = ld128s(rws, pz0, j * 8 * PART_STRIDE);
    }
#pragma unroll
    for (int j = 0; j < 8; ++j) dk[j] = ld128s(rws, pk0, j * 8 * PART_STRIDE);
    unsigned m = 0;
    sk = 0.f; sz = 0.f;
#pragma unroll
    for (int j = 0; j < 8; ++j) { dz[j] ^= tg; sz = mma32(sel, dz[j], sz); m = or4(m, dz[j]); SCHED_FENCE(); }
#pragma unroll
    for (int j = 0; j < 8; ++j) { dk[j] ^= tg; sk = mma32(sel, dk[j], sk); m = or4(m, dk[j]); SCHED_FENCE(); }
    if (__builtin_expect(stale(m), 0)) {
#pragma unroll 1
        for (int it = 0; it < SPIN_CAP; ++it) {
            BACKOFF();
            m = 0;
            if (c16 < 4) {
#pragma unroll
                for (int j = 0; j < 8; ++j) dz[j] = ld128s(rws, pz0, j * 8 * PART_STRIDE) ^ tg;
            }
#pragma unroll
            for (int j = 0; j < 8; ++j) dk[j] = ld128s(rws, pk0, j * 8 * PART_STRIDE) ^ tg;
#pragma unroll
            for (int j = 0; j < 8; ++j) m = or4(or4(m, dz[j]), dk[j]);
            if (!stale(m)) break;
        }
        sk = 0.f; sz = 0.f;
#pragma unroll
        for (int j = 0; j < 8; ++j) { sz = mma32(sel, dz[j], sz); sk = mma32(sel, dk[j], sk); }
    }
    const unsigned ex = red + 8192 * rh, ey = red + 8192 * (rh ^ 1);
    lwf128(ex, q4(sk, 0)); lwf128(ex + 1024, q4(sk, 1)); lwf128(ex + 2048, q4(sz, 0)); lwf128(ex + 3072, q4(sz, 1));
    BAR();
    STAMP(4);
    const u32x4 KV = cat(pkv(q4(sk, 0) + lrf128(ey)), pkv(q4(sk, 1) + lrf128(ey + 1024)));
    u32x4 ZZ = cat(pkv(q4(sz, 0) + lrf128(ey + 2048)), pkv(q4(sz, 1) + lrf128(ey + 3072)));
#pragma unroll
    for (int k = 0; k < 4; ++k) ZZ[k] = __builtin_amdgcn_update_dpp(ZZ[k], ZZ[k], 0x114, 0xF, 0x2, false);
#pragma unroll
    for (int b = 0; b < 2; ++b) {
        const u32x4 A = ((g & 1) == b) ? KV : ZZ;
#pragma unroll
        for (int i = 0; i < 2; ++i) {
            const f32x16 nd = mma32(A, pq[i][b], 0.f);
            const float ri = TAG_GAIN * __builtin_amdgcn_rcpf(nd[b ? 0 : 8] + 1e-6f);
            st128(rws, ob + i * 32768 + b * 512, catt(q4(nd, 2 * b) * ri, q4(nd, 2 * b + 1) * ri, tg));
        }
    }
    STAMP(5);
}

constexpr unsigned L_U = 98304;
constexpr unsigned L_S = 65536;
constexpr unsigned L_W = L_U + 19200;
constexpr unsigned L_SP = L_W + 20480;
constexpr unsigned L_C = L_SP + 512;
constexpr unsigned L_PW = L_C + 14336;
constexpr unsigned L_PX = L_PW + 4096;
#define X2POS 0
struct BIn { const char* pf; size_t pm; unsigned po, spv; };
DI void touch(const BIn& in, unsigned add) {
    asm volatile("s_mov_b64 exec, %2\n\tglobal_load_dword v255, %0, %1\n\ts_mov_b64 exec, -1" :: "v"(in.po), "s"(in.pf + add), "s"(in.pm) : "memory", "v255");
}
DI void b_loads(const Params& p, BIn& in, int xcd, int li, int tid, int lane, int w) {
    const int g = lane >> 4, c16 = lane & 15, me = xcd * 16 + li;
    in.pf = (w & 1) ? (const char*)p.ax : (const char*)p.wqkv;
    in.pm = (w & 1) ? 0xFFFFFFFFull : 0x0FFF0FFF0FFF0FFFull;
    in.po = (w & 1) ? ((xcd >> 1) * 512 + li * 32 + (lane & 31)) * 1024 + (1 + (w >> 1)) * 128
                    : (64 + 32 * w + li * 4 + g) * 3072 + (xcd & 1) * 512 + (c16 >> 2) * 1024 + (c16 & 3) * 128;
    if (w < 2) touch(in, 0);
    asm volatile("s_mov_b32 m0, %0\n\ts_nop 0\n\tbuffer_load_dwordx4 %1, %3, 0 offen nt lds\n\tbuffer_load_dwordx4 %2, %3, 0 offen offset:1024 nt lds"
                 :: "s"(w * 2048), "v"((256 * w + me) * 1024 + lane * 16), "v"((256 * w + me + 127) * 1024 + lane * 16), "s"(mkrsrc(p.bx, 2048 * 512 * 2)) : "memory");
    if (w < 2) {
        const int v = min(tid >> 4, 5), ks = c16 + 16 * me;
        const unsigned short* sp = ((v & 1) ? p.wi : p.wr) + (v < 2 ? ((2048 - ks) & 2047) * 257 : ks * 257 + (v < 4 ? 256 : 128));
        asm volatile("global_load_ushort %0, %1, off" : "=v"(in.spv) : "v"(sp) : "memory");
    }
}
struct BMid { Mat F; Cx tw1; u32x4 tl, l[4]; unsigned tb; };
DI void b_first(const Params& p, rsrc_t rws, unsigned& ep, BIn& in, BMid& mid, int xcd, int li, int tid, int lane, int w, int ct STP_PARAM) {
    const int g = lane >> 4, c16 = lane & 15;
    const int me = xcd * 16 + li;
    const int j1 = (li >> 3) ? (xcd ? 16 - xcd : 8) : xcd, kb = li & 7;
    const int r8 = lane >> 3, ch = lane & 7, jm = (16 - c16) & 15;
    const bool special = (kb == 0 && w == 0);
    if ((w >> 1) == 1) touch(in, 0);
    if (w < 7) {
        const int k4 = 4 * w, sU = (0x0040004 >> k4) & 15, sQ = (0x0400040 >> k4) & 15;
        const int P = (((c16 & ((0xF7FF7FF >> k4) & 15)) << sU) + (sU ? j1 : 0)) << ((0x3007833 >> k4) & 15);
        const int n0 = P * (((4 * (g & ((0x3313133 >> k4) & 15))) << sQ) + (sQ ? j1 : 0)), dn = P << sQ;
        const Cx t = angles(n0, dn, 2047, 1.f / 2048);
        Mat m = Mat{__builtin_bit_cast(u32x4, t.re), __builtin_bit_cast(u32x4, t.im)};
        if (w < 4) m = tomat(t);
        if (w == 2 && (g >> 1) != (c16 >> 3)) m = Mat{u32x4{0, 0, 0, 0}, u32x4{0, 0, 0, 0}};
        lw128(L_C + w * 2048 + lane * 16, m.re); lw128(L_C + w * 2048 + 1024 + lane * 16, m.im);
    }
    const int km0 = 16 * w + c16, km1 = km0 ? 256 - km0 : 128, ks = j1 + 16 * ((tid >> 1) & 127), hf = (tid & 1) * 8;
    unsigned ca = L_C + lane * 16, xa = w * 2048 + g * 256 + c16 * 4;
    unsigned ua = L_U + (2 * w) * 1072 + g * 272 + c16 * 16;
    unsigned wo0 = (ks * 257 + 16 * kb + hf) * 2, wo1 = (((2048 - ks) & 2047) * 257 + 241 - 16 * kb + hf) * 2;
    PIN(ca); PIN(xa); PIN(ua); PIN(wo0); PIN(wo1);
    if (w >= 4) touch(in, 0);
    epoch_get(ep);
    BAR();
    Mat F = {lr128(ca + 6144), lr128(ca + 7168)};
    Cx tw1 = {lrf128(ca + 12288), lrf128(ca + 13312)};
    unsigned a0 = L_U + 4 * g * 1072 + ((km0 >> 2) & 3) * 272 + (km0 >> 4) * 16 + (km0 & 3) * 4;
    unsigned a1s = L_U + 4 * g * 1072 + ((km1 >> 2) & 3) * 272 + (km1 >> 4) * 16 + (km1 & 3) * 4;
    unsigned sw0 = L_S + w * 2304 + c16 * 144 + g * 16, sw1 = L_S + w * 2304 + jm * 144 + 64 + g * 16;
    unsigned sr0 = L_S + w * 2304 + r8 * 144 + ch * 16, tb1 = WS_T1 + me * TB + w * 2048 + lane * 16;
    u32x4 tg = tag_bits(ep, g), wl0, wl1;
    PIN(a0); PIN(a1s); PIN(sw0); PIN(sw1); PIN(sr0); PIN(tb1); PIN(tg);
    if (w < 6) touch(in, (w & 1) ? 512 : 256 * 3072);
    asm volatile("global_load_dwordx4 %0, %2, %4 nt\n\tglobal_load_dwordx4 %1, %3, %4 nt" : "=&v"(wl0), "=&v"(wl1)
                 : "v"(wo0), "v"(wo1), "s"(w >= 4 ? p.wi : p.wr) : "memory");
    if (w >= 2 && w < 6) VMWAIT(4); else VMWAIT(3);
    PIN(in.spv);
    STAMP(1);
    const u32x4 x0 = {lr32(xa), lr32(xa + 64), lr32(xa + 128), lr32(xa + 192)}, x1 = {lr32(xa + 1024), lr32(xa + 1088), lr32(xa + 1152), lr32(xa + 1216)};
    if (w < 2) { const int v = min(tid >> 4, 5); *LP(unsigned short, L_PX + c16 * 32 + 4 + 2 * v + (v & 4)) = (unsigned short)in.spv; }
    lw128(ua, pk4(dftA(twpk(dftA(x0, F), tw1), F))); lw128(ua + 1072, pk4(dftA(twpk(dftA(x1, F), tw1), F)));
    BAR();
    {
        const u32x4 d0 = {lr32(a0), lr32(a0 + 1072), lr32(a0 + 2144), lr32(a0 + 3216)};
        const u32x4 d1 = {lr32(a1s), lr32(a1s + 1072), lr32(a1s + 2144), lr32(a1s + 3216)};
        u32x4 pkt = lr128(L_PX + jm * 32 + (g & 1) * 16);
        const Cx c0 = dftA(d0, F), c1 = dftA(d1, F);
        lw128(sw0, zipt(c0.re, c0.im, tg)); lw128(sw1, zipt(c1.re, -c1.im, tg));
        if (g == 0) pkt[0] = pk(c0.re[0], c0.im[0]) & ~TM;
        pkt[3] = ep;
        const u32x4 s0 = lr128(sr0), s1 = lr128(sr0 + 8 * 144);
        st128(rws, tb1, s0); st128(rws, tb1 + 1024, s1);
        if (tid < 32) st128(rws, WS_T1 + me * TB + 16384 + jm * 32 + g * 16, pkt);
    }
    STAMP(2);
    asm volatile("s_mov_b64 exec, 0xFFFF\n\tglobal_load_dword v255, %0, %1\n\ts_mov_b64 exec, -1" :: "v"(((li & 3) * 128 + w * 16 + c16) * 1024 + ct * 128), "s"(p.wo) : "memory", "v255");
    const rsrc_t ro = mkrsrc(p.wo, 512 * 512 * 2);
    const int lam = tid >> 3, s2a = (lam >> 5) * 32 + (lam & 3) * 8 + ((lam >> 2) & 7);
    const int t16 = (w >> 2) * 32 + (lane & 8) * 8 + (lane & 3) * 8 + (w & 3) * 2 + ((lane >> 2) & 1);
    unsigned wst = L_W + (w >> 2) * 5120 + ((tid >> 1) & 127) * 40 + (tid & 1) * 16;
    unsigned sb = L_U + ((ch & 3) * 2 + (ch >> 2)) * 1040 + ((lam >> 5) * 16 + ((lam >> 2) & 7)) * 16 + (lam & 3) * 4;
    unsigned tb2 = WS_T1 + s2a * TB + (kb * 16 + j1) * 128 + ch * 16;
    unsigned vo = (w * 64 + r8) * 1024 + ct * 128 + ((ch ^ (r8 & 6)) << 4);
    u32x4 tl = tag_bits(ep, lane & 3);
    PIN(wst); PIN(sb); PIN(tb2); PIN(vo); PIN(tl);
    const unsigned tb4 = WS_T1 + t16 * TB + 16384 + j1 * 32 + (g & 1) * 16;
    asm volatile("s_waitcnt vmcnt(1)" : "+v"(wl0), "+v"(wl1) :: "memory");
    lw64(wst, u32x2{wl0[0], wl0[1]}); lw64(wst + 8, u32x2{wl0[2], wl0[3]});
    lw64(wst + 10240, u32x2{wl1[0], wl1[1]}); lw64(wst + 10248, u32x2{wl1[2], wl1[3]});
    BAR();
    STAMP(3);
    u32x4 i0 = ld128(rws, tb2), i1 = ld128s(rws, tb2, 64 * TB), isp = {0, 0, 0, ep};
    if (kb == 0) isp = ld128(rws, tb4);
    FENCE();
    Mat F1 = {lr128(ca), lr128(ca + 1024)}, F2 = {lr128(ca + 2048), lr128(ca + 3072)}, G = {lr128(ca + 4096), lr128(ca + 5120)};
    Cx twa = {lrf128(ca + 8192), lrf128(ca + 9216)}, twb = {lrf128(ca + 10240), lrf128(ca + 11264)};
    unsigned ta = L_U + ((w >> 1) * 2 + 8 * (w & 1)) * 1040 + lane * 16;
    const u32x4 tw = tag_bits(ep, w >> 1, 1);
    unsigned t2 = (w & 1) ? ((lane & 8) ? tw[3] : tw[2]) : ((lane & 8) ? tw[1] : tw[0]);
    const int col = 2 * w + (g >> 1);
    const float th = (float)(16 * kb + col) * (1.f / 512);
    float sn = __builtin_amdgcn_sinf(th), cs = __builtin_amdgcn_cosf(th);
    unsigned wp = L_W + (c16 + 64 * (g & 1)) * 40 + col * 2, wq = wp + 10240 + 30 - col * 4;
    PIN(F1.re); PIN(F1.im); PIN(F2.re); PIN(F2.im); PIN(G.re); PIN(G.im); PIN(twa.re); PIN(twa.im); PIN(twb.re); PIN(twb.im);
    PIN(ta); PIN(t2); PIN(sn); PIN(cs); PIN(wp); PIN(wq);
    Cx a1, bz, a2, bv;
    {
        f32x4 pr, pi, qr, qi;
#pragma unroll
        for (int r = 0; r < 4; ++r) { pr[r] = lrbf(wp + r * 640); pi[r] = lrbf(wp + r * 640 + 5120); qr[r] = lrbf(wq + r * 640); qi[r] = lrbf(wq + r * 640 + 5120); }
        const float sc = TAG_GAIN * TAG_GAIN / 1048576;
        const f32x4 sr = (pr + qr) * sc, si = (pi - qi) * sc, dr = (pr - qr) * sc, di = (pi + qi) * sc;
        a1.re = sr - dr * sn; a1.im = si - di * sn; a2.re = sr + dr * sn; a2.im = si + di * sn;
        bz.re = dr * cs; bz.im = di * cs;
    }
    PIN(a1.re); PIN(a1.im); PIN(a2.re); PIN(a2.im); PIN(bz.re); PIN(bz.im);
    i0 ^= tl; i1 ^= tl;
#pragma unroll 1
    for (int it = 0; it < SPIN_CAP && (stale(or4(or4(0, i0), i1)) || __any(isp[3] != ep)); ++it) {
        BACKOFF();
        i0 = ld128(rws, tb2) ^ tl; i1 = ld128s(rws, tb2, 64 * TB) ^ tl;
        if (kb == 0) isp = ld128(rws, tb4);
    }
    STAMP(10);
    lw32(sb, i0[0]); lw32(sb + 128, i0[1]); lw32(sb + 8320, i0[2]); lw32(sb + 8320 + 128, i0[3]);
    lw32(sb + 512, i1[0]); lw32(sb + 512 + 128, i1[1]); lw32(sb + 512 + 8320, i1[2]); lw32(sb + 512 + 8320 + 128, i1[3]);
    if (kb == 0) { if (g == 0) lw32(L_SP + ((t16 >> 5) * 8 + (t16 & 7)) * 16 + ((t16 >> 3) & 3) * 4, isp[0]); lw128(L_PW + t16 * 32 + (g & 1) * 16, isp); }
    BAR();
    STAMP(4);
    auto wo2 = [&](int q) { dma(ro, vo, q * 8192, w * 8192 + q * 1024); dma(ro, vo, q * 8192 + 8192, w * 8192 + q * 1024 + 1024); };
    const u32x4 zi = lr128(ta), vi = lr128(ta + 1040);
    wo2(0);
    u32x4 zf = twpk(dftA(zi, F1), twa), vf = twpk(dftA(vi, F1), twa);
    PIN(zf); PIN(vf);
    wo2(2);
    {
        const Cx Z = dftB(zf, G), V = dftB(vf, G);
        Cx Vz = V;
        bv = bz;
        if (special) {
            u32x4 v0 = lr128(L_SP + (g * 8 + (c16 & 7)) * 16) ^ 0x80000000u;
            if (c16 >= 8) v0 = vi;
            Vz = dftB(twpk(dftA(v0, F1), twa), G);
            if (g < 2) {
                const float hs = 0.5f * TAG_GAIN * TAG_GAIN / 1048576;
#pragma unroll
                for (int r = 0; r < 4; ++r) {
                    const u32x4 e = lr128(L_PW + (c16 + 64 * g + 16 * r) * 32);
                    const unsigned er = lr32(L_PW + (c16 + 64 * g + 16 * r) * 32 + 16);
                    const float ar = bf(e[1]), ai = bf(e[1] >> 16), br = bf(e[2]), bi = bf(e[2] >> 16);
                    a1.re[r] = 0.5f * a1.re[r] + hs * (ar + br); a1.im[r] = 0.5f * a1.im[r] + hs * (bi - ai);
                    bz.re[r] = 0.5f * bz.re[r] + hs * (ar - br); bz.im[r] = 0.5f * bz.im[r] - hs * (ai + bi);
                    a2.re[r] = 4.f * hs * bf(er); a2.im[r] = 4.f * hs * bf(er >> 16);
                    bv.re[r] = 0.f; bv.im[r] = 0.f;
                }
            }
        }
        u32x4 zc = zip(a1.re * Z.re - a1.im * Z.im - bz.re * Vz.im - bz.im * Vz.re, bz.im * Vz.im - a1.re * Z.im - a1.im * Z.re - bz.re * Vz.re);
        u32x4 vc = zip(a2.re * V.re - a2.im * V.im + bv.re * Z.im + bv.im * Z.re, bv.re * Z.re - a2.re * V.im - a2.im * V.re - bv.im * Z.im);
        PIN(zc); PIN(vc);
        wo2(4);
        u32x4 zt = twpk(dftA(zc, G), twb), vt = twpk(dftA(vc, G), twb);
        PIN(zt); PIN(vt);
        wo2(6);
        const Cx zo = dftB(zt, F2), vo2 = dftB(vt, F2);
        lw128(ta, zipt(zo.re, zo.im, u32x4(t2))); lw128(ta + 1040, zipt(vo2.re, -vo2.im, u32x4(t2)));
        BAR();
        const u32x4 o0 = {lr32(sb), lr32(sb + 128), lr32(sb + 8320), lr32(sb + 8320 + 128)};
        const u32x4 o1 = {lr32(sb + 512), lr32(sb + 512 + 128), lr32(sb + 512 + 8320), lr32(sb + 512 + 8320 + 128)};
        st128(rws, tb2, o0); __builtin_amdgcn_raw_buffer_store_b128(o1, rws, tb2, 64 * TB, 16);
    }
    STAMP(5);
    unsigned tb = WS_T1 + me * TB + ((2 * w + (c16 >> 3)) * 16 + 4 * g) * 128 + ch * 16;
    tl[0] ^= (lane & 3) ? 0u : TM;
    PIN(tb);
    VMWAIT(0);
    BAR();
    STAMP(6);
    if (X2POS == 0 && w < 4) { mid.l[0] = ld128(rws, tb); mid.l[1] = ld128(rws, tb + 128); mid.l[2] = ld128(rws, tb + 256); mid.l[3] = ld128(rws, tb + 384); }
    FENCE();
    mid.F = F; mid.tw1 = tw1; mid.tl = tl; mid.tb = tb;
}
DI void b_second(const Params& p, rsrc_t rws, unsigned ep, const BMid& mid, int xcd, int li, int lane, int w, int rt STP_PARAM) {
    const int g = lane >> 4, c16 = lane & 15, ch = lane & 7, jm = (16 - c16) & 15, me = xcd * 16 + li;
    const Mat& F = mid.F; const Cx& tw1 = mid.tw1; const u32x4 tl = mid.tl;
    const rsrc_t rout = mkrsrc(p.out, 2048 * 1024 * 2);
    unsigned ra[4];
#pragma unroll
    for (int i = 0; i < 4; ++i) {
        const int km = 64 * g + 16 * i + c16, kq = km < 128 ? km : (256 - km) & 127;
        ra[i] = L_U + ((km >> 7) * 16 + 2 * w) * 592 + (kq + 16 * (kq >> 6)) * 4;
        PIN(ra[i]);
    }
    const int set = g & 1, k0 = 16 * (2 * w + (g >> 1));
    unsigned oo = (256 * w + me) * 2048 + 1024 + (64 * g + c16) * 4;
    unsigned u2 = L_U + (set * 16 + (set ? jm : c16)) * 592 + (k0 + 16 * (k0 >> 6)) * 4;
    PIN(oo); PIN(u2);
    if (w < 4) {
        const unsigned tb = mid.tb;
        u32x4 l0, l1, l2, l3;
        if (X2POS == 0) { l0 = mid.l[0] ^ tl; l1 = mid.l[1] ^ tl; l2 = mid.l[2] ^ tl; l3 = mid.l[3] ^ tl; }
        else { l0 = ld128(rws, tb) ^ tl; l1 = ld128(rws, tb + 128) ^ tl; l2 = ld128(rws, tb + 256) ^ tl; l3 = ld128(rws, tb + 384) ^ tl; }
#pragma unroll 1
        for (int it = 0; it < SPIN_CAP && stale(or4(or4(or4(or4(0, l0), l1), l2), l3)); ++it) {
            BACKOFF();
            l0 = ld128(rws, tb) ^ tl; l1 = ld128(rws, tb + 128) ^ tl; l2 = ld128(rws, tb + 256) ^ tl; l3 = ld128(rws, tb + 384) ^ tl;
        }
        u32x4 t[4];
#pragma unroll
        for (int e = 0; e < 4; ++e) t[e] = pk4(dftA(u32x4{l0[e], l1[e], l2[e], l3[e]}, F));
#pragma unroll
        for (int r = 0; r < 4; ++r) lw128(u2 + r * 16, u32x4{t[0][r], t[1][r], t[2][r], t[3][r]});
    }
    BAR();
    STAMP(11);
    {
        const unsigned ts = tag_bits(ep, 0)[1], sa = WS_O + (rt * 64 + lane) * 1024 + 4;
        unsigned sen = ts;
        if (w == 0) sen = ld32(rws, sa);
        const u32x4 d0 = {lr32(ra[0]), lr32(ra[1]), lr32(ra[2]), lr32(ra[3])};
        const u32x4 d1 = {lr32(ra[0] + 592), lr32(ra[1] + 592), lr32(ra[2] + 592), lr32(ra[3] + 592)};
        const u32x4 y0 = pk4c(dftB(twpk(dftA(d0, F), tw1), F)), y1 = pk4c(dftB(twpk(dftA(d1, F), tw1), F));
#pragma unroll
        for (int r = 0; r < 4; ++r) {
            __builtin_amdgcn_raw_buffer_store_b32(y0[r], rout, oo + 64 * r, 0, OUTB_AUX);
            __builtin_amdgcn_raw_buffer_store_b32(y1[r], rout, oo + 64 * r, 128 * 2048, OUTB_AUX);
        }
        STAMP(13);
#pragma unroll 1
        for (int it = 0; it < SPIN_CAP && stale(sen ^ ts); ++it) {
            const int first = __builtin_ctzll(__ballot((sen ^ ts) & TM));
            BACKOFF();
            if (lane == first) sen = ld32(rws, sa);
            if (!((__ballot((sen ^ ts) & TM) >> first) & 1)) { FENCE(); if ((sen ^ ts) & TM) sen = ld32(rws, sa); }
        }
    }
}

__global__ void __launch_bounds__(512, 2) fused_kernel(Params p) {
    const int b = blockIdx.x, xcd = b & 7, i = b >> 3, li = i & 15;
    const int tid = threadIdx.x, lane = tid & 63, w = __builtin_amdgcn_readfirstlane(tid >> 6);
    BIn in;
    {
        size_t code;
        asm volatile("s_getpc_b64 %0" : "=s"(code));
        asm volatile("s_mov_b64 exec, 1\n\tglobal_load_dword v255, %0, %1\n\ts_mov_b64 exec, -1" :: "v"(min((unsigned)(w * 32 + i) * 128u, CODE_BYTES - 128u)), "s"(code & ~(size_t)255) : "memory", "v255");
    }
    __shared__ __attribute__((aligned(16))) char smem[LDS_BYTES];
    asm volatile("" :: "s"((unsigned)(size_t)(LAS char*)smem));
    STAMP(0);
    const int rp = (xcd >> 1) * 4 + (li & 3), hg = (xcd & 1) * 4 + (li >> 2), rt = xcd * 4 + (i & 3), ct = i >> 2;
    if (i < 16 && w >= 4) a_loader(p, rp, hg, li, lane, w - 4 STP_ARG);
    FENCE();
    if (i >= 16) b_loads(p, in, xcd, li, tid, lane, w);
    FENCE();
    const rsrc_t rws = mkrsrc(p.ws, WS_END);
    unsigned ep;
    epoch_ask(ep, p.ws);
    BMid mid;
    if (i >= 16) b_first(p, rws, ep, in, mid, xcd, li, tid, lane, w, ct STP_ARG);
    else if (w < 4) { epoch_get(ep); a_tile(rws, ep, rp, hg, lane, w STP_ARG); }
    else { a_loader_tail(p, rws, ep, rp, hg, lane, w - 4, ct STP_ARG); }
    const int g = lane >> 4, c16 = lane & 15, q = c16 >> 2, kh = w >> 2;
    unsigned ob = WS_O + (rt * 4 + (w & 3)) * 16384 + kh * 8192 + lane * 16;
    unsigned wa = kh * 32768 + (16 * (g >> 1) + 4 * (g & 1) + q) * 128 + 8 * (lane & 3) + (((2 * g + (q >> 1)) & 3) << 5);
    const unsigned scr = 81920;
    unsigned rw = scr + kh * 17408 + ((w & 3) * 16 + c16) * 272 + g * 16, rr = scr + (tid >> 3) * 272 + (tid & 7) * 32;
    unsigned oo = (rt * 64 + (tid >> 3)) * 2048 + ct * 128 + (tid & 7) * 16;
    u32x4 tl = tag_bits(ep, lane & 3);
    u32x4 o[8], wf[8][4];
#pragma unroll
    for (int k = 0; k < 8; ++k)
#pragma unroll
        for (int t = 0; t < 4; ++t) { const unsigned a = (wa ^ (t << 5)) + k * 4096; wf[k][t] = ltr2(a, a + 1024); }
    PIN(ob); PIN(rw); PIN(rr); PIN(oo); PIN(tl);
    SCHED_FENCE();
    if (i < 16) VMWAIT(0);
    else b_second(p, rws, ep, mid, xcd, li, lane, w, rt STP_ARG);
    BAR();
    STAMP(7);
#pragma unroll
    for (int k = 0; k < 8; ++k) o[k] = ld128s(rws, ob, k * 1024);
    f32x4 acc[4] = {Z4, Z4, Z4, Z4};
    unsigned m = 0;
#pragma unroll
    for (int k = 0; k < 8; ++k) {
        o[k] ^= tl;
#pragma unroll
        for (int t = 0; t < 4; ++t) acc[t] = mma(wf[k][t], o[k], acc[t]);
        m = or4(m, o[k]);
        SCHED_FENCE();
    }
    if (__builtin_expect(stale(m), 0)) {
#pragma unroll 1
        for (int it = 0; it < SPIN_CAP; ++it) {
            BACKOFF();
            m = 0;
#pragma unroll
            for (int k = 0; k < 8; ++k) o[k] = ld128s(rws, ob, k * 1024) ^ tl;
#pragma unroll
            for (int k = 0; k < 8; ++k) m = or4(m, o[k]);
            if (!stale(m)) break;
        }
#pragma unroll
        for (int t = 0; t < 4; ++t) acc[t] = Z4;
#pragma unroll
        for (int k = 0; k < 8; ++k)
#pragma unroll
            for (int t = 0; t < 4; ++t) acc[t] = mma(wf[k][t], o[k], acc[t]);
    }
    STAMP(8);
#pragma unroll
    for (int t = 0; t < 4; ++t) lwf128(rw + t * 64, acc[t]);
    BAR();
    const f32x4 y0 = lrf128(rr) + lrf128(rr + 17408), y1 = lrf128(rr + 16) + lrf128(rr + 17408 + 16);
    __builtin_amdgcn_raw_buffer_store_b128(cat(pkv(y0), pkv(y1)), mkrsrc(p.out, 2048 * 1024 * 2), oo, 0, OUT_AUX);
    if (b == 128 && tid == 0) st32(rws, WS_EPOCH, ep);
    STAMP(9);
}

extern "C" void kernel_launch(void* const* d_in, const int* in_sizes, int n_in,
                              void* d_out, int out_size, void* d_ws, size_t ws_size,
                              hipStream_t stream) {
    Params p;
    p.ax = (const unsigned short*)d_in[0]; p.wqkv = (const unsigned short*)d_in[1]; p.wo = (const unsigned short*)d_in[2];
    p.bx = (const unsigned short*)d_in[3]; p.wr = (const unsigned short*)d_in[4]; p.wi = (const unsigned short*)d_in[5];
    p.out = (unsigned short*)d_out; p.ws = (unsigned char*)d_ws;
    fused_kernel<<<256, 512, 0, stream>>>(p);
}
```

```cpp
#define HINT 2
#include <hip/hip_runtime.h>
#include <stdint.h>

typedef short    s16x4 __attribute__((ext_vector_type(4)));
typedef __bf16   bf16x8 __attribute__((ext_vector_type(8)));
typedef __bf16   bf16x2 __attribute__((ext_vector_type(2)));
typedef float    f32x2 __attribute__((ext_vector_type(2)));
typedef float    f32x4 __attribute__((ext_vector_type(4)));
typedef float    f32x16 __attribute__((ext_vector_type(16)));
typedef unsigned u32x2 __attribute__((ext_vector_type(2)));
typedef unsigned u32x4 __attribute__((ext_vector_type(4)));
typedef __amdgpu_buffer_rsrc_t rsrc_t;
#define LAS __attribute__((address_space(3)))
#define DI __device__ __forceinline__

constexpr unsigned WS_EPOCH = 0;
constexpr unsigned WS_HINT = 4096;
constexpr unsigned PART_STRIDE = 2688, WS_PART = 32768;
constexpr unsigned TB = 17024;
constexpr unsigned WS_T1 = 1u << 19;
constexpr unsigned WS_O = WS_T1 + 256 * TB, WS_END = WS_O + (1u << 21);
constexpr int SPIN_CAP = 1 << 15;
#define RSLEEP 2
#define BACKOFF() do { __builtin_amdgcn_s_sleep(RSLEEP); FENCE(); } while (0)
constexpr unsigned LDS_BYTES = 163840;
#define OUTB_AUX 16
#define OUT_AUX 16
#define STAMP(i) do {} while (0)
#define STP_PARAM
#define STP_ARG

struct Params {
    const unsigned short *ax, *wqkv, *wo, *bx, *wr, *wi;
    unsigned short* out;
    unsigned char* ws;
};

DI rsrc_t mkrsrc(const void* ptr, unsigned bytes) { return __builtin_amdgcn_make_buffer_rsrc((void*)ptr, 0, bytes, 0x00020000); }
DI u32x4 ld128(rsrc_t r, unsigned off)  { return __builtin_amdgcn_raw_buffer_load_b128(r, off, 0, 16); }
DI u32x2 ld64(rsrc_t r, unsigned off)   { return __builtin_amdgcn_raw_buffer_load_b64(r, off, 0, 16); }
DI unsigned ld32(rsrc_t r, unsigned off){ return __builtin_amdgcn_raw_buffer_load_b32(r, off, 0, 16); }
DI u32x2 ld64s(rsrc_t r, unsigned off, unsigned so)   { return __builtin_amdgcn_raw_buffer_load_b64(r, off, so, 16); }
DI u32x4 ld128s(rsrc_t r, unsigned off, unsigned so)  { return __builtin_amdgcn_raw_buffer_load_b128(r, off, so, 16); }
DI unsigned ld32s(rsrc_t r, unsigned off, unsigned so){ return __builtin_amdgcn_raw_buffer_load_b32(r, off, so, 16); }
DI void st128(rsrc_t r, unsigned off, u32x4 v) { __builtin_amdgcn_raw_buffer_store_b128(v, r, off, 0, 16); }
DI void st64(rsrc_t r, unsigned off, u32x2 v) { __builtin_amdgcn_raw_buffer_store_b64(v, r, off, 0, 16); }
DI void st32(rsrc_t r, unsigned off, unsigned v) { __builtin_amdgcn_raw_buffer_store_b32(v, r, off, 0, 16); }
#define FENCE() asm volatile("" ::: "memory")
#define BAR() asm volatile("s_waitcnt lgkmcnt(0)\n\ts_barrier" ::: "memory")
#define VMWAIT(n) asm volatile("s_waitcnt vmcnt(" #n ")" ::: "memory")
#define PIN(x) asm volatile("" : "+v"(x))
#define SCHED_FENCE() __builtin_amdgcn_sched_barrier(0)
DI void dma(rsrc_t r, unsigned voff, unsigned soff, unsigned lds) {
    asm volatile("s_mov_b32 m0, %0\n\ts_nop 0\n\tbuffer_load_dwordx4 %1, %2, %3 offen lds"
                 :: "s"(lds), "v"(voff), "s"(r), "s"(soff) : "memory");
}
DI void dma4(rsrc_t r, unsigned v0, unsigned v1, unsigned v2, unsigned v3, unsigned soff, unsigned lds) {
    asm volatile("s_mov_b32 m0, %0\n\ts_nop 0\n\tbuffer_load_dwordx4 %1, %5, %6 offen lds\n\tbuffer_load_dwordx4 %2, %5, %6 offen offset:1024 lds\n\t"
                 "buffer_load_dwordx4 %3, %5, %6 offen offset:2048 lds\n\tbuffer_load_dwordx4 %4, %5, %6 offen offset:3072 lds"
                 :: "s"(lds), "v"(v0), "v"(v1), "v"(v2), "v"(v3), "s"(r), "s"(soff) : "memory");
}
DI void dma2(rsrc_t r, unsigned v0, unsigned v1, unsigned soff, unsigned lds) {
    asm volatile("s_mov_b32 m0, %0\n\ts_nop 0\n\tbuffer_load_dwordx4 %1, %3, %4 offen lds\n\tbuffer_load_dwordx4 %2, %3, %4 offen offset:1024 lds"
                 :: "s"(lds), "v"(v0), "v"(v1), "s"(r), "s"(soff) : "memory");
}
DI unsigned pk(float lo, float hi) { f32x2 v = {lo, hi}; return __builtin_bit_cast(unsigned, __builtin_convertvector(v, bf16x2)); }
DI u32x2 pkv(f32x4 v) { return u32x2{pk(v[0], v[1]), pk(v[2], v[3])}; }
DI u32x4 cat(u32x2 a, u32x2 b) { return u32x4{a[0], a[1], b[0], b[1]}; }
DI f32x4 mma(u32x4 a, u32x4 b, f32x4 c) {
    return __builtin_amdgcn_mfma_f32_16x16x32_bf16(__builtin_bit_cast(bf16x8, a), __builtin_bit_cast(bf16x8, b), c, 0, 0, 0); }
DI f32x16 mma32(u32x4 a, u32x4 b, f32x16 c) {
    return __builtin_amdgcn_mfma_f32_32x32x16_bf16(__builtin_bit_cast(bf16x8, a), __builtin_bit_cast(bf16x8, b), c, 0, 0, 0); }
DI f32x4 mma16(u32x2 a, u32x2 b, f32x4 c) {
    return __builtin_amdgcn_mfma_f32_16x16x16bf16_1k(__builtin_bit_cast(s16x4, a), __builtin_bit_cast(s16x4, b), c, 0, 0, 0); }
#define LP(T, a) ((LAS T*)(size_t)(a))
DI u32x4 lr128(unsigned a) { return *LP(const u32x4, a); }
DI f32x4 lrf128(unsigned a) { return *LP(const f32x4, a); }
DI u32x2 lr64(unsigned a) { return *LP(const u32x2, a); }
DI unsigned lr32(unsigned a) { return *LP(const unsigned, a); }
DI float bf(unsigned short v) { return __builtin_bit_cast(float, (unsigned)v << 16); }
DI float lrbf(unsigned a) { return bf(*LP(const unsigned short, a)); }
DI void lw128(unsigned a, u32x4 v) { *LP(u32x4, a) = v; }
DI void lwf128(unsigned a, f32x4 v) { *LP(f32x4, a) = v; }
DI void lw64(unsigned a, u32x2 v) { *LP(u32x2, a) = v; }
DI void lw32(unsigned a, unsigned v) { *LP(unsigned, a) = v; }
DI u32x2 ltr(unsigned a) { return __builtin_bit_cast(u32x2, __builtin_amdgcn_ds_read_tr16_b64_v4i16(LP(s16x4, a))); }
DI u32x4 ltr2(unsigned a, unsigned b) { return cat(ltr(a), ltr(b)); }
constexpr unsigned TM = 0x00010001u;
DI u32x4 tag_bits(unsigned ep, int unit, unsigned use = 2) {
    const unsigned t = ((ep << 2) | use) >> (8 * unit);
    u32x4 m;
#pragma unroll
    for (int k = 0; k < 4; ++k) m[k] = ((t >> (2 * k)) & 1u) | (((t >> (2 * k + 1)) & 1u) << 16);
    return m;
}
constexpr float TAG_GAIN = 1.0027f;
DI unsigned pkt1(float a, float b, unsigned bits) { return __builtin_amdgcn_bitop3_b32(pk(a, b), TM, bits, 0xBA); }
DI u32x4 zipt(f32x4 a, f32x4 b, u32x4 t) { return u32x4{pkt1(a[0], b[0], t[0]), pkt1(a[1], b[1], t[1]), pkt1(a[2], b[2], t[2]), pkt1(a[3], b[3], t[3])}; }
DI u32x4 catt(f32x4 a, f32x4 b, u32x4 t) { return u32x4{pkt1(a[0], a[1], t[0]), pkt1(a[2], a[3], t[1]), pkt1(b[0], b[1], t[2]), pkt1(b[2], b[3], t[3])}; }
DI unsigned or4(unsigned m, u32x4 v) { return m | v[0] | v[1] | v[2] | v[3]; }
DI bool stale(unsigned m) { return __any(m & TM); }
DI u32x2 phi4(f32x4 t) {
    f32x4 e, m;
#pragma unroll
    for (int r = 0; r < 4; ++r) {
        e[r] = __builtin_amdgcn_fmed3f(__builtin_amdgcn_exp2f(t[r] * 1.44269504f), 0.f, 1.f);
        m[r] = __builtin_elementwise_maximum(t[r], 0.f);
    }
    return pkv((m + e) + 1e-6f);
}
constexpr f32x4 Z4 = {0.f, 0.f, 0.f, 0.f};

struct Mat { u32x4 re, im; };
struct Cx  { f32x4 re, im; };
DI Cx angles(int n0, int dn, int mask, float inv) {
    Cx t;
#pragma unroll
    for (int r = 0; r < 4; ++r) { float a = (float)((n0 + r * dn) & mask) * inv;
        t.re[r] = __builtin_amdgcn_cosf(a); t.im[r] = __builtin_amdgcn_sinf(a); }
    return t;
}
DI u32x4 zip(f32x4 a, f32x4 b) { return u32x4{pk(a[0], b[0]), pk(a[1], b[1]), pk(a[2], b[2]), pk(a[3], b[3])}; }
DI Mat tomat(Cx t) { return Mat{zip(t.re, t.im), zip(-t.im, t.re)}; }
DI Cx dftA(u32x4 d, const Mat& f) { return Cx{mma(d, f.re, Z4), mma(d, f.im, Z4)}; }
DI Cx dftB(u32x4 d, const Mat& f) { return Cx{mma(f.re, d, Z4), mma(f.im, d, Z4)}; }
DI u32x4 twpk(Cx v, const Cx& w) { return zip(v.re * w.re + v.im * w.im, v.im * w.re - v.re * w.im); }
DI u32x4 pk4(Cx v)  { return zip(v.re, v.im); }
DI u32x4 pk4c(Cx v) { return zip(v.re, v.im) ^ 0x80000000u; }

DI void epoch_ask(unsigned& e, const unsigned char* ws) { asm volatile("s_load_dword %0, %1, 0x0" : "=s"(e) : "s"(ws) : "memory"); }
DI void epoch_get(unsigned& e) {
    unsigned r;
    asm volatile("s_waitcnt lgkmcnt(0)\n\ts_add_u32 %0, %1, 1" : "=s"(r) : "s"(e) : "memory", "scc");
    e = r;
}
DI f32x4 q4(const f32x16& v, int i) { return f32x4{v[4 * i], v[4 * i + 1], v[4 * i + 2], v[4 * i + 3]}; }
struct Frag { u32x4 a[2][2], b[2][3]; };
DI void frag_read(Frag& f, unsigned xa, unsigned wa) {
#pragma unroll
    for (int k = 0; k < 2; ++k) {
        f.a[k][0] = lr128(xa ^ (k * 32)); f.b[k][0] = ltr2(wa + k * 10240, wa + k * 10240 + 512);
        f.a[k][1] = lr128((xa ^ (k * 32)) + 10240);
        f.b[k][1] = ltr2(wa + k * 10240 + 2048, wa + k * 10240 + 2560); f.b[k][2] = ltr2(wa + k * 10240 + 4096, wa + k * 10240 + 4608);
    }
}
DI void frag_mma(const Frag& f, f32x16 (&acc)[2][3]) {
#pragma unroll
    for (int k = 0; k < 2; ++k)
#pragma unroll
        for (int j = 0; j < 3; ++j)
#pragma unroll
            for (int i = 0; i < 2; ++i) acc[i][j] = (j < 2) ? mma32(f.a[k][i], f.b[k][j], acc[i][j]) : mma32(f.b[k][j], f.a[k][i], acc[i][j]);
}
DI void interleave() {
#pragma unroll
    for (int i = 0; i < 8; ++i) { __builtin_amdgcn_sched_group_barrier(0x8, 1, 0); __builtin_amdgcn_sched_group_barrier(0x100, 2, 0); }
    __builtin_amdgcn_sched_group_barrier(0x8, 4, 0);
}
#define DEPTH 2
#define ROTK 0
constexpr unsigned CODE_BYTES = 22784;

constexpr unsigned SLOT = 40960;
#define HINTA 1
constexpr unsigned A_RED = 65536;
DI void a_loader(const Params& p, int rp, int hg, int li, int lane, int lw, int ct STP_PARAM) {
    const int rot = ROTK == 0 ? 0 : ROTK == 1 ? 2 * (li >> 2) + 2 * (li & 3) : (li & 1) * 4;
    const int r8 = lane >> 3, ch = lane & 7;
    const rsrc_t rx = mkrsrc(p.ax, 2048 * 512 * 2), rw = mkrsrc(p.wqkv, 512 * 1536 * 2);
    const unsigned vx = (rp * 128 + lw * 32 + r8) * 1024 + ((ch ^ (r8 >> 1)) << 4);
    const unsigned vw = (lw * 16 + r8) * 3072 + hg * 128 + ((ch ^ ((r8 & 2) << 1)) << 4);
    const unsigned vx1 = (vx ^ 64) + 7168, vx2 = vx + 14336, vx3 = (vx ^ 64) + 21504, vk = vw + 1024, vw1 = vw + 24576 - 1024;
#pragma unroll 1
    for (int s = 0; s < 8; ++s) {
        if (s == 0) STAMP(6);
        if (s >= 2) asm volatile("s_waitcnt vmcnt(10)\n\ts_barrier" ::: "memory");
        if (s == 2) STAMP(11);
        const unsigned kb = (s + rot) & 7, so = kb * (64 * 3072), l = (s & 3) * SLOT + lw * 10240;
        dma4(rx, vx, vx1, vx2, vx3, kb * 128, l);
        dma4(rw, vk, vw + 24576, vw, vw1, so, l + 4096);
        dma2(rw, vw, vw1, so, l + 8192);
    }
}
DI void a_loader_tail(const Params& p, rsrc_t rws, unsigned& ep, int rp, int hg, int lane, int lw, int ct STP_PARAM) {
    const int r8 = lane >> 3, ch = lane & 7;
    const rsrc_t ro = mkrsrc(p.wo, 512 * 512 * 2);
    const unsigned vo = (lw * 64 + r8) * 1024 + ct * 128 + ((ch ^ (r8 & 6)) << 4);
    VMWAIT(10);
#pragma unroll 1
    for (int u = 0; u < 2; ++u) {
        BAR();
#pragma unroll 1
        for (int q = 0; q < 8; ++q) dma(ro, vo, u * 262144 + q * 8192, u * 32768 + lw * 8192 + q * 1024);
        if (u == 0) VMWAIT(8);
    }
    STAMP(1);
    BAR();
    epoch_get(ep);
    BAR(); VMWAIT(0); BAR();
}
DI void a_tile(rsrc_t rws, unsigned ep, int rp, int hg, int lane, int w STP_PARAM) {
    const int g = lane >> 4, c16 = lane & 15, h = lane >> 5, r32 = lane & 31, rh = w >> 1, hp = w & 1;
    const unsigned xa = rh * 20480 + r32 * 128 + ((h ^ ((r32 >> 1) & 7)) << 4);
    const unsigned wa = 4096 + (8 * h + (c16 >> 2)) * 128 + ((hp ^ (c16 >> 3)) << 6) + (g & 1) * 32 + 8 * (lane & 3);
    f32x16 acc[2][3];
#pragma unroll
    for (int i = 0; i < 2; ++i)
#pragma unroll
        for (int j = 0; j < 3; ++j) { acc[i][j] = 0.f; PIN(acc[i][j]); }
    const unsigned pg = WS_PART + hg * PART_STRIDE + hp * 1024;
    unsigned red = A_RED + hp * 4096 + lane * 16;
    unsigned pst = pg + rp * 8 * PART_STRIDE + lane * 16;
    unsigned pz = c16 < 4 ? pg + rp * 8 * PART_STRIDE + 2048 - hp * 768 + g * 64 + c16 * 16 : WS_END;
    unsigned pk0 = pg + rh * 64 * PART_STRIDE + lane * 16, pz0 = pg + rh * 64 * PART_STRIDE + 2048 - hp * 768 + (g ^ 1) * 64 + (lane & 3) * 16;
    unsigned ob = WS_O + ((rp * 8 + rh * 4 + (g & 1)) * 16 + hg * 2 + hp) * 1024 + (h * 16 + c16) * 16;
    u32x4 tg = tag_bits(ep, lane & 3), sel;
#pragma unroll
    for (int k = 0; k < 4; ++k)
        sel[k] = (r32 < 16 && ((r32 >> 2) & 1) == h && ((r32 >> 3) * 2 + ((r32 >> 1) & 1)) == k) ? ((r32 & 1) ? 0x3F800000u : 0x00003F80u) : 0u;
    PIN(red); PIN(pst); PIN(pz); PIN(pk0); PIN(pz0); PIN(ob); PIN(tg); PIN(sel);
    Frag f0, f1;
    BAR();
    frag_read(f0, xa, wa);
#pragma unroll 1
    for (int s = 0; s < 7; ++s) {
        const unsigned slot = (s & 3) * SLOT, nslot = ((s + 1) & 3) * SLOT;
        SCHED_FENCE();
        frag_read(f1, slot + (xa ^ 64), slot + wa + 20480); frag_mma(f0, acc); interleave();
        SCHED_FENCE(); BAR();
        frag_read(f0, nslot + xa, nslot + wa); frag_mma(f1, acc); interleave();
    }
    SCHED_FENCE();
    frag_read(f1, 3 * SLOT + (xa ^ 64), 3 * SLOT + wa + 20480); frag_mma(f0, acc); interleave();
    SCHED_FENCE();
    frag_mma(f1, acc);
    STAMP(1);
    {
        f32x16 kv = 0.f, zz = 0.f;
        const u32x4 ones = {0x3F803F80u, 0x3F803F80u, 0x3F803F80u, 0x3F803F80u};
#pragma unroll
        for (int i = 0; i < 2; ++i)
#pragma unroll
            for (int u = 0; u < 2; ++u) {
                const u32x4 A = cat(phi4(q4(acc[i][0], 2 * u)), phi4(q4(acc[i][0], 2 * u + 1))), B = cat(pkv(q4(acc[i][1], 2 * u)), pkv(q4(acc[i][1], 2 * u + 1)));
                kv = mma32(A, B, kv); zz = mma32(A, ones, zz);
            }
        f32x4 k0 = (g & 1) ? q4(kv, 2) : q4(kv, 0), k1 = (g & 1) ? q4(kv, 3) : q4(kv, 1);
        f32x4 z0 = (g & 1) ? q4(zz, 2) : q4(zz, 0), z1 = (g & 1) ? q4(zz, 3) : q4(zz, 1);
        if (rh) { lwf128(red, k0); lwf128(red + 1024, k1); lwf128(red + 2048, z0); lwf128(red + 3072, z1); }
        BAR();
        if (!rh) {
            k0 += lrf128(red); k1 += lrf128(red + 1024); z0 += lrf128(red + 2048); z1 += lrf128(red + 3072);
            st128(rws, pst, catt(k0, k1, tg)); st128(rws, pz, catt(z0, z1, tg));
        }
    }
    STAMP(2);
    SCHED_FENCE();
    u32x4 pq[2][2];
#pragma unroll
    for (int i = 0; i < 2; ++i)
#pragma unroll
        for (int b = 0; b < 2; ++b) { pq[i][b] = cat(phi4(q4(acc[i][2], 2 * b)), phi4(q4(acc[i][2], 2 * b + 1))); PIN(pq[i][b]); }
    f32x16 sk = 0.f, sz = 0.f;
    u32x4 dk[8], dz[8];
#pragma unroll
    for (int j = 0; j < 8; ++j) { dz[j] = tg; PIN(dz[j]); }
    PIN(sk); PIN(sz);
    VMWAIT(0);
    BAR();
    STAMP(3);
#pragma unroll
    for (int j = 0; j < 8; ++j) dk[j] = ld128s(rws, pk0, j * 8 * PART_STRIDE);
    if (c16 < 4) {
#pragma unroll
        for (int j = 0; j < 8; ++j) dz[j] = ld128s(rws, pz0, j * 8 * PART_STRIDE);
    }
    unsigned m = 0;
#pragma unroll
    for (int j = 0; j < 8; ++j) { dk[j] ^= tg; sk = mma32(sel, dk[j], sk); m = or4(m, dk[j]); SCHED_FENCE(); }
#pragma unroll
    for (int j = 0; j < 8; ++j) { dz[j] ^= tg; sz = mma32(sel, dz[j], sz); m = or4(m, dz[j]); SCHED_FENCE(); }
    if (__builtin_expect(stale(m), 0)) {
#pragma unroll 1
        for (int it = 0; it < SPIN_CAP; ++it) {
            BACKOFF();
            m = 0;
            if (c16 < 4) {
#pragma unroll
                for (int j = 0; j < 8; ++j) dz[j] = ld128s(rws, pz0, j * 8 * PART_STRIDE) ^ tg;
            }
#pragma unroll
            for (int j = 0; j < 8; ++j) dk[j] = ld128s(rws, pk0, j * 8 * PART_STRIDE) ^ tg;
#pragma unroll
            for (int j = 0; j < 8; ++j) m = or4(or4(m, dz[j]), dk[j]);
            if (!stale(m)) break;
        }
        sk = 0.f; sz = 0.f;
#pragma unroll
        for (int j = 0; j < 8; ++j) { sz = mma32(sel, dz[j], sz); sk = mma32(sel, dk[j], sk); }
    }
    const unsigned ex = red + 8192 * rh, ey = red + 8192 * (rh ^ 1);
    lwf128(ex, q4(sk, 0)); lwf128(ex + 1024, q4(sk, 1)); lwf128(ex + 2048, q4(sz, 0)); lwf128(ex + 3072, q4(sz, 1));
    BAR();
    if (HINT == 1 && w == 0 && lane == 0) st32(rws, WS_HINT + (rp * 8 + hg) * 128, ep);
    STAMP(4);
    const u32x4 KV = cat(pkv(q4(sk, 0) + lrf128(ey)), pkv(q4(sk, 1) + lrf128(ey + 1024)));
    u32x4 ZZ = cat(pkv(q4(sz, 0) + lrf128(ey + 2048)), pkv(q4(sz, 1) + lrf128(ey + 3072)));
#pragma unroll
    for (int k = 0; k < 4; ++k) ZZ[k] = __builtin_amdgcn_update_dpp(ZZ[k], ZZ[k], 0x114, 0xF, 0x2, false);
#pragma unroll
    for (int b = 0; b < 2; ++b) {
        const u32x4 A = ((g & 1) == b) ? KV : ZZ;
#pragma unroll
        for (int i = 0; i < 2; ++i) {
            const f32x16 nd = mma32(A, pq[i][b], 0.f);
            const float ri = TAG_GAIN * __builtin_amdgcn_rcpf(nd[b ? 0 : 8] + 1e-6f);
            st128(rws, ob + i * 32768 + b * 512, catt(q4(nd, 2 * b) * ri, q4(nd, 2 * b + 1) * ri, tg));
        }
    }
    if (HINT == 2 && w == 0 && lane == 0) st32(rws, WS_HINT + (rp * 8 + hg) * 128, ep);
    STAMP(5);
}

constexpr unsigned L_U = 98304;
constexpr unsigned L_S = 65536;
constexpr unsigned L_W = L_U + 19200;
constexpr unsigned L_SP = L_W + 20480;
constexpr unsigned L_C = L_SP + 512;
constexpr unsigned L_PW = L_C + 14336;
constexpr unsigned L_R = 16384;
#define YPOS 1
#define X2POS 0
struct BIn { const char* pf; size_t pm; unsigned po; };
DI void touch(const BIn& in, unsigned add) {
    asm volatile("s_mov_b64 exec, %2\n\tglobal_load_dword v255, %0, %1\n\ts_mov_b64 exec, -1" :: "v"(in.po), "s"(in.pf + add), "s"(in.pm) : "memory", "v255");
}
DI void b_loads(const Params& p, BIn& in, int xcd, int li, int tid, int lane, int w) {
    const int g = lane >> 4, c16 = lane & 15;
    in.pf = (w & 1) ? (const char*)p.ax : (const char*)p.wqkv;
    in.pm = (w & 1) ? 0xFFFFFFFFull : 0x0FFF0FFF0FFF0FFFull;
    in.po = (w & 1) ? ((xcd >> 1) * 512 + li * 32 + (lane & 31)) * 1024 + (1 + (w >> 1)) * 128
                    : (64 + 32 * w + li * 4 + g) * 3072 + (xcd & 1) * 512 + (c16 >> 2) * 1024 + (c16 & 3) * 128;
    if (w < 2) touch(in, 0);
}
struct BMid { Mat F; Cx tw1; u32x4 tl, l[4], y0, y1; unsigned tb, oo; };
DI void b_first(const Params& p, rsrc_t rws, unsigned& ep, BIn& in, BMid& mid, int xcd, int li, int tid, int lane, int w, int ct STP_PARAM) {
    const int g = lane >> 4, c16 = lane & 15;
    const int me = xcd * 16 + li;
    const int j1 = (li >> 3) ? (xcd ? 16 - xcd : 8) : xcd, kb = li & 7;
    const int r8 = lane >> 3, ch = lane & 7, jm = (16 - c16) & 15;
    const bool special = (kb == 0 && w == 0);
    if ((w >> 1) == 1) touch(in, 0);
    if (w < 7) {
        const int k4 = 4 * w, sU = (0x0040004 >> k4) & 15, sQ = (0x0400040 >> k4) & 15;
        const int P = (((c16 & ((0xF7FF7FF >> k4) & 15)) << sU) + (sU ? j1 : 0)) << ((0x3007833 >> k4) & 15);
        const int n0 = P * (((4 * (g & ((0x3313133 >> k4) & 15))) << sQ) + (sQ ? j1 : 0)), dn = P << sQ;
        const Cx t = angles(n0, dn, 2047, 1.f / 2048);
        Mat m = Mat{__builtin_bit_cast(u32x4, t.re), __builtin_bit_cast(u32x4, t.im)};
        if (w < 4) m = tomat(t);
        if (w == 2 && (g >> 1) != (c16 >> 3)) m = Mat{u32x4{0, 0, 0, 0}, u32x4{0, 0, 0, 0}};
        lw128(L_C + w * 2048 + lane * 16, m.re); lw128(L_C + w * 2048 + 1024 + lane * 16, m.im);
    }
    {
        const unsigned ws = __builtin_amdgcn_readfirstlane(w), vo = me * 8224 + lane * 16;
        asm volatile("s_mov_b32 m0, %0\n\ts_nop 0\n\tbuffer_load_dwordx4 %1, %3, 0 offen nt lds\n\tbuffer_load_dwordx4 %2, %3, 0 offen offset:1024 nt lds"
                     :: "s"(ws * 2048), "v"((256 * w + me) * 1024 + lane * 16), "v"((256 * w + me + 127) * 1024 + lane * 16), "s"(mkrsrc(p.bx, 2048 * 512 * 2)) : "memory");
        const rsrc_t rr = mkrsrc(p.wr, 2048 * 514), ri = mkrsrc(p.wi, 2048 * 514);
        asm volatile("s_mov_b32 m0, %0\n\ts_nop 0\n\tbuffer_load_dwordx4 %3, %4, %7 offen nt lds\n\t"
                     "s_mov_b32 m0, %1\n\ts_nop 0\n\tbuffer_load_dwordx4 %3, %5, %7 offen nt lds\n\t"
                     "s_mov_b32 m0, %2\n\ts_mov_b64 exec, 3\n\tbuffer_load_dwordx4 %3, %6, %8 offen nt lds\n\ts_mov_b64 exec, -1"
                     :: "s"(L_R + ws * 1024), "s"(L_R + 9216 + ws * 1024), "s"(L_R + 8192 + (ws & 1) * 9216), "v"(vo), "s"(rr), "s"(ri),
                        "s"(mkrsrc((ws & 1) ? p.wi : p.wr, 2048 * 514)), "s"(ws * 1024), "s"(8192) : "memory");
    }
    const int km0 = 16 * w + c16, km1 = km0 ? 256 - km0 : 128;
    unsigned ca = L_C + lane * 16, xa = w * 2048 + g * 256 + c16 * 4;
    unsigned ua = L_U + (2 * w) * 1072 + g * 272 + c16 * 16;
    PIN(ca); PIN(xa); PIN(ua);
    if (w >= 4) touch(in, 0);
    epoch_get(ep);
    BAR();
    if (w < 6) touch(in, (w & 1) ? 512 : 256 * 3072);
    Mat F = {lr128(ca + 6144), lr128(ca + 7168)};
    Cx tw1 = {lrf128(ca + 12288), lrf128(ca + 13312)};
    unsigned a0 = L_U + 4 * g * 1072 + ((km0 >> 2) & 3) * 272 + (km0 >> 4) * 16 + (km0 & 3) * 4;
    unsigned a1s = L_U + 4 * g * 1072 + ((km1 >> 2) & 3) * 272 + (km1 >> 4) * 16 + (km1 & 3) * 4;
    unsigned sw0 = L_S + w * 2304 + c16 * 144 + g * 16, sw1 = L_S + w * 2304 + jm * 144 + 64 + g * 16;
    unsigned sr0 = L_S + w * 2304 + r8 * 144 + ch * 16, tb1 = WS_T1 + me * TB + w * 2048 + lane * 16;
    u32x4 tg = tag_bits(ep, g), tl = tag_bits(ep, lane & 3);
    const int cj = (tid >> 3) & 15, cc = 16 * (tid >> 7) + 4 * (ch & 3);
    unsigned ra = L_R + ((ch & 4) ? ((16 - cj) & 15) * 514 + (256 - cc) * 2 : cj * 514 + cc * 2), rs = (ch & 4) ? -2 : 2;
    PIN(a0); PIN(a1s); PIN(sw0); PIN(sw1); PIN(sr0); PIN(tb1); PIN(tg); PIN(tl); PIN(ra); PIN(rs);
    if (w >= 4 && w < 6) VMWAIT(2); else VMWAIT(1);
    BAR();
    STAMP(1);
    auto pair = [](unsigned a) { return (unsigned)*LP(const unsigned short, a) | (unsigned)*LP(const unsigned short, a + 9216) << 16; };
    {
        u32x4 v0, v1;
#pragma unroll
        for (int k = 0; k < 4; ++k) { v0[k] = (pair(ra + k * rs) & ~TM) | tl[k]; v1[k] = (pair(ra + (64 + k) * rs) & ~TM) | tl[k]; }
        st128(rws, WS_T1 + (128 + me) * TB + tid * 16, v0); st128(rws, WS_T1 + (128 + me) * TB + 8192 + tid * 16, v1);
    }
    const u32x4 x0 = {lr32(xa), lr32(xa + 64), lr32(xa + 128), lr32(xa + 192)}, x1 = {lr32(xa + 1024), lr32(xa + 1088), lr32(xa + 1152), lr32(xa + 1216)};
    lw128(ua, pk4(dftA(twpk(dftA(x0, F), tw1), F))); lw128(ua + 1072, pk4(dftA(twpk(dftA(x1, F), tw1), F)));
    BAR();
    {
        const u32x4 d0 = {lr32(a0), lr32(a0 + 1072), lr32(a0 + 2144), lr32(a0 + 3216)};
        const u32x4 d1 = {lr32(a1s), lr32(a1s + 1072), lr32(a1s + 2144), lr32(a1s + 3216)};
        const Cx c0 = dftA(d0, F), c1 = dftA(d1, F);
        lw128(sw0, zipt(c0.re, c0.im, tg)); lw128(sw1, zipt(c1.re, -c1.im, tg));
        const u32x4 s0 = lr128(sr0), s1 = lr128(sr0 + 8 * 144);
        st128(rws, tb1, s0); st128(rws, tb1 + 1024, s1);
        if (tid < 32) {
            const unsigned r128 = pair(L_R + jm * 514 + 256);
            st128(rws, WS_T1 + me * TB + 16384 + jm * 32 + g * 16,
                  u32x4{g ? r128 & ~TM : pk(c0.re[0], c0.im[0]) & ~TM, pair(L_R + c16 * 514) & ~TM, pair(L_R + jm * 514 + 512) & ~TM, ep});
        }
    }
    STAMP(2);
    asm volatile("s_mov_b64 exec, 0xFFFF\n\tglobal_load_dword v255, %0, %1\n\ts_mov_b64 exec, -1" :: "v"(((li & 3) * 128 + w * 16 + c16) * 1024 + ct * 128), "s"(p.wo) : "memory", "v255");
    const rsrc_t ro = mkrsrc(p.wo, 512 * 512 * 2);
    const int lam = tid >> 3, s2a = (lam >> 5) * 32 + (lam & 3) * 8 + ((lam >> 2) & 7);
    const int t16 = (w >> 2) * 32 + (lane & 8) * 8 + (lane & 3) * 8 + (w & 3) * 2 + ((lane >> 2) & 1);
    const int pm = j1 ? 127 - s2a : (128 - s2a) & 127;
    unsigned wst = L_W + (ch & 3) * 16 + ((ch & 4) ? 10240 + pm * 80 : s2a * 80), wsd = L_W + (ch & 3) * 16 + ((ch & 4) ? 10240 + (pm ^ 64) * 80 : (s2a + 64) * 80);
    unsigned sb = L_U + ((ch & 3) * 2 + (ch >> 2)) * 1040 + ((lam >> 5) * 16 + ((lam >> 2) & 7)) * 16 + (lam & 3) * 4;
    unsigned tb2 = WS_T1 + s2a * TB + (kb * 16 + j1) * 128 + ch * 16;
    unsigned vo = (w * 64 + r8) * 1024 + ct * 128 + ((ch ^ (r8 & 6)) << 4);
    PIN(wst); PIN(wsd); PIN(sb); PIN(tb2); PIN(vo);
    const unsigned tb4 = WS_T1 + t16 * TB + 16384 + j1 * 32 + (g & 1) * 16;
    {
        u32x4 v0 = ld128s(rws, tb2, 128 * TB) ^ tl, v1 = ld128s(rws, tb2, 192 * TB) ^ tl;
#pragma unroll 1
        for (int it = 0; it < SPIN_CAP && stale(or4(or4(0, v0), v1)); ++it) {
            BACKOFF();
            v0 = ld128s(rws, tb2, 128 * TB) ^ tl; v1 = ld128s(rws, tb2, 192 * TB) ^ tl;
        }
        lw128(wst, v0); lw128(wsd, v1);
    }
    BAR();
    STAMP(3);
    u32x4 i0 = ld128(rws, tb2), i1 = ld128s(rws, tb2, 64 * TB), isp = {0, 0, 0, ep};
    if (kb == 0) isp = ld128(rws, tb4);
    FENCE();
    Mat F1 = {lr128(ca), lr128(ca + 1024)}, F2 = {lr128(ca + 2048), lr128(ca + 3072)}, G = {lr128(ca + 4096), lr128(ca + 5120)};
    Cx twa = {lrf128(ca + 8192), lrf128(ca + 9216)}, twb = {lrf128(ca + 10240), lrf128(ca + 11264)};
    unsigned ta = L_U + ((w >> 1) * 2 + 8 * (w & 1)) * 1040 + lane * 16;
    const u32x4 tw = tag_bits(ep, w >> 1, 1);
    unsigned t2 = (w & 1) ? ((lane & 8) ? tw[3] : tw[2]) : ((lane & 8) ? tw[1] : tw[0]);
    const int col = 2 * w + (g >> 1);
    const float th = (float)(16 * kb + col) * (1.f / 512);
    float sn = __builtin_amdgcn_sinf(th), cs = __builtin_amdgcn_cosf(th);
    unsigned wp = L_W + (c16 + 64 * (g & 1)) * 80 + col * 4;
    PIN(F1.re); PIN(F1.im); PIN(F2.re); PIN(F2.im); PIN(G.re); PIN(G.im); PIN(twa.re); PIN(twa.im); PIN(twb.re); PIN(twb.im);
    PIN(ta); PIN(t2); PIN(sn); PIN(cs); PIN(wp);
    Cx a1, bz, a2, bv;
    {
        f32x4 pr, pi, qr, qi;
#pragma unroll
        for (int r = 0; r < 4; ++r) {
            const unsigned pv = lr32(wp + r * 1280), qv = lr32(wp + r * 1280 + 10240);
            pr[r] = __builtin_bit_cast(float, pv << 16); pi[r] = __builtin_bit_cast(float, pv & 0xFFFF0000u);
            qr[r] = __builtin_bit_cast(float, qv << 16); qi[r] = __builtin_bit_cast(float, qv & 0xFFFF0000u);
        }
        const float sc = TAG_GAIN * TAG_GAIN * TAG_GAIN / 1048576;
        const f32x4 sr = (pr + qr) * sc, si = (pi - qi) * sc, dr = (pr - qr) * sc, di = (pi + qi) * sc;
        a1.re = sr - dr * sn; a1.im = si - di * sn; a2.re = sr + dr * sn; a2.im = si + di * sn;
        bz.re = dr * cs; bz.im = di * cs;
    }
    PIN(a1.re); PIN(a1.im); PIN(a2.re); PIN(a2.im); PIN(bz.re); PIN(bz.im);
    i0 ^= tl; i1 ^= tl;
#pragma unroll 1
    for (int it = 0; it < SPIN_CAP && (stale(or4(or4(0, i0), i1)) || __any(isp[3] != ep)); ++it) {
        BACKOFF();
        i0 = ld128(rws, tb2) ^ tl; i1 = ld128s(rws, tb2, 64 * TB) ^ tl;
        if (kb == 0) isp = ld128(rws, tb4);
    }
    STAMP(10);
    lw32(sb, i0[0]); lw32(sb + 128, i0[1]); lw32(sb + 8320, i0[2]); lw32(sb + 8320 + 128, i0[3]);
    lw32(sb + 512, i1[0]); lw32(sb + 512 + 128, i1[1]); lw32(sb + 512 + 8320, i1[2]); lw32(sb + 512 + 8320 + 128, i1[3]);
    if (kb == 0) { if (g == 0) lw32(L_SP + ((t16 >> 5) * 8 + (t16 & 7)) * 16 + ((t16 >> 3) & 3) * 4, isp[0]); lw128(L_PW + t16 * 32 + (g & 1) * 16, isp); }
    BAR();
    STAMP(4);
    auto wo2 = [&](int q) { dma(ro, vo, q * 8192, w * 8192 + q * 1024); dma(ro, vo, q * 8192 + 8192, w * 8192 + q * 1024 + 1024); };
    const u32x4 zi = lr128(ta), vi = lr128(ta + 1040);
    wo2(0);
    u32x4 zf = twpk(dftA(zi, F1), twa), vf = twpk(dftA(vi, F1), twa);
    PIN(zf); PIN(vf);
    wo2(2);
    {
        const Cx Z = dftB(zf, G), V = dftB(vf, G);
        Cx Vz = V;
        bv = bz;
        if (special) {
            u32x4 v0 = lr128(L_SP + (g * 8 + (c16 & 7)) * 16) ^ 0x80000000u;
            if (c16 >= 8) v0 = vi;
            Vz = dftB(twpk(dftA(v0, F1), twa), G);
            if (g < 2) {
                const float hs = 0.5f * TAG_GAIN * TAG_GAIN * TAG_GAIN / 1048576;
#pragma unroll
                for (int r = 0; r < 4; ++r) {
                    const int j2 = c16 + 64 * g + 16 * r;
                    const unsigned ea = lr32(L_PW + (j1 ? 127 - j2 : (128 - j2) & 127) * 32 + 4), eb = lr32(L_PW + j2 * 32 + 8), er = lr32(L_PW + j2 * 32 + 16);
                    const float ar = bf(ea), ai = bf(ea >> 16), br = bf(eb), bi = bf(eb >> 16);
                    a1.re[r] = 0.5f * a1.re[r] + hs * (ar + br); a1.im[r] = 0.5f * a1.im[r] + hs * (bi - ai);
                    bz.re[r] = 0.5f * bz.re[r] + hs * (ar - br); bz.im[r] = 0.5f * bz.im[r] - hs * (ai + bi);
                    a2.re[r] = 4.f * hs * bf(er); a2.im[r] = 4.f * hs * bf(er >> 16);
                    bv.re[r] = 0.f; bv.im[r] = 0.f;
                }
            }
        }
        u32x4 zc = zip(a1.re * Z.re - a1.im * Z.im - bz.re * Vz.im - bz.im * Vz.re, bz.im * Vz.im - a1.re * Z.im - a1.im * Z.re - bz.re * Vz.re);
        u32x4 vc = zip(a2.re * V.re - a2.im * V.im + bv.re * Z.im + bv.im * Z.re, bv.re * Z.re - a2.re * V.im - a2.im * V.re - bv.im * Z.im);
        PIN(zc); PIN(vc);
        wo2(4);
        u32x4 zt = twpk(dftA(zc, G), twb), vt = twpk(dftA(vc, G), twb);
        PIN(zt); PIN(vt);
        wo2(6);
        const Cx zo = dftB(zt, F2), vo2 = dftB(vt, F2);
        lw128(ta, zipt(zo.re, zo.im, u32x4(t2))); lw128(ta + 1040, zipt(vo2.re, -vo2.im, u32x4(t2)));
        BAR();
        const u32x4 o0 = {lr32(sb), lr32(sb + 128), lr32(sb + 8320), lr32(sb + 8320 + 128)};
        const u32x4 o1 = {lr32(sb + 512), lr32(sb + 512 + 128), lr32(sb + 512 + 8320), lr32(sb + 512 + 8320 + 128)};
        st128(rws, tb2, o0); __builtin_amdgcn_raw_buffer_store_b128(o1, rws, tb2, 64 * TB, 16);
    }
    STAMP(5);
    unsigned tb = WS_T1 + me * TB + ((2 * w + (c16 >> 3)) * 16 + 4 * g) * 128 + ch * 16;
    tl[0] ^= (lane & 3) ? 0u : TM;
    PIN(tb);
    VMWAIT(0);
    BAR();
    STAMP(6);
    if (X2POS == 0 && w < 4) { mid.l[0] = ld128(rws, tb); mid.l[1] = ld128(rws, tb + 128); mid.l[2] = ld128(rws, tb + 256); mid.l[3] = ld128(rws, tb + 384); }
    FENCE();
    mid.F = F; mid.tw1 = tw1; mid.tl = tl; mid.tb = tb;
}
DI void b_second(const Params& p, rsrc_t rws, unsigned ep, BMid& mid, int xcd, int li, int lane, int w, int rt STP_PARAM) {
    const int g = lane >> 4, c16 = lane & 15, ch = lane & 7, jm = (16 - c16) & 15, me = xcd * 16 + li;
    const Mat& F = mid.F; const Cx& tw1 = mid.tw1; const u32x4 tl = mid.tl;
    unsigned ra[4];
#pragma unroll
    for (int i = 0; i < 4; ++i) {
        const int km = 64 * g + 16 * i + c16, kq = km < 128 ? km : (256 - km) & 127;
        ra[i] = L_U + ((km >> 7) * 16 + 2 * w) * 592 + (kq + 16 * (kq >> 6)) * 4;
        PIN(ra[i]);
    }
    const int set = g & 1, k0 = 16 * (2 * w + (g >> 1));
    unsigned oo = (256 * w + me) * 2048 + 1024 + lane * 16, bp = ((lane & 3) * 16 + (lane >> 2)) * 4;
    unsigned u2 = L_U + (set * 16 + (set ? jm : c16)) * 592 + (k0 + 16 * (k0 >> 6)) * 4;
    PIN(oo); PIN(u2); PIN(bp);
    if (w < 4) {
        const unsigned tb = mid.tb;
        u32x4 l0, l1, l2, l3;
        if (X2POS == 0) { l0 = mid.l[0] ^ tl; l1 = mid.l[1] ^ tl; l2 = mid.l[2] ^ tl; l3 = mid.l[3] ^ tl; }
        else { l0 = ld128(rws, tb) ^ tl; l1 = ld128(rws, tb + 128) ^ tl; l2 = ld128(rws, tb + 256) ^ tl; l3 = ld128(rws, tb + 384) ^ tl; }
#pragma unroll 1
        for (int it = 0; it < SPIN_CAP && stale(or4(or4(or4(or4(0, l0), l1), l2), l3)); ++it) {
            BACKOFF();
            l0 = ld128(rws, tb) ^ tl; l1 = ld128(rws, tb + 128) ^ tl; l2 = ld128(rws, tb + 256) ^ tl; l3 = ld128(rws, tb + 384) ^ tl;
        }
        u32x4 t[4];
#pragma unroll
        for (int e = 0; e < 4; ++e) t[e] = pk4(dftA(u32x4{l0[e], l1[e], l2[e], l3[e]}, F));
#pragma unroll
        for (int r = 0; r < 4; ++r) lw128(u2 + r * 16, u32x4{t[0][r], t[1][r], t[2][r], t[3][r]});
    }
    BAR();
    STAMP(11);
    {
        const u32x4 d0 = {lr32(ra[0]), lr32(ra[1]), lr32(ra[2]), lr32(ra[3])};
        const u32x4 d1 = {lr32(ra[0] + 592), lr32(ra[1] + 592), lr32(ra[2] + 592), lr32(ra[3] + 592)};
        u32x4 y0 = pk4c(dftA(twpk(dftA(d0, F), tw1), F)), y1 = pk4c(dftA(twpk(dftA(d1, F), tw1), F));
#pragma unroll
        for (int r = 0; r < 4; ++r) {
            y0[r] = __builtin_amdgcn_ds_bpermute(bp, y0[r]); y1[r] = __builtin_amdgcn_ds_bpermute(bp, y1[r]);
        }
        PIN(y0); PIN(y1);
        mid.y0 = y0; mid.y1 = y1; mid.oo = oo;
        STAMP(13);
    }
}

__global__ void __launch_bounds__(512, 2) fused_kernel(Params p) {
    const int b = blockIdx.x, xcd = b & 7, i = b >> 3, li = i & 15;
    const int tid = threadIdx.x, lane = tid & 63, w = __builtin_amdgcn_readfirstlane(tid >> 6);
    BIn in;
    {
        size_t code;
        asm volatile("s_getpc_b64 %0" : "=s"(code));
        asm volatile("s_mov_b64 exec, 1\n\tglobal_load_dword v255, %0, %1\n\ts_mov_b64 exec, -1" :: "v"(min((unsigned)(w * 32 + i) * 128u, CODE_BYTES - 128u)), "s"(code & ~(size_t)255) : "memory", "v255");
    }
    __shared__ __attribute__((aligned(16))) char smem[LDS_BYTES];
    asm volatile("" :: "s"((unsigned)(size_t)(LAS char*)smem));
    STAMP(0);
    const int rp = (xcd >> 1) * 4 + (li & 3), hg = (xcd & 1) * 4 + (li >> 2), rt = xcd * 4 + (i & 3), ct = i >> 2;
    if (i < 16 && w >= 4) a_loader(p, rp, hg, li, lane, w - 4, ct STP_ARG);
    FENCE();
    if (i >= 16) b_loads(p, in, xcd, li, tid, lane, w);
    FENCE();
    const rsrc_t rws = mkrsrc(p.ws, WS_END);
    unsigned ep;
    epoch_ask(ep, p.ws);
    BMid mid;
    if (i >= 16) b_first(p, rws, ep, in, mid, xcd, li, tid, lane, w, ct STP_ARG);
    else if (w < 4) { epoch_get(ep); a_tile(rws, ep, rp, hg, lane, w STP_ARG); }
    else { a_loader_tail(p, rws, ep, rp, hg, lane, w - 4, ct STP_ARG); }
    const int g = lane >> 4, c16 = lane & 15, q = c16 >> 2, kh = w >> 2;
    unsigned ob = WS_O + (rt * 4 + (w & 3)) * 16384 + kh * 8192 + lane * 16;
    unsigned wa = kh * 32768 + (16 * (g >> 1) + 4 * (g & 1) + q) * 128 + 8 * (lane & 3) + (((2 * g + (q >> 1)) & 3) << 5);
    const unsigned scr = 81920;
    unsigned rw = scr + kh * 17408 + ((w & 3) * 16 + c16) * 272 + g * 16, rr = scr + (tid >> 3) * 272 + (tid & 7) * 32;
    unsigned oo = (rt * 64 + (tid >> 3)) * 2048 + ct * 128 + (tid & 7) * 16;
    u32x4 tl = tag_bits(ep, lane & 3);
    u32x4 o[8], wf[8][4];
#pragma unroll
    for (int k = 0; k < 8; ++k)
#pragma unroll
        for (int t = 0; t < 4; ++t) { const unsigned a = (wa ^ (t << 5)) + k * 4096; wf[k][t] = ltr2(a, a + 1024); }
    PIN(ob); PIN(rw); PIN(rr); PIN(oo); PIN(tl);
    SCHED_FENCE();
    if (i >= 16) b_second(p, rws, ep, mid, xcd, li, lane, w, rt STP_ARG);
    if (w == 7 && (HINTA || i >= 16)) {
#pragma unroll 1
        for (int it = 0; it < SPIN_CAP; ++it) {
            FENCE();
            if (__all(ld32(rws, WS_HINT + ((rt >> 1) * 8 + (lane & 7)) * 128) == ep)) break;
            __builtin_amdgcn_s_sleep(2);
        }
    }
    if (i < 16) VMWAIT(0);
    BAR();
    STAMP(7);
#pragma unroll
    for (int k = 0; k < 8; ++k) o[k] = ld128s(rws, ob, k * 1024);
    const rsrc_t rout = mkrsrc(p.out, 2048 * 1024 * 2);
    f32x4 acc[4] = {Z4, Z4, Z4, Z4};
    unsigned m = 0;
#pragma unroll
    for (int k = 0; k < 8; ++k) {
        o[k] ^= tl;
#pragma unroll
        for (int t = 0; t < 4; ++t) acc[t] = mma(wf[k][t], o[k], acc[t]);
        m = or4(m, o[k]);
        SCHED_FENCE();
    }
    if (__builtin_expect(stale(m), 0)) {
#pragma unroll 1
        for (int it = 0; it < SPIN_CAP; ++it) {
            BACKOFF();
            m = 0;
#pragma unroll
            for (int k = 0; k < 8; ++k) o[k] = ld128s(rws, ob, k * 1024) ^ tl;
#pragma unroll
            for (int k = 0; k < 8; ++k) m = or4(m, o[k]);
            if (!stale(m)) break;
        }
#pragma unroll
        for (int t = 0; t < 4; ++t) acc[t] = Z4;
#pragma unroll
        for (int k = 0; k < 8; ++k)
#pragma unroll
            for (int t = 0; t < 4; ++t) acc[t] = mma(wf[k][t], o[k], acc[t]);
    }
    STAMP(8);
    if (i >= 16) {
        __builtin_amdgcn_raw_buffer_store_b128(mid.y0, rout, mid.oo, 0, OUTB_AUX);
        __builtin_amdgcn_raw_buffer_store_b128(mid.y1, rout, mid.oo, 128 * 2048, OUTB_AUX);
    }
#pragma unroll
    for (int t = 0; t < 4; ++t) lwf128(rw + t * 64, acc[t]);
    BAR();
    const f32x4 y0 = lrf128(rr) + lrf128(rr + 17408), y1 = lrf128(rr + 16) + lrf128(rr + 17408 + 16);
    __builtin_amdgcn_raw_buffer_store_b128(cat(pkv(y0), pkv(y1)), rout, oo, 0, OUT_AUX);
    if (b == 128 && tid == 0) st32(rws, WS_EPOCH, ep);
    STAMP(9);
}

extern "C" void kernel_launch(void* const* d_in, const int* in_sizes, int n_in,
                              void* d_out, int out_size, void* d_ws, size_t ws_size,
                              hipStream_t stream) {
    Params p;
    p.ax = (const unsigned short*)d_in[0]; p.wqkv = (const unsigned short*)d_in[1]; p.wo = (const unsigned short*)d_in[2];
    p.bx = (const unsigned short*)d_in[3]; p.wr = (const unsigned short*)d_in[4]; p.wi = (const unsigned short*)d_in[5];
    p.out = (unsigned short*)d_out; p.ws = (unsigned char*)d_ws;
    fused_kernel<<<256, 512, 0, stream>>>(p);
}
```

```cpp
#define HINT 2
#include <hip/hip_runtime.h>
#include <stdint.h>

typedef short    s16x4 __attribute__((ext_vector_type(4)));
typedef __bf16   bf16x8 __attribute__((ext_vector_type(8)));
typedef __bf16   bf16x2 __attribute__((ext_vector_type(2)));
typedef float    f32x2 __attribute__((ext_vector_type(2)));
typedef float    f32x4 __attribute__((ext_vector_type(4)));
typedef float    f32x16 __attribute__((ext_vector_type(16)));
typedef unsigned u32x2 __attribute__((ext_vector_type(2)));
typedef unsigned u32x4 __attribute__((ext_vector_type(4)));
typedef __amdgpu_buffer_rsrc_t rsrc_t;
#define LAS __attribute__((address_space(3)))
#define DI __device__ __forceinline__

constexpr unsigned WS_EPOCH = 0;
constexpr unsigned WS_HINT = 4096;
constexpr unsigned PART_STRIDE = 2688, WS_PART = 32768;
constexpr unsigned TB = 17024;
constexpr unsigned WS_T1 = 1u << 19;
constexpr unsigned WS_O = WS_T1 + 256 * TB, WS_END = WS_O + (1u << 21);
constexpr int SPIN_CAP = 1 << 15;
#define RSLEEP 2
#define BACKOFF() do { __builtin_amdgcn_s_sleep(RSLEEP); FENCE(); } while (0)
constexpr unsigned LDS_BYTES = 163840;
#define OUTB_AUX 16
#define OUT_AUX 16
#define STAMP(i) do {} while (0)
#define STP_PARAM
#define STP_ARG

struct Params {
    const unsigned short *ax, *wqkv, *wo, *bx, *wr, *wi;
    unsigned short* out;
    unsigned char* ws;
};

DI rsrc_t mkrsrc(const void* ptr, unsigned bytes) { return __builtin_amdgcn_make_buffer_rsrc((void*)ptr, 0, bytes, 0x00020000); }
DI u32x4 ld128(rsrc_t r, unsigned off)  { return __builtin_amdgcn_raw_buffer_load_b128(r, off, 0, 16); }
DI u32x2 ld64(rsrc_t r, unsigned off)   { return __builtin_amdgcn_raw_buffer_load_b64(r, off, 0, 16); }
DI unsigned ld32(rsrc_t r, unsigned off){ return __builtin_amdgcn_raw_buffer_load_b32(r, off, 0, 16); }
DI u32x2 ld64s(rsrc_t r, unsigned off, unsigned so)   { return __builtin_amdgcn_raw_buffer_load_b64(r, off, so, 16); }
DI u32x4 ld128s(rsrc_t r, unsigned off, unsigned so)  { return __builtin_amdgcn_raw_buffer_load_b128(r, off, so, 16); }
DI unsigned ld32s(rsrc_t r, unsigned off, unsigned so){ return __builtin_amdgcn_raw_buffer_load_b32(r, off, so, 16); }
DI void st128(rsrc_t r, unsigned off, u32x4 v) { __builtin_amdgcn_raw_buffer_store_b128(v, r, off, 0, 16); }
DI void st64(rsrc_t r, unsigned off, u32x2 v) { __builtin_amdgcn_raw_buffer_store_b64(v, r, off, 0, 16); }
DI void st32(rsrc_t r, unsigned off, unsigned v) { __builtin_amdgcn_raw_buffer_store_b32(v, r, off, 0, 16); }
#define FENCE() asm volatile("" ::: "memory")
#define BAR() asm volatile("s_waitcnt lgkmcnt(0)\n\ts_barrier" ::: "memory")
#define VMWAIT(n) asm volatile("s_waitcnt vmcnt(" #n ")" ::: "memory")
#define PIN(x) asm volatile("" : "+v"(x))
#define SCHED_FENCE() __builtin_amdgcn_sched_barrier(0)
DI void dma(rsrc_t r, unsigned voff, unsigned soff, unsigned lds) {
    asm volatile("s_mov_b32 m0, %0\n\ts_nop 0\n\tbuffer_load_dwordx4 %1, %2, %3 offen lds"
                 :: "s"(lds), "v"(voff), "s"(r), "s"(soff) : "memory");
}
DI void dma4(rsrc_t r, unsigned v0, unsigned v1, unsigned v2, unsigned v3, unsigned soff, unsigned lds) {
    asm volatile("s_mov_b32 m0, %0\n\ts_nop 0\n\tbuffer_load_dwordx4 %1, %5, %6 offen lds\n\tbuffer_load_dwordx4 %2, %5, %6 offen offset:1024 lds\n\t"
                 "buffer_load_dwordx4 %3, %5, %6 offen offset:2048 lds\n\tbuffer_load_dwordx4 %4, %5, %6 offen offset:3072 lds"
                 :: "s"(lds), "v"(v0), "v"(v1), "v"(v2), "v"(v3), "s"(r), "s"(soff) : "memory");
}
DI void dma2(rsrc_t r, unsigned v0, unsigned v1, unsigned soff, unsigned lds) {
    asm volatile("s_mov_b32 m0, %0\n\ts_nop 0\n\tbuffer_load_dwordx4 %1, %3, %4 offen lds\n\tbuffer_load_dwordx4 %2, %3, %4 offen offset:1024 lds"
                 :: "s"(lds), "v"(v0), "v"(v1), "s"(r), "s"(soff) : "memory");
}
DI unsigned pk(float lo, float hi) { f32x2 v = {lo, hi}; return __builtin_bit_cast(unsigned, __builtin_convertvector(v, bf16x2)); }
DI u32x2 pkv(f32x4 v) { return u32x2{pk(v[0], v[1]), pk(v[2], v[3])}; }
DI u32x4 cat(u32x2 a, u32x2 b) { return u32x4{a[0], a[1], b[0], b[1]}; }
DI f32x4 mma(u32x4 a, u32x4 b, f32x4 c) {
    return __builtin_amdgcn_mfma_f32_16x16x32_bf16(__builtin_bit_cast(bf16x8, a), __builtin_bit_cast(bf16x8, b), c, 0, 0, 0); }
DI f32x16 mma32(u32x4 a, u32x4 b, f32x16 c) {
    return __builtin_amdgcn_mfma_f32_32x32x16_bf16(__builtin_bit_cast(bf16x8, a), __builtin_bit_cast(bf16x8, b), c, 0, 0, 0); }
DI f32x4 mma16(u32x2 a, u32x2 b, f32x4 c) {
    return __builtin_amdgcn_mfma_f32_16x16x16bf16_1k(__builtin_bit_cast(s16x4, a), __builtin_bit_cast(s16x4, b), c, 0, 0, 0); }
#define LP(T, a) ((LAS T*)(size_t)(a))
DI u32x4 lr128(unsigned a) { return *LP(const u32x4, a); }
DI f32x4 lrf128(unsigned a) { return *LP(const f32x4, a); }
DI u32x2 lr64(unsigned a) { return *LP(const u32x2, a); }
DI unsigned lr32(unsigned a) { return *LP(const unsigned, a); }
DI float bf(unsigned short v) { return __builtin_bit_cast(float, (unsigned)v << 16); }
DI float lrbf(unsigned a) { return bf(*LP(const unsigned short, a)); }
DI void lw128(unsigned a, u32x4 v) { *LP(u32x4, a) = v; }
DI void lwf128(unsigned a, f32x4 v) { *LP(f32x4, a) = v; }
DI void lw64(unsigned a, u32x2 v) { *LP(u32x2, a) = v; }
DI void lw32(unsigned a, unsigned v) { *LP(unsigned, a) = v; }
DI u32x2 ltr(unsigned a) { return __builtin_bit_cast(u32x2, __builtin_amdgcn_ds_read_tr16_b64_v4i16(LP(s16x4, a))); }
DI u32x4 ltr2(unsigned a, unsigned b) { return cat(ltr(a), ltr(b)); }
constexpr unsigned TM = 0x00010001u;
DI u32x4 tag_bits(unsigned ep, int unit, unsigned use = 2) {
    const unsigned t = ((ep << 2) | use) >> (8 * unit);
    u32x4 m;
#pragma unroll
    for (int k = 0; k < 4; ++k) m[k] = ((t >> (2 * k)) & 1u) | (((t >> (2 * k + 1)) & 1u) << 16);
    return m;
}
constexpr float TAG_GAIN = 1.0027f;
DI unsigned pkt1(float a, float b, unsigned bits) { return __builtin_amdgcn_bitop3_b32(pk(a, b), TM, bits, 0xBA); }
DI u32x4 zipt(f32x4 a, f32x4 b, u32x4 t) { return u32x4{pkt1(a[0], b[0], t[0]), pkt1(a[1], b[1], t[1]), pkt1(a[2], b[2], t[2]), pkt1(a[3], b[3], t[3])}; }
DI u32x4 catt(f32x4 a, f32x4 b, u32x4 t) { return u32x4{pkt1(a[0], a[1], t[0]), pkt1(a[2], a[3], t[1]), pkt1(b[0], b[1], t[2]), pkt1(b[2], b[3], t[3])}; }
DI unsigned or4(unsigned m, u32x4 v) { return m | v[0] | v[1] | v[2] | v[3]; }
DI bool stale(unsigned m) { return __any(m & TM); }
constexpr float PHI_EPS = 1e-6f;
DI u32x2 phi4(f32x4 u) {
    f32x4 a = u * 1.44269504f - PHI_EPS * 1.44269504f, e, m;
    PIN(a);
#pragma unroll
    for (int r = 0; r < 4; ++r) {
        e[r] = __builtin_amdgcn_fmed3f(__builtin_amdgcn_exp2f(a[r]), 0.f, 1.f);
        m[r] = __builtin_elementwise_maximum(u[r], PHI_EPS);
    }
    return pkv(m + e);
}
constexpr f32x4 Z4 = {0.f, 0.f, 0.f, 0.f};

struct Mat { u32x4 re, im; };
struct Cx  { f32x4 re, im; };
DI Cx angles(int n0, int dn, int mask, float inv) {
    Cx t;
#pragma unroll
    for (int r = 0; r < 4; ++r) { float a = (float)((n0 + r * dn) & mask) * inv;
        t.re[r] = __builtin_amdgcn_cosf(a); t.im[r] = __builtin_amdgcn_sinf(a); }
    return t;
}
DI u32x4 zip(f32x4 a, f32x4 b) { return u32x4{pk(a[0], b[0]), pk(a[1], b[1]), pk(a[2], b[2]), pk(a[3], b[3])}; }
DI Mat tomat(Cx t) { return Mat{zip(t.re, t.im), zip(-t.im, t.re)}; }
DI Cx dftA(u32x4 d, const Mat& f) { return Cx{mma(d, f.re, Z4), mma(d, f.im, Z4)}; }
DI Cx dftB(u32x4 d, const Mat& f) { return Cx{mma(f.re, d, Z4), mma(f.im, d, Z4)}; }
DI u32x4 twpk(Cx v, const Cx& w) { return zip(v.re * w.re + v.im * w.im, v.im * w.re - v.re * w.im); }
DI u32x4 pk4(Cx v)  { return zip(v.re, v.im); }
DI u32x4 pk4c(Cx v) { return zip(v.re, v.im) ^ 0x80000000u; }

DI void epoch_ask(unsigned& e, const unsigned char* ws) { asm volatile("s_load_dword %0, %1, 0x0" : "=s"(e) : "s"(ws) : "memory"); }
DI void epoch_get(unsigned& e) {
    unsigned r;
    asm volatile("s_waitcnt lgkmcnt(0)\n\ts_add_u32 %0, %1, 1" : "=s"(r) : "s"(e) : "memory", "scc");
    e = r;
}
DI f32x4 q4(const f32x16& v, int i) { return f32x4{v[4 * i], v[4 * i + 1], v[4 * i + 2], v[4 * i + 3]}; }
struct Frag { u32x4 a[2][2], b[2][3]; };
template <int J0, int J1> DI void frag_read(Frag& f, unsigned xa, unsigned wa) {
#pragma unroll
    for (int k = 0; k < 2; ++k) {
        f.a[k][0] = lr128(xa ^ (k * 32)); f.b[k][J0] = ltr2(wa + k * 10240 + J0 * 2048, wa + k * 10240 + J0 * 2048 + 512);
        f.a[k][1] = lr128((xa ^ (k * 32)) + 10240);
#pragma unroll
        for (int j = J0 + 1; j < J1; ++j) f.b[k][j] = ltr2(wa + k * 10240 + j * 2048, wa + k * 10240 + j * 2048 + 512);
    }
}
template <int J0, int J1> DI void frag_mma(const Frag& f, f32x16 (&acc)[2][3]) {
#pragma unroll
    for (int k = 0; k < 2; ++k)
#pragma unroll
        for (int j = J0; j < J1; ++j)
#pragma unroll
            for (int i = 0; i < 2; ++i) acc[i][j] = (j < 2) ? mma32(f.a[k][i], f.b[k][j], acc[i][j]) : mma32(f.b[k][j], f.a[k][i], acc[i][j]);
}
template <int NJ> DI void interleave() {
#pragma unroll
    for (int i = 0; i < 2 + 2 * NJ; ++i) { __builtin_amdgcn_sched_group_barrier(0x8, 1, 0); __builtin_amdgcn_sched_group_barrier(0x100, 2, 0); }
    __builtin_amdgcn_sched_group_barrier(0x8, 4, 0);
}
template <int J0, int J1> DI void tile_step(Frag& f0, Frag& f1, f32x16 (&acc)[2][3], unsigned xa, unsigned wa, int s, bool bar) {
    const unsigned slot = (s & 3) * 40960, nslot = ((s + 1) & 3) * 40960;
    SCHED_FENCE();
    frag_read<J0, J1>(f1, slot + (xa ^ 64), slot + wa + 20480); frag_mma<J0, J1>(f0, acc); interleave<J1 - J0>();
    SCHED_FENCE();
    if (bar) BAR();
    frag_read<J0, J1>(f0, nslot + xa, nslot + wa); frag_mma<J0, J1>(f1, acc); interleave<J1 - J0>();
}
constexpr unsigned CODE_BYTES = 23424;

constexpr unsigned SLOT = 40960;
constexpr unsigned A_RED = 65536;
DI void a_loader(const Params& p, int rp, int hg, int lane, int lw STP_PARAM) {
    const int r8 = lane >> 3, ch = lane & 7;
    const rsrc_t rx = mkrsrc(p.ax, 2048 * 512 * 2), rw = mkrsrc(p.wqkv, 512 * 1536 * 2);
    const unsigned vx = (rp * 128 + lw * 32 + r8) * 1024 + ((ch ^ (r8 >> 1)) << 4);
    const unsigned vw = (lw * 16 + r8) * 3072 + hg * 128 + ((ch ^ ((r8 & 2) << 1)) << 4);
    const unsigned vx1 = (vx ^ 64) + 7168, vx2 = vx + 14336, vx3 = (vx ^ 64) + 21504, vk = vw + 1024, vw1 = vw + 24576 - 1024;
#pragma unroll 1
    for (int s = 0; s < 8; ++s) {
        if (s == 0) STAMP(6);
        if (s >= 2) { if (s < 7) VMWAIT(10); else VMWAIT(8); asm volatile("s_barrier" ::: "memory"); }
        if (s == 2) STAMP(11);
        const unsigned so = s * (64 * 3072), l = (s & 3) * SLOT + lw * 10240;
        dma4(rx, vx, vx1, vx2, vx3, s * 128, l);
        dma4(rw, vk, vw + 24576, vw, vw1, so, l + 4096);
        if (s < 6) dma2(rw, vw, vw1, so, l + 8192);
    }
#pragma unroll 1
    for (int s = 6; s < 8; ++s) dma2(rw, vw, vw1, s * (64 * 3072), (s & 3) * SLOT + lw * 10240 + 8192);
}
DI u32x4 sel_matrix(int lane) {
    const int h = lane >> 5, r32 = lane & 31;
    u32x4 sel;
#pragma unroll
    for (int k = 0; k < 4; ++k)
        sel[k] = (r32 < 16 && ((r32 >> 2) & 1) == h && ((r32 >> 3) * 2 + ((r32 >> 1) & 1)) == k) ? ((r32 & 1) ? 0x3F800000u : 0x00003F80u) : 0u;
    return sel;
}
DI void sum_panels(rsrc_t rws, u32x4 (&d)[8], unsigned src, bool on, u32x4 tg, u32x4 sel, unsigned dst) {
    f32x16 sum = 0.f;
    unsigned m = 0;
#pragma unroll
    for (int j = 0; j < 8; ++j) { d[j] ^= tg; sum = mma32(sel, d[j], sum); m = or4(m, d[j]); SCHED_FENCE(); }
    lwf128(dst, q4(sum, 0)); lwf128(dst + 1024, q4(sum, 1));
    if (__builtin_expect(stale(m), 0)) {
#pragma unroll 1
        for (int it = 0; it < SPIN_CAP; ++it) {
            BACKOFF();
            m = 0;
            if (on) {
#pragma unroll
                for (int j = 0; j < 8; ++j) d[j] = ld128s(rws, src, j * 8 * PART_STRIDE) ^ tg;
            }
#pragma unroll
            for (int j = 0; j < 8; ++j) m = or4(m, d[j]);
            if (!stale(m)) break;
        }
        f32x16 s2 = 0.f;
#pragma unroll
        for (int j = 0; j < 8; ++j) s2 = mma32(sel, d[j], s2);
        lwf128(dst, q4(s2, 0)); lwf128(dst + 1024, q4(s2, 1));
    }
}
DI void a_loader_tail(const Params& p, rsrc_t rws, unsigned& ep, int hg, int lane, int lw, int ct STP_PARAM) {
    const int r8 = lane >> 3, ch = lane & 7;
    const rsrc_t ro = mkrsrc(p.wo, 512 * 512 * 2);
    const unsigned vo = (lw * 64 + r8) * 1024 + ct * 128 + ((ch ^ (r8 & 6)) << 4);
    VMWAIT(12);
#pragma unroll 1
    for (int u = 0; u < 2; ++u) {
        BAR();
#pragma unroll 1
        for (int q = 0; q < 8; ++q) dma(ro, vo, u * 262144 + q * 8192, u * 32768 + lw * 8192 + q * 1024);
        if (u == 0) VMWAIT(12);
    }
    VMWAIT(16);
    STAMP(1);
    BAR();
    epoch_get(ep);
    const int rh = lw >> 1, hp = lw & 1;
    unsigned src = WS_PART + hg * PART_STRIDE + rh * 64 * PART_STRIDE + 2048 + hp * 256 + ((lane >> 4) ^ 1) * 64 + (lane & 3) * 16;
    unsigned dst = A_RED + hp * 4096 + rh * 8192 + 2048 + lane * 16;
    u32x4 tg = tag_bits(ep, lane & 3), sel = sel_matrix(lane), d[8];
#pragma unroll
    for (int j = 0; j < 8; ++j) { d[j] = tg; PIN(d[j]); }
    PIN(src); PIN(dst); PIN(tg); PIN(sel);
    VMWAIT(0);
    BAR();
    __builtin_amdgcn_s_sleep(1);
    if ((lane & 15) < 4) {
#pragma unroll
        for (int j = 0; j < 8; ++j) d[j] = ld128s(rws, src, j * 8 * PART_STRIDE);
    }
    sum_panels(rws, d, src, (lane & 15) < 4, tg, sel, dst);
    BAR();
}
DI void a_tile(rsrc_t rws, unsigned ep, int rp, int hg, int lane, int w STP_PARAM) {
    const int g = lane >> 4, c16 = lane & 15, h = lane >> 5, r32 = lane & 31, rh = w >> 1, hp = w & 1;
    const unsigned xa = rh * 20480 + r32 * 128 + ((h ^ ((r32 >> 1) & 7)) << 4);
    const unsigned wa = 4096 + (8 * h + (c16 >> 2)) * 128 + ((hp ^ (c16 >> 3)) << 6) + (g & 1) * 32 + 8 * (lane & 3);
    f32x16 acc[2][3];
#pragma unroll
    for (int i = 0; i < 2; ++i)
#pragma unroll
        for (int j = 0; j < 3; ++j) { acc[i][j] = j == 1 ? 0.f : PHI_EPS; PIN(acc[i][j]); }
    const unsigned pg = WS_PART + hg * PART_STRIDE + hp * 1024;
    unsigned red = A_RED + hp * 4096 + lane * 16;
    unsigned pst = pg + rp * 8 * PART_STRIDE + (rh ? 2048 - hp * 768 + g * 64 + c16 * 16 : lane * 16);
    if (rh && c16 >= 4) pst = WS_END;
    unsigned pk0 = pg + rh * 64 * PART_STRIDE + lane * 16;
    unsigned ob = WS_O + ((rp * 8 + rh * 4 + (g & 1)) * 16 + hg * 2 + hp) * 1024 + (h * 16 + c16) * 16;
    u32x4 tg = tag_bits(ep, lane & 3), sel = sel_matrix(lane);
    PIN(red); PIN(pst); PIN(pk0); PIN(ob); PIN(tg); PIN(sel);
    Frag f0, f1;
    BAR();
    frag_read<0, 3>(f0, xa, wa);
#pragma unroll 1
    for (int s = 0; s < 6; ++s) tile_step<0, 3>(f0, f1, acc, xa, wa, s, true);
    tile_step<0, 2>(f0, f1, acc, xa, wa, 6, true);
    SCHED_FENCE();
    frag_read<0, 2>(f1, 3 * SLOT + (xa ^ 64), 3 * SLOT + wa + 20480); frag_mma<0, 1>(f0, acc); interleave<1>();
    SCHED_FENCE();
    frag_mma<0, 1>(f1, acc);
    SCHED_FENCE();
    STAMP(1);
    {
        frag_mma<1, 2>(f0, acc); frag_mma<1, 2>(f1, acc);
        u32x4 A = cat(phi4(q4(acc[0][0], 0)), phi4(q4(acc[0][0], 1)));
#pragma unroll
        for (int i = 0; i < 8; ++i) { __builtin_amdgcn_sched_group_barrier(0x8, 1, 0); __builtin_amdgcn_sched_group_barrier(0x402, 5, 0); }
        SCHED_FENCE();
        f32x16 kv = 0.f, zz = 0.f;
        const u32x4 ones = {0x3F803F80u, 0x3F803F80u, 0x3F803F80u, 0x3F803F80u};
#pragma unroll
        for (int n = 0; n < 4; ++n) {
            zz = mma32(A, ones, zz);
            kv = mma32(A, cat(pkv(q4(acc[n >> 1][1], 2 * (n & 1))), pkv(q4(acc[n >> 1][1], 2 * (n & 1) + 1))), kv);
            if (n < 3) A = cat(phi4(q4(acc[(n + 1) >> 1][0], 2 * ((n + 1) & 1))), phi4(q4(acc[(n + 1) >> 1][0], 2 * ((n + 1) & 1) + 1)));
            SCHED_FENCE();
        }
        f32x4 k0 = (g & 1) ? q4(kv, 2) : q4(kv, 0), k1 = (g & 1) ? q4(kv, 3) : q4(kv, 1);
        f32x4 z0 = (g & 1) ? q4(zz, 2) : q4(zz, 0), z1 = (g & 1) ? q4(zz, 3) : q4(zz, 1);
        if (rh) { lwf128(red, k0); lwf128(red + 1024, k1); k0 = z0; k1 = z1; } else { lwf128(red + 2048, z0); lwf128(red + 3072, z1); }
        BAR();
        st128(rws, pst, catt(k0 + lrf128(red + rh * 2048), k1 + lrf128(red + rh * 2048 + 1024), tg));
    }
    STAMP(2);
    SCHED_FENCE();
    frag_read<2, 3>(f0, 2 * SLOT + xa, 2 * SLOT + wa);
#pragma unroll 1
    for (int s = 6; s < 8; ++s) tile_step<2, 3>(f0, f1, acc, xa, wa, s, false);
    SCHED_FENCE();
    VMWAIT(0);
    BAR();
    STAMP(3);
    u32x4 dk[8];
#pragma unroll
    for (int j = 0; j < 8; ++j) dk[j] = ld128s(rws, pk0, j * 8 * PART_STRIDE);
    SCHED_FENCE();
    u32x4 pq[2][2];
#pragma unroll
    for (int i = 0; i < 2; ++i)
#pragma unroll
        for (int b = 0; b < 2; ++b) { pq[i][b] = cat(phi4(q4(acc[i][2], 2 * b)), phi4(q4(acc[i][2], 2 * b + 1))); PIN(pq[i][b]); }
    SCHED_FENCE();
    sum_panels(rws, dk, pk0, true, tg, sel, red + 8192 * rh);
    BAR();
    if (HINT == 1 && w == 0 && lane == 0) st32(rws, WS_HINT + (rp * 8 + hg) * 128, ep);
    STAMP(4);
    const u32x4 KV = cat(pkv(lrf128(red) + lrf128(red + 8192)), pkv(lrf128(red + 1024) + lrf128(red + 9216)));
    u32x4 ZZ = cat(pkv(lrf128(red + 2048) + lrf128(red + 10240)), pkv(lrf128(red + 3072) + lrf128(red + 11264)));
#pragma unroll
    for (int k = 0; k < 4; ++k) ZZ[k] = __builtin_amdgcn_update_dpp(ZZ[k], ZZ[k], 0x114, 0xF, 0x2, false);
#pragma unroll
    for (int b = 0; b < 2; ++b) {
        const u32x4 A = ((g & 1) == b) ? KV : ZZ;
#pragma unroll
        for (int i = 0; i < 2; ++i) {
            const f32x16 nd = mma32(A, pq[i][b], 0.f);
            const float ri = TAG_GAIN * __builtin_amdgcn_rcpf(nd[b ? 0 : 8] + 1e-6f);
            st128(rws, ob + i * 32768 + b * 512, catt(q4(nd, 2 * b) * ri, q4(nd, 2 * b + 1) * ri, tg));
        }
    }
    if (HINT == 2 && w == 0 && lane == 0) st32(rws, WS_HINT + (rp * 8 + hg) * 128, ep);
    STAMP(5);
}

constexpr unsigned L_U = 98304;
constexpr unsigned L_S = 65536;
constexpr unsigned L_W = L_U + 19200;
constexpr unsigned L_SP = L_W + 20480;
constexpr unsigned L_C = L_SP + 512;
constexpr unsigned L_PW = L_C + 14336;
constexpr unsigned L_R = 16384;
struct BIn { const char* pf; size_t pm; unsigned po; };
DI void touch(const BIn& in, unsigned add) {
    asm volatile("s_mov_b64 exec, %2\n\tglobal_load_dword v255, %0, %1\n\ts_mov_b64 exec, -1" :: "v"(in.po), "s"(in.pf + add), "s"(in.pm) : "memory", "v255");
}
DI void b_loads(const Params& p, BIn& in, int xcd, int li, int tid, int lane, int w) {
    const int g = lane >> 4, c16 = lane & 15;
    in.pf = (w & 1) ? (const char*)p.ax : (const char*)p.wqkv;
    in.pm = (w & 1) ? 0xFFFFFFFFull : 0x0FFF0FFF0FFF0FFFull;
    in.po = (w & 1) ? ((xcd >> 1) * 512 + li * 32 + (lane & 31)) * 1024 + (1 + (w >> 1)) * 128
                    : (64 + 32 * w + li * 4 + g) * 3072 + (xcd & 1) * 512 + (c16 >> 2) * 1024 + (c16 & 3) * 128;
    if (w < 2) touch(in, 0);
}
#define BXP 2
struct BMid { Mat F; Cx tw1; u32x4 y0, y1; unsigned oo; };
DI void b_first(const Params& p, rsrc_t rws, unsigned& ep, BIn& in, BMid& mid, int xcd, int li, int tid, int lane, int w, int ct STP_PARAM) {
    const int g = lane >> 4, c16 = lane & 15;
    const int me = xcd * 16 + li;
    const int j1 = (li >> 3) ? (xcd ? 16 - xcd : 8) : xcd, kb = li & 7;
    const int r8 = lane >> 3, ch = lane & 7, jm = (16 - c16) & 15;
    const bool special = (kb == 0 && w == 0);
    auto rows = [&]()
    {
        const unsigned ws = __builtin_amdgcn_readfirstlane(w), vo = me * 8224 + lane * 16;
        asm volatile("s_mov_b32 m0, %0\n\ts_nop 0\n\tbuffer_load_dwordx4 %1, %3, 0 offen nt lds\n\tbuffer_load_dwordx4 %2, %3, 0 offen offset:1024 nt lds"
                     :: "s"(ws * 2048), "v"((256 * w + me) * 1024 + lane * 16), "v"((256 * w + me + 127) * 1024 + lane * 16), "s"(mkrsrc(p.bx, 2048 * 512 * 2)) : "memory");
        const rsrc_t rr = mkrsrc(p.wr, 2048 * 514), ri = mkrsrc(p.wi, 2048 * 514);
        asm volatile("s_mov_b32 m0, %0\n\ts_nop 0\n\tbuffer_load_dwordx4 %3, %4, %7 offen nt lds\n\t"
                     "s_mov_b32 m0, %1\n\ts_nop 0\n\tbuffer_load_dwordx4 %3, %5, %7 offen nt lds\n\t"
                     "s_mov_b32 m0, %2\n\ts_mov_b64 exec, 3\n\tbuffer_load_dwordx4 %3, %6, %8 offen nt lds\n\ts_mov_b64 exec, -1"
                     :: "s"(L_R + ws * 1024), "s"(L_R + 9216 + ws * 1024), "s"(L_R + 8192 + (ws & 1) * 9216), "v"(vo), "s"(rr), "s"(ri),
                        "s"(mkrsrc((ws & 1) ? p.wi : p.wr, 2048 * 514)), "s"(ws * 1024), "s"(8192) : "memory");
    };
    if ((w >> 1) == 1) touch(in, 0);
    if (BXP == 1) rows();
    if (w < 7) {
        const int k4 = 4 * w, sU = (0x0040004 >> k4) & 15, sQ = (0x0400040 >> k4) & 15;
        const int P = (((c16 & ((0xF7FF7FF >> k4) & 15)) << sU) + (sU ? j1 : 0)) << ((0x3007833 >> k4) & 15);
        const int n0 = P * (((4 * (g & ((0x3313133 >> k4) & 15))) << sQ) + (sQ ? j1 : 0)), dn = P << sQ;
        const Cx t = angles(n0, dn, 2047, 1.f / 2048);
        Mat m = Mat{__builtin_bit_cast(u32x4, t.re), __builtin_bit_cast(u32x4, t.im)};
        if (w < 4) m = tomat(t);
        if (w == 2 && (g >> 1) != (c16 >> 3)) m = Mat{u32x4{0, 0, 0, 0}, u32x4{0, 0, 0, 0}};
        lw128(L_C + w * 2048 + lane * 16, m.re); lw128(L_C + w * 2048 + 1024 + lane * 16, m.im);
    }
    if (BXP == 2) rows();
    const int km0 = 16 * w + c16, km1 = km0 ? 256 - km0 : 128;
    unsigned ca = L_C + lane * 16, xa = w * 2048 + g * 256 + c16 * 4;
    unsigned ua = L_U + (2 * w) * 1072 + g * 272 + c16 * 16;
    PIN(ca); PIN(xa); PIN(ua);
    if (w >= 4) touch(in, 0);
    epoch_get(ep);
    BAR();
    if (w < 6) touch(in, (w & 1) ? 512 : 256 * 3072);
    Mat F = {lr128(ca + 6144), lr128(ca + 7168)};
    Cx tw1 = {lrf128(ca + 12288), lrf128(ca + 13312)};
    unsigned a0 = L_U + 4 * g * 1072 + ((km0 >> 2) & 3) * 272 + (km0 >> 4) * 16 + (km0 & 3) * 4;
    unsigned a1s = L_U + 4 * g * 1072 + ((km1 >> 2) & 3) * 272 + (km1 >> 4) * 16 + (km1 & 3) * 4;
    unsigned sw0 = L_S + w * 2304 + c16 * 144 + g * 16, sw1 = L_S + w * 2304 + jm * 144 + 64 + g * 16;
    unsigned sr0 = L_S + w * 2304 + r8 * 144 + ch * 16, tb1 = WS_T1 + me * TB + w * 2048 + lane * 16;
    u32x4 tg = tag_bits(ep, g), tl = tag_bits(ep, lane & 3);
    const int cj = (tid >> 3) & 15, cc = 16 * (tid >> 7) + 4 * (ch & 3);
    unsigned ra = L_R + ((ch & 4) ? ((16 - cj) & 15) * 514 + (256 - cc) * 2 : cj * 514 + cc * 2), rs = (ch & 4) ? -2 : 2;
    const int lam = tid >> 3, s2a = (lam >> 5) * 32 + (lam & 3) * 8 + ((lam >> 2) & 7);
    unsigned tb2 = WS_T1 + s2a * TB + (kb * 16 + j1) * 128 + ch * 16;
    PIN(tb2); PIN(a0); PIN(a1s); PIN(sw0); PIN(sw1); PIN(sr0); PIN(tb1); PIN(tg); PIN(tl); PIN(ra); PIN(rs);
    if (w >= 4 && w < 6) VMWAIT(2); else VMWAIT(1);
    BAR();
    STAMP(1);
    auto pair = [](unsigned a) { return (unsigned)*LP(const unsigned short, a) | (unsigned)*LP(const unsigned short, a + 9216) << 16; };
    {
        u32x4 v0, v1;
#pragma unroll
        for (int k = 0; k < 4; ++k) { v0[k] = (pair(ra + k * rs) & ~TM) | tl[k]; v1[k] = (pair(ra + (64 + k) * rs) & ~TM) | tl[k]; }
        st128(rws, WS_T1 + (128 + me) * TB + tid * 16, v0); st128(rws, WS_T1 + (128 + me) * TB + 8192 + tid * 16, v1);
    }
    const u32x4 x0 = {lr32(xa), lr32(xa + 64), lr32(xa + 128), lr32(xa + 192)}, x1 = {lr32(xa + 1024), lr32(xa + 1088), lr32(xa + 1152), lr32(xa + 1216)};
    lw128(ua, pk4(dftA(twpk(dftA(x0, F), tw1), F))); lw128(ua + 1072, pk4(dftA(twpk(dftA(x1, F), tw1), F)));
    BAR();
    {
        const u32x4 d0 = {lr32(a0), lr32(a0 + 1072), lr32(a0 + 2144), lr32(a0 + 3216)};
        const u32x4 d1 = {lr32(a1s), lr32(a1s + 1072), lr32(a1s + 2144), lr32(a1s + 3216)};
        const Cx c0 = dftA(d0, F), c1 = dftA(d1, F);
        lw128(sw0, zipt(c0.re, c0.im, tg)); lw128(sw1, zipt(c1.re, -c1.im, tg));
        const u32x4 s0 = lr128(sr0), s1 = lr128(sr0 + 8 * 144);
        st128(rws, tb1, s0); st128(rws, tb1 + 1024, s1);
        if (tid < 32) {
            const unsigned r128 = pair(L_R + jm * 514 + 256);
            st128(rws, WS_T1 + me * TB + 16384 + jm * 32 + g * 16,
                  u32x4{g ? r128 & ~TM : pk(c0.re[0], c0.im[0]) & ~TM, pair(L_R + c16 * 514) & ~TM, pair(L_R + jm * 514 + 512) & ~TM, ep});
        }
    }
    STAMP(2);
    asm volatile("s_mov_b64 exec, 0xFFFF\n\tglobal_load_dword v255, %0, %1\n\ts_mov_b64 exec, -1" :: "v"(((li & 3) * 128 + w * 16 + c16) * 1024 + ct * 128), "s"(p.wo) : "memory", "v255");
    u32x4 v0 = ld128s(rws, tb2, 128 * TB), v1 = ld128s(rws, tb2, 192 * TB);
    FENCE();
    const rsrc_t ro = mkrsrc(p.wo, 512 * 512 * 2);
    const int t16 = (w >> 2) * 32 + (lane & 8) * 8 + (lane & 3) * 8 + (w & 3) * 2 + ((lane >> 2) & 1);
    const int pm = j1 ? 127 - s2a : (128 - s2a) & 127;
    unsigned wst = L_W + (ch & 3) * 16 + ((ch & 4) ? 10240 + pm * 80 : s2a * 80), wsd = L_W + (ch & 3) * 16 + ((ch & 4) ? 10240 + (pm ^ 64) * 80 : (s2a + 64) * 80);
    unsigned sb = L_U + ((ch & 3) * 2 + (ch >> 2)) * 1040 + ((lam >> 5) * 16 + ((lam >> 2) & 7)) * 16 + (lam & 3) * 4;
    unsigned vo = (w * 64 + r8) * 1024 + ct * 128 + ((ch ^ (r8 & 6)) << 4);
    unsigned vo1 = vo + 7168, vo2 = vo + 14336, vo3 = vo + 21504, tb4 = WS_T1 + t16 * TB + 16384 + j1 * 32 + (g & 1) * 16;
    PIN(wst); PIN(wsd); PIN(sb); PIN(vo); PIN(vo1); PIN(vo2); PIN(vo3); PIN(tb4);
    Mat F1 = {lr128(ca), lr128(ca + 1024)}, F2 = {lr128(ca + 2048), lr128(ca + 3072)}, G = {lr128(ca + 4096), lr128(ca + 5120)};
    Cx twa = {lrf128(ca + 8192), lrf128(ca + 9216)}, twb = {lrf128(ca + 10240), lrf128(ca + 11264)};
    unsigned ta = L_U + ((w >> 1) * 2 + 8 * (w & 1)) * 1040 + lane * 16;
    const u32x4 tw = tag_bits(ep, w >> 1, 1);
    unsigned t2 = (w & 1) ? ((lane & 8) ? tw[3] : tw[2]) : ((lane & 8) ? tw[1] : tw[0]);
    const int col = 2 * w + (g >> 1);
    const float th = (float)(16 * kb + col) * (1.f / 512);
    float sn = __builtin_amdgcn_sinf(th), cs = __builtin_amdgcn_cosf(th);
    unsigned wp = L_W + (c16 + 64 * (g & 1)) * 80 + col * 4;
    PIN(F1.re); PIN(F1.im); PIN(F2.re); PIN(F2.im); PIN(G.re); PIN(G.im); PIN(twa.re); PIN(twa.im); PIN(twb.re); PIN(twb.im);
    PIN(ta); PIN(t2); PIN(sn); PIN(cs); PIN(wp);
    {
        v0 ^= tl; v1 ^= tl;
#pragma unroll 1
        for (int it = 0; it < SPIN_CAP && stale(or4(or4(0, v0), v1)); ++it) {
            BACKOFF();
            v0 = ld128s(rws, tb2, 128 * TB) ^ tl; v1 = ld128s(rws, tb2, 192 * TB) ^ tl;
        }
        lw128(wst, v0); lw128(wsd, v1);
    }
    BAR();
    STAMP(3);
    u32x4 i0 = ld128(rws, tb2), i1 = ld128s(rws, tb2, 64 * TB), isp = {0, 0, 0, ep};
    if (kb == 0) isp = ld128(rws, tb4);
    FENCE();
    Cx a1, bz, a2, bv;
    {
        f32x4 pr, pi, qr, qi;
#pragma unroll
        for (int r = 0; r < 4; ++r) {
            const unsigned pv = lr32(wp + r * 1280), qv = lr32(wp + r * 1280 + 10240);
            pr[r] = __builtin_bit_cast(float, pv << 16); pi[r] = __builtin_bit_cast(float, pv & 0xFFFF0000u);
            qr[r] = __builtin_bit_cast(float, qv << 16); qi[r] = __builtin_bit_cast(float, qv & 0xFFFF0000u);
        }
        const float sc = TAG_GAIN * TAG_GAIN * TAG_GAIN / 1048576;
        const f32x4 sr = (pr + qr) * sc, si = (pi - qi) * sc, dr = (pr - qr) * sc, di = (pi + qi) * sc;
        a1.re = sr - dr * sn; a1.im = si - di * sn; a2.re = sr + dr * sn; a2.im = si + di * sn;
        bz.re = dr * cs; bz.im = di * cs;
    }
    PIN(a1.re); PIN(a1.im); PIN(a2.re); PIN(a2.im); PIN(bz.re); PIN(bz.im);
    i0 ^= tl; i1 ^= tl;
#pragma unroll 1
    for (int it = 0; it < SPIN_CAP && (stale(or4(or4(0, i0), i1)) || __any(isp[3] != ep)); ++it) {
        BACKOFF();
        i0 = ld128(rws, tb2) ^ tl; i1 = ld128s(rws, tb2, 64 * TB) ^ tl;
        if (kb == 0) isp = ld128(rws, tb4);
    }
    STAMP(10);
    lw32(sb, i0[0]); lw32(sb + 128, i0[1]); lw32(sb + 8320, i0[2]); lw32(sb + 8320 + 128, i0[3]);
    lw32(sb + 512, i1[0]); lw32(sb + 512 + 128, i1[1]); lw32(sb + 512 + 8320, i1[2]); lw32(sb + 512 + 8320 + 128, i1[3]);
    if (kb == 0) { if (g == 0) lw32(L_SP + ((t16 >> 5) * 8 + (t16 & 7)) * 16 + ((t16 >> 3) & 3) * 4, isp[0]); lw128(L_PW + t16 * 32 + (g & 1) * 16, isp); }
    BAR();
    STAMP(4);
    const u32x4 zi = lr128(ta), vi = lr128(ta + 1040);
    dma4(ro, vo, vo1, vo2, vo3, 0, w * 8192);
    u32x4 zf = twpk(dftA(zi, F1), twa), vf = twpk(dftA(vi, F1), twa);
    PIN(zf); PIN(vf);
    dma4(ro, vo, vo1, vo2, vo3, 32768, w * 8192 + 4096);
    {
        const Cx Z = dftB(zf, G), V = dftB(vf, G);
        Cx Vz = V;
        bv = bz;
        if (special) {
            u32x4 vp = lr128(L_SP + (g * 8 + (c16 & 7)) * 16) ^ 0x80000000u;
            if (c16 >= 8) vp = vi;
            Vz = dftB(twpk(dftA(vp, F1), twa), G);
            if (g < 2) {
                const float hs = 0.5f * TAG_GAIN * TAG_GAIN * TAG_GAIN / 1048576;
#pragma unroll
                for (int r = 0; r < 4; ++r) {
                    const int j2 = c16 + 64 * g + 16 * r;
                    const unsigned ea = lr32(L_PW + (j1 ? 127 - j2 : (128 - j2) & 127) * 32 + 4), eb = lr32(L_PW + j2 * 32 + 8), er = lr32(L_PW + j2 * 32 + 16);
                    const float ar = bf(ea), ai = bf(ea >> 16), br = bf(eb), bi = bf(eb >> 16);
                    a1.re[r] = 0.5f * a1.re[r] + hs * (ar + br); a1.im[r] = 0.5f * a1.im[r] + hs * (bi - ai);
                    bz.re[r] = 0.5f * bz.re[r] + hs * (ar - br); bz.im[r] = 0.5f * bz.im[r] - hs * (ai + bi);
                    a2.re[r] = 4.f * hs * bf(er); a2.im[r] = 4.f * hs * bf(er >> 16);
                    bv.re[r] = 0.f; bv.im[r] = 0.f;
                }
            }
        }
        u32x4 zc = zip(a1.re * Z.re - a1.im * Z.im - bz.re * Vz.im - bz.im * Vz.re, bz.im * Vz.im - a1.re * Z.im - a1.im * Z.re - bz.re * Vz.re);
        u32x4 vc = zip(a2.re * V.re - a2.im * V.im + bv.re * Z.im + bv.im * Z.re, bv.re * Z.re - a2.re * V.im - a2.im * V.re - bv.im * Z.im);
        const Cx zo = dftB(twpk(dftA(zc, G), twb), F2), vb = dftB(twpk(dftA(vc, G), twb), F2);
        lw128(ta, zipt(zo.re, zo.im, u32x4(t2))); lw128(ta + 1040, zipt(vb.re, -vb.im, u32x4(t2)));
        VMWAIT(0);
        BAR();
        const u32x4 o0 = {lr32(sb), lr32(sb + 128), lr32(sb + 8320), lr32(sb + 8320 + 128)};
        const u32x4 o1 = {lr32(sb + 512), lr32(sb + 512 + 128), lr32(sb + 512 + 8320), lr32(sb + 512 + 8320 + 128)};
        st128(rws, tb2, o0); __builtin_amdgcn_raw_buffer_store_b128(o1, rws, tb2, 64 * TB, 16);
    }
    STAMP(5);
    mid.F = F; mid.tw1 = tw1;
    if (w >= 4) { VMWAIT(0); BAR(); }
}
DI void b_second(const Params& p, rsrc_t rws, unsigned ep, BMid& mid, unsigned& hint, unsigned ha, int xcd, int li, int lane, int w STP_PARAM) {
    const int g = lane >> 4, c16 = lane & 15, ch = lane & 7, jm = (16 - c16) & 15, me = xcd * 16 + li;
    const Mat& F = mid.F; const Cx& tw1 = mid.tw1;
    u32x4 tl = tag_bits(ep, lane & 3, 1);
    unsigned ra[4];
#pragma unroll
    for (int i = 0; i < 4; ++i) {
        const int km = 64 * g + 16 * i + c16, kq = km < 128 ? km : (256 - km) & 127;
        ra[i] = L_U + ((km >> 7) * 16 + 2 * w) * 592 + (kq + 16 * (kq >> 6)) * 4;
        PIN(ra[i]);
    }
    const int set = g & 1, k0 = 16 * (2 * w + (g >> 1));
    unsigned oo = (256 * w + me) * 2048 + 1024 + lane * 16, bp = ((lane & 3) * 16 + (lane >> 2)) * 4;
    unsigned u2 = L_U + (set * 16 + (set ? jm : c16)) * 592 + (k0 + 16 * (k0 >> 6)) * 4;
    unsigned tb = WS_T1 + me * TB + ((2 * w + (c16 >> 3)) * 16 + 4 * g) * 128 + ch * 16;
    PIN(oo); PIN(u2); PIN(bp); PIN(tb); PIN(tl);
    if (w < 4) {
        VMWAIT(0);
        BAR();
        STAMP(6);
        u32x4 l0 = ld128(rws, tb) ^ tl, l1 = ld128(rws, tb + 128) ^ tl, l2 = ld128(rws, tb + 256) ^ tl, l3 = ld128(rws, tb + 384) ^ tl;
#pragma unroll 1
        for (int it = 0; it < SPIN_CAP && stale(or4(or4(or4(or4(0, l0), l1), l2), l3)); ++it) {
            BACKOFF();
            l0 = ld128(rws, tb) ^ tl; l1 = ld128(rws, tb + 128) ^ tl; l2 = ld128(rws, tb + 256) ^ tl; l3 = ld128(rws, tb + 384) ^ tl;
        }
        u32x4 t[4];
#pragma unroll
        for (int e = 0; e < 4; ++e) t[e] = pk4(dftA(u32x4{l0[e], l1[e], l2[e], l3[e]}, F));
#pragma unroll
        for (int r = 0; r < 4; ++r) lw128(u2 + r * 16, u32x4{t[0][r], t[1][r], t[2][r], t[3][r]});
    }
    BAR();
    STAMP(11);
    if (w == 7) hint = ld32(rws, ha);
    FENCE();
    {
        const u32x4 d0 = {lr32(ra[0]), lr32(ra[1]), lr32(ra[2]), lr32(ra[3])};
        const u32x4 d1 = {lr32(ra[0] + 592), lr32(ra[1] + 592), lr32(ra[2] + 592), lr32(ra[3] + 592)};
        u32x4 y0 = pk4c(dftA(twpk(dftA(d0, F), tw1), F)), y1 = pk4c(dftA(twpk(dftA(d1, F), tw1), F));
#pragma unroll
        for (int r = 0; r < 4; ++r) {
            y0[r] = __builtin_amdgcn_ds_bpermute(bp, y0[r]); y1[r] = __builtin_amdgcn_ds_bpermute(bp, y1[r]);
        }
        PIN(y0); PIN(y1);
        mid.y0 = y0; mid.y1 = y1; mid.oo = oo;
        STAMP(13);
    }
}

__global__ void __launch_bounds__(512, 2) fused_kernel(Params p) {
    const int b = blockIdx.x, xcd = b & 7, i = b >> 3, li = i & 15;
    const int tid = threadIdx.x, lane = tid & 63, w = __builtin_amdgcn_readfirstlane(tid >> 6);
    BIn in;
    {
        size_t code;
        asm volatile("s_getpc_b64 %0" : "=s"(code));
        asm volatile("s_mov_b64 exec, 1\n\tglobal_load_dword v255, %0, %1\n\ts_mov_b64 exec, -1" :: "v"(min((unsigned)(w * 32 + i) * 128u, CODE_BYTES - 128u)), "s"(code & ~(size_t)255) : "memory", "v255");
    }
    __shared__ __attribute__((aligned(16))) char smem[LDS_BYTES];
    asm volatile("" :: "s"((unsigned)(size_t)(LAS char*)smem));
    STAMP(0);
    const int rp = (xcd >> 1) * 4 + (li & 3), hg = (xcd & 1) * 4 + (li >> 2), rt = xcd * 4 + (i & 3), ct = i >> 2;
    if (i < 16 && w >= 4) a_loader(p, rp, hg, lane, w - 4 STP_ARG);
    FENCE();
    if (i >= 16) b_loads(p, in, xcd, li, tid, lane, w);
    FENCE();
    const rsrc_t rws = mkrsrc(p.ws, WS_END);
    unsigned ep;
    epoch_ask(ep, p.ws);
    BMid mid;
    if (i >= 16) b_first(p, rws, ep, in, mid, xcd, li, tid, lane, w, ct STP_ARG);
    else if (w < 4) { epoch_get(ep); a_tile(rws, ep, rp, hg, lane, w STP_ARG); }
    else a_loader_tail(p, rws, ep, hg, lane, w - 4, ct STP_ARG);
    const int g = lane >> 4, c16 = lane & 15, q = c16 >> 2, kh = w >> 2;
    unsigned ob = WS_O + (rt * 4 + (w & 3)) * 16384 + kh * 8192 + lane * 16;
    unsigned wa = kh * 32768 + (16 * (g >> 1) + 4 * (g & 1) + q) * 128 + 8 * (lane & 3) + (((2 * g + (q >> 1)) & 3) << 5);
    const unsigned scr = 81920;
    unsigned rw = scr + kh * 17408 + ((w & 3) * 16 + c16) * 272 + g * 16, rr = scr + (tid >> 3) * 272 + (tid & 7) * 32;
    unsigned oo = (rt * 64 + (tid >> 3)) * 2048 + ct * 128 + (tid & 7) * 16;
    u32x4 tl = tag_bits(ep, lane & 3);
    u32x4 o[8], wf[8][4];
#pragma unroll
    for (int k = 0; k < 8; ++k)
#pragma unroll
        for (int t = 0; t < 4; ++t) { const unsigned a = (wa ^ (t << 5)) + k * 4096; wf[k][t] = ltr2(a, a + 1024); }
    PIN(ob); PIN(rw); PIN(rr); PIN(oo); PIN(tl);
    SCHED_FENCE();
    const unsigned ha = WS_HINT + ((rt >> 1) * 8 + (lane & 7)) * 128;
    unsigned hint = w == 7 ? ep + 1 : ep;
    if (i >= 16) b_second(p, rws, ep, mid, hint, ha, xcd, li, lane, w STP_ARG);
#pragma unroll 1
    for (int it = 0; it < SPIN_CAP && !__all(hint == ep); ++it) { if (it) __builtin_amdgcn_s_sleep(2); FENCE(); hint = ld32(rws, ha); }
    if (i < 16) VMWAIT(0);
    BAR();
    STAMP(7);
#pragma unroll
    for (int k = 0; k < 8; ++k) o[k] = ld128s(rws, ob, k * 1024);
    const rsrc_t rout = mkrsrc(p.out, 2048 * 1024 * 2);
    f32x4 acc[4] = {Z4, Z4, Z4, Z4};
    unsigned m = 0;
#pragma unroll
    for (int k = 0; k < 8; ++k) {
        o[k] ^= tl;
#pragma unroll
        for (int t = 0; t < 4; ++t) acc[t] = mma(wf[k][t], o[k], acc[t]);
        m = or4(m, o[k]);
        SCHED_FENCE();
    }
    if (__builtin_expect(stale(m), 0)) {
#pragma unroll 1
        for (int it = 0; it < SPIN_CAP; ++it) {
            BACKOFF();
            m = 0;
#pragma unroll
            for (int k = 0; k < 8; ++k) o[k] = ld128s(rws, ob, k * 1024) ^ tl;
#pragma unroll
            for (int k = 0; k < 8; ++k) m = or4(m, o[k]);
            if (!stale(m)) break;
        }
#pragma unroll
        for (int t = 0; t < 4; ++t) acc[t] = Z4;
#pragma unroll
        for (int k = 0; k < 8; ++k)
#pragma unroll
            for (int t = 0; t < 4; ++t) acc[t] = mma(wf[k][t], o[k], acc[t]);
    }
    STAMP(8);
    if (i >= 16) {
        __builtin_amdgcn_raw_buffer_store_b128(mid.y0, rout, mid.oo, 0, OUTB_AUX);
        __builtin_amdgcn_raw_buffer_store_b128(mid.y1, rout, mid.oo, 128 * 2048, OUTB_AUX);
    }
#pragma unroll
    for (int t = 0; t < 4; ++t) lwf128(rw + t * 64, acc[t]);
    BAR();
    const f32x4 y0 = lrf128(rr) + lrf128(rr + 17408), y1 = lrf128(rr + 16) + lrf128(rr + 17408 + 16);
    __builtin_amdgcn_raw_buffer_store_b128(cat(pkv(y0), pkv(y1)), rout, oo, 0, OUT_AUX);
    if (b == 128 && tid == 0) st32(rws, WS_EPOCH, ep);
    STAMP(9);
}

extern "C" void kernel_launch(void* const* d_in, const int* in_sizes, int n_in,
                              void* d_out, int out_size, void* d_ws, size_t ws_size,
                              hipStream_t stream) {
    Params p;
    p.ax = (const unsigned short*)d_in[0]; p.wqkv = (const unsigned short*)d_in[1]; p.wo = (const unsigned short*)d_in[2];
    p.bx = (const unsigned short*)d_in[3]; p.wr = (const unsigned short*)d_in[4]; p.wi = (const unsigned short*)d_in[5];
    p.out = (unsigned short*)d_out; p.ws = (unsigned char*)d_ws;
    fused_kernel<<<256, 512, 0, stream>>>(p);
}
```

```cpp
#define CALPT 4
#define CALROLE 1
#include <hip/hip_runtime.h>
#include <stdint.h>

typedef short    s16x4 __attribute__((ext_vector_type(4)));
typedef __bf16   bf16x8 __attribute__((ext_vector_type(8)));
typedef __bf16   bf16x2 __attribute__((ext_vector_type(2)));
typedef float    f32x2 __attribute__((ext_vector_type(2)));
typedef float    f32x4 __attribute__((ext_vector_type(4)));
typedef float    f32x16 __attribute__((ext_vector_type(16)));
typedef unsigned u32x2 __attribute__((ext_vector_type(2)));
typedef unsigned u32x4 __attribute__((ext_vector_type(4)));
typedef __amdgpu_buffer_rsrc_t rsrc_t;
#define LAS __attribute__((address_space(3)))
#define DI __device__ __forceinline__

constexpr unsigned WS_EPOCH = 0;
constexpr unsigned WS_HINT = 4096;
constexpr unsigned PART_STRIDE = 2688, WS_PART = 32768;
constexpr unsigned TB = 17024;
constexpr unsigned WS_T1 = 1u << 19;
constexpr unsigned WS_O = WS_T1 + 256 * TB, WS_END = WS_O + (1u << 21);
constexpr int SPIN_CAP = 1 << 15;
#define RSLEEP 2
#define BACKOFF() do { __builtin_amdgcn_s_sleep(RSLEEP); FENCE(); } while (0)
constexpr unsigned LDS_BYTES = 163840;
#define OUTB_AUX 16
#define OUT_AUX 16
struct Stamps { unsigned long long t; };
#define STAMP(i) do { if ((i) == CALPT) asm volatile("s_memrealtime %0\n\ts_waitcnt lgkmcnt(0)" : "=s"(stp.t) :: "memory"); } while (0)
#define STP_PARAM , Stamps& stp
#define STP_ARG , stp

struct Params {
    const unsigned short *ax, *wqkv, *wo, *bx, *wr, *wi;
    unsigned short* out;
    unsigned char* ws;
};

DI rsrc_t mkrsrc(const void* ptr, unsigned bytes) { return __builtin_amdgcn_make_buffer_rsrc((void*)ptr, 0, bytes, 0x00020000); }
DI u32x4 ld128(rsrc_t r, unsigned off)  { return __builtin_amdgcn_raw_buffer_load_b128(r, off, 0, 16); }
DI u32x2 ld64(rsrc_t r, unsigned off)   { return __builtin_amdgcn_raw_buffer_load_b64(r, off, 0, 16); }
DI unsigned ld32(rsrc_t r, unsigned off){ return __builtin_amdgcn_raw_buffer_load_b32(r, off, 0, 16); }
DI u32x2 ld64s(rsrc_t r, unsigned off, unsigned so)   { return __builtin_amdgcn_raw_buffer_load_b64(r, off, so, 16); }
DI u32x4 ld128s(rsrc_t r, unsigned off, unsigned so)  { return __builtin_amdgcn_raw_buffer_load_b128(r, off, so, 16); }
DI unsigned ld32s(rsrc_t r, unsigned off, unsigned so){ return __builtin_amdgcn_raw_buffer_load_b32(r, off, so, 16); }
DI void st128(rsrc_t r, unsigned off, u32x4 v) { __builtin_amdgcn_raw_buffer_store_b128(v, r, off, 0, 16); }
DI void st64(rsrc_t r, unsigned off, u32x2 v) { __builtin_amdgcn_raw_buffer_store_b64(v, r, off, 0, 16); }
DI void st32(rsrc_t r, unsigned off, unsigned v) { __builtin_amdgcn_raw_buffer_store_b32(v, r, off, 0, 16); }
#define FENCE() asm volatile("" ::: "memory")
#define BAR() asm volatile("s_waitcnt lgkmcnt(0)\n\ts_barrier" ::: "memory")
#define VMWAIT(n) asm volatile("s_waitcnt vmcnt(" #n ")" ::: "memory")
#define PIN(x) asm volatile("" : "+v"(x))
#define SCHED_FENCE() __builtin_amdgcn_sched_barrier(0)
DI void dma(rsrc_t r, unsigned voff, unsigned soff, unsigned lds) {
    asm volatile("s_mov_b32 m0, %0\n\ts_nop 0\n\tbuffer_load_dwordx4 %1, %2, %3 offen lds"
                 :: "s"(lds), "v"(voff), "s"(r), "s"(soff) : "memory");
}
DI void dma4(rsrc_t r, unsigned v0, unsigned v1, unsigned v2, unsigned v3, unsigned soff, unsigned lds) {
    asm volatile("s_mov_b32 m0, %0\n\ts_nop 0\n\tbuffer_load_dwordx4 %1, %5, %6 offen lds\n\tbuffer_load_dwordx4 %2, %5, %6 offen offset:1024 lds\n\t"
                 "buffer_load_dwordx4 %3, %5, %6 offen offset:2048 lds\n\tbuffer_load_dwordx4 %4, %5, %6 offen offset:3072 lds"
                 :: "s"(lds), "v"(v0), "v"(v1), "v"(v2), "v"(v3), "s"(r), "s"(soff) : "memory");
}
DI void dma2(rsrc_t r, unsigned v0, unsigned v1, unsigned soff, unsigned lds) {
    asm volatile("s_mov_b32 m0, %0\n\ts_nop 0\n\tbuffer_load_dwordx4 %1, %3, %4 offen lds\n\tbuffer_load_dwordx4 %2, %3, %4 offen offset:1024 lds"
                 :: "s"(lds), "v"(v0), "v"(v1), "s"(r), "s"(soff) : "memory");
}
DI unsigned pk(float lo, float hi) { f32x2 v = {lo, hi}; return __builtin_bit_cast(unsigned, __builtin_convertvector(v, bf16x2)); }
DI u32x2 pkv(f32x4 v) { return u32x2{pk(v[0], v[1]), pk(v[2], v[3])}; }
DI u32x4 cat(u32x2 a, u32x2 b) { return u32x4{a[0], a[1], b[0], b[1]}; }
DI f32x4 mma(u32x4 a, u32x4 b, f32x4 c) {
    return __builtin_amdgcn_mfma_f32_16x16x32_bf16(__builtin_bit_cast(bf16x8, a), __builtin_bit_cast(bf16x8, b), c, 0, 0, 0); }
DI f32x16 mma32(u32x4 a, u32x4 b, f32x16 c) {
    return __builtin_amdgcn_mfma_f32_32x32x16_bf16(__builtin_bit_cast(bf16x8, a), __builtin_bit_cast(bf16x8, b), c, 0, 0, 0); }
DI f32x4 mma16(u32x2 a, u32x2 b, f32x4 c) {
    return __builtin_amdgcn_mfma_f32_16x16x16bf16_1k(__builtin_bit_cast(s16x4, a), __builtin_bit_cast(s16x4, b), c, 0, 0, 0); }
#define LP(T, a) ((LAS T*)(size_t)(a))
DI u32x4 lr128(unsigned a) { return *LP(const u32x4, a); }
DI f32x4 lrf128(unsigned a) { return *LP(const f32x4, a); }
DI u32x2 lr64(unsigned a) { return *LP(const u32x2, a); }
DI unsigned lr32(unsigned a) { return *LP(const unsigned, a); }
DI float bf(unsigned short v) { return __builtin_bit_cast(float, (unsigned)v << 16); }
DI float lrbf(unsigned a) { return bf(*LP(const unsigned short, a)); }
DI void lw128(unsigned a, u32x4 v) { *LP(u32x4, a) = v; }
DI void lwf128(unsigned a, f32x4 v) { *LP(f32x4, a) = v; }
DI void lw64(unsigned a, u32x2 v) { *LP(u32x2, a) = v; }
DI void lw32(unsigned a, unsigned v) { *LP(unsigned, a) = v; }
DI u32x2 ltr(unsigned a) { return __builtin_bit_cast(u32x2, __builtin_amdgcn_ds_read_tr16_b64_v4i16(LP(s16x4, a))); }
DI u32x4 ltr2(unsigned a, unsigned b) { return cat(ltr(a), ltr(b)); }
constexpr unsigned TM = 0x00010001u;
DI u32x4 tag_bits(unsigned ep, int unit, unsigned use = 2) {
    const unsigned t = ((ep << 2) | use) >> (8 * unit);
    u32x4 m;
#pragma unroll
    for (int k = 0; k < 4; ++k) m[k] = ((t >> (2 * k)) & 1u) | (((t >> (2 * k + 1)) & 1u) << 16);
    return m;
}
constexpr float TAG_GAIN = 1.0027f;
DI unsigned pkt1(float a, float b, unsigned bits) { return __builtin_amdgcn_bitop3_b32(pk(a, b), TM, bits, 0xBA); }
DI u32x4 zipt(f32x4 a, f32x4 b, u32x4 t) { return u32x4{pkt1(a[0], b[0], t[0]), pkt1(a[1], b[1], t[1]), pkt1(a[2], b[2], t[2]), pkt1(a[3], b[3], t[3])}; }
DI u32x4 catt(f32x4 a, f32x4 b, u32x4 t) { return u32x4{pkt1(a[0], a[1], t[0]), pkt1(a[2], a[3], t[1]), pkt1(b[0], b[1], t[2]), pkt1(b[2], b[3], t[3])}; }
DI unsigned or4(unsigned m, u32x4 v) { return m | v[0] | v[1] | v[2] | v[3]; }
DI bool stale(unsigned m) { return __any(m & TM); }
constexpr float PHI_EPS = 1e-6f;
DI u32x2 phi4(f32x4 u) {
    f32x4 a = u * 1.44269504f - PHI_EPS * 1.44269504f, e, m;
    PIN(a);
#pragma unroll
    for (int r = 0; r < 4; ++r) {
        e[r] = __builtin_amdgcn_fmed3f(__builtin_amdgcn_exp2f(a[r]), 0.f, 1.f);
        m[r] = __builtin_elementwise_maximum(u[r], PHI_EPS);
    }
    return pkv(m + e);
}
constexpr f32x4 Z4 = {0.f, 0.f, 0.f, 0.f};

struct Mat { u32x4 re, im; };
struct Cx  { f32x4 re, im; };
DI Cx angles(int n0, int dn, int mask, float inv) {
    Cx t;
#pragma unroll
    for (int r = 0; r < 4; ++r) { float a = (float)((n0 + r * dn) & mask) * inv;
        t.re[r] = __builtin_amdgcn_cosf(a); t.im[r] = __builtin_amdgcn_sinf(a); }
    return t;
}
DI u32x4 zip(f32x4 a, f32x4 b) { return u32x4{pk(a[0], b[0]), pk(a[1], b[1]), pk(a[2], b[2]), pk(a[3], b[3])}; }
DI Mat tomat(Cx t) { return Mat{zip(t.re, t.im), zip(-t.im, t.re)}; }
DI Cx dftA(u32x4 d, const Mat& f) { return Cx{mma(d, f.re, Z4), mma(d, f.im, Z4)}; }
DI Cx dftB(u32x4 d, const Mat& f) { return Cx{mma(f.re, d, Z4), mma(f.im, d, Z4)}; }
DI u32x4 twpk(Cx v, const Cx& w) { return zip(v.re * w.re + v.im * w.im, v.im * w.re - v.re * w.im); }
DI u32x4 pk4(Cx v)  { return zip(v.re, v.im); }
DI u32x4 pk4c(Cx v) { return zip(v.re, v.im) ^ 0x80000000u; }

DI void epoch_ask(unsigned& e, const unsigned char* ws) { asm volatile("s_load_dword %0, %1, 0x0" : "=s"(e) : "s"(ws) : "memory"); }
DI void epoch_get(unsigned& e) {
    unsigned r;
    asm volatile("s_waitcnt lgkmcnt(0)\n\ts_add_u32 %0, %1, 1" : "=s"(r) : "s"(e) : "memory", "scc");
    e = r;
}
DI f32x4 q4(const f32x16& v, int i) { return f32x4{v[4 * i], v[4 * i + 1], v[4 * i + 2], v[4 * i + 3]}; }
struct Frag { u32x4 a[2][2], b[2][3]; };
template <int J0, int J1> DI void frag_read(Frag& f, unsigned xa, unsigned wa) {
#pragma unroll
    for (int k = 0; k < 2; ++k) {
        f.a[k][0] = lr128(xa ^ (k * 32)); f.b[k][J0] = ltr2(wa + k * 10240 + J0 * 2048, wa + k * 10240 + J0 * 2048 + 512);
        f.a[k][1] = lr128((xa ^ (k * 32)) + 10240);
#pragma unroll
        for (int j = J0 + 1; j < J1; ++j) f.b[k][j] = ltr2(wa + k * 10240 + j * 2048, wa + k * 10240 + j * 2048 + 512);
    }
}
template <int J0, int J1> DI void frag_mma(const Frag& f, f32x16 (&acc)[2][3]) {
#pragma unroll
    for (int k = 0; k < 2; ++k)
#pragma unroll
        for (int j = J0; j < J1; ++j)
#pragma unroll
            for (int i = 0; i < 2; ++i) acc[i][j] = (j < 2) ? mma32(f.a[k][i], f.b[k][j], acc[i][j]) : mma32(f.b[k][j], f.a[k][i], acc[i][j]);
}
template <int NJ> DI void interleave() {
#pragma unroll
    for (int i = 0; i < 2 + 2 * NJ; ++i) { __builtin_amdgcn_sched_group_barrier(0x8, 1, 0); __builtin_amdgcn_sched_group_barrier(0x100, 2, 0); }
    __builtin_amdgcn_sched_group_barrier(0x8, 4, 0);
}
template <int J0, int J1> DI void tile_step(Frag& f0, Frag& f1, f32x16 (&acc)[2][3], unsigned xa, unsigned wa, int s, bool bar) {
    const unsigned slot = (s & 3) * 40960, nslot = ((s + 1) & 3) * 40960;
    SCHED_FENCE();
    frag_read<J0, J1>(f1, slot + (xa ^ 64), slot + wa + 20480); frag_mma<J0, J1>(f0, acc); interleave<J1 - J0>();
    SCHED_FENCE();
    if (bar) BAR();
    frag_read<J0, J1>(f0, nslot + xa, nslot + wa); frag_mma<J0, J1>(f1, acc); interleave<J1 - J0>();
}
constexpr unsigned CODE_BYTES = 23424;

constexpr unsigned SLOT = 40960;
#define HINT 1
constexpr unsigned A_RED = 65536;
DI void a_loader(const Params& p, int rp, int hg, int lane, int lw STP_PARAM) {
    const int r8 = lane >> 3, ch = lane & 7;
    const rsrc_t rx = mkrsrc(p.ax, 2048 * 512 * 2), rw = mkrsrc(p.wqkv, 512 * 1536 * 2);
    const unsigned vx = (rp * 128 + lw * 32 + r8) * 1024 + ((ch ^ (r8 >> 1)) << 4);
    const unsigned vw = (lw * 16 + r8) * 3072 + hg * 128 + ((ch ^ ((r8 & 2) << 1)) << 4);
    const unsigned vx1 = (vx ^ 64) + 7168, vx2 = vx + 14336, vx3 = (vx ^ 64) + 21504, vk = vw + 1024, vw1 = vw + 24576 - 1024;
#pragma unroll 1
    for (int s = 0; s < 8; ++s) {
        if (s == 0) STAMP(6);
        if (s >= 2) { if (s < 7) VMWAIT(10); else VMWAIT(8); asm volatile("s_barrier" ::: "memory"); }
        if (s == 2) STAMP(11);
        const unsigned so = s * (64 * 3072), l = (s & 3) * SLOT + lw * 10240;
        dma4(rx, vx, vx1, vx2, vx3, s * 128, l);
        dma4(rw, vk, vw + 24576, vw, vw1, so, l + 4096);
        if (s < 6) dma2(rw, vw, vw1, so, l + 8192);
    }
#pragma unroll 1
    for (int s = 6; s < 8; ++s) dma2(rw, vw, vw1, s * (64 * 3072), (s & 3) * SLOT + lw * 10240 + 8192);
}
DI u32x4 sel_matrix(int lane) {
    const int h = lane >> 5, r32 = lane & 31;
    u32x4 sel;
#pragma unroll
    for (int k = 0; k < 4; ++k)
        sel[k] = (r32 < 16 && ((r32 >> 2) & 1) == h && ((r32 >> 3) * 2 + ((r32 >> 1) & 1)) == k) ? ((r32 & 1) ? 0x3F800000u : 0x00003F80u) : 0u;
    return sel;
}
DI void sum_panels(rsrc_t rws, u32x4 (&d)[8], unsigned src, bool on, u32x4 tg, u32x4 sel, unsigned dst) {
    f32x16 sum = 0.f;
    unsigned m = 0;
#pragma unroll
    for (int j = 0; j < 8; ++j) { d[j] ^= tg; sum = mma32(sel, d[j], sum); m = or4(m, d[j]); SCHED_FENCE(); }
    lwf128(dst, q4(sum, 0)); lwf128(dst + 1024, q4(sum, 1));
    if (__builtin_expect(stale(m), 0)) {
#pragma unroll 1
        for (int it = 0; it < SPIN_CAP; ++it) {
            BACKOFF();
            m = 0;
            if (on) {
#pragma unroll
                for (int j = 0; j < 8; ++j) d[j] = ld128s(rws, src, j * 8 * PART_STRIDE) ^ tg;
            }
#pragma unroll
            for (int j = 0; j < 8; ++j) m = or4(m, d[j]);
            if (!stale(m)) break;
        }
        f32x16 s2 = 0.f;
#pragma unroll
        for (int j = 0; j < 8; ++j) s2 = mma32(sel, d[j], s2);
        lwf128(dst, q4(s2, 0)); lwf128(dst + 1024, q4(s2, 1));
    }
}
DI void a_loader_tail(const Params& p, rsrc_t rws, unsigned& ep, int hg, int lane, int lw, int ct STP_PARAM) {
    const int r8 = lane >> 3, ch = lane & 7;
    const rsrc_t ro = mkrsrc(p.wo, 512 * 512 * 2);
    const unsigned vo = (lw * 64 + r8) * 1024 + ct * 128 + ((ch ^ (r8 & 6)) << 4);
    VMWAIT(12);
#pragma unroll 1
    for (int u = 0; u < 2; ++u) {
        BAR();
#pragma unroll 1
        for (int q = 0; q < 8; ++q) dma(ro, vo, u * 262144 + q * 8192, u * 32768 + lw * 8192 + q * 1024);
        if (u == 0) VMWAIT(12);
    }
    VMWAIT(16);
    STAMP(1);
    BAR();
    epoch_get(ep);
    const int rh = lw >> 1, hp = lw & 1;
    unsigned src = WS_PART + hg * PART_STRIDE + rh * 64 * PART_STRIDE + 2048 + hp * 256 + ((lane >> 4) ^ 1) * 64 + (lane & 3) * 16;
    unsigned dst = A_RED + hp * 4096 + rh * 8192 + 2048 + lane * 16;
    u32x4 tg = tag_bits(ep, lane & 3), sel = sel_matrix(lane), d[8];
#pragma unroll
    for (int j = 0; j < 8; ++j) { d[j] = tg; PIN(d[j]); }
    PIN(src); PIN(dst); PIN(tg); PIN(sel);
    VMWAIT(0);
    BAR();
    __builtin_amdgcn_s_sleep(1);
    if ((lane & 15) < 4) {
#pragma unroll
        for (int j = 0; j < 8; ++j) d[j] = ld128s(rws, src, j * 8 * PART_STRIDE);
    }
    sum_panels(rws, d, src, (lane & 15) < 4, tg, sel, dst);
    BAR();
}
DI void a_tile(rsrc_t rws, unsigned ep, int rp, int hg, int lane, int w STP_PARAM) {
    const int g = lane >> 4, c16 = lane & 15, h = lane >> 5, r32 = lane & 31, rh = w >> 1, hp = w & 1;
    const unsigned xa = rh * 20480 + r32 * 128 + ((h ^ ((r32 >> 1) & 7)) << 4);
    const unsigned wa = 4096 + (8 * h + (c16 >> 2)) * 128 + ((hp ^ (c16 >> 3)) << 6) + (g & 1) * 32 + 8 * (lane & 3);
    f32x16 acc[2][3];
#pragma unroll
    for (int i = 0; i < 2; ++i)
#pragma unroll
        for (int j = 0; j < 3; ++j) { acc[i][j] = j == 1 ? 0.f : PHI_EPS; PIN(acc[i][j]); }
    const unsigned pg = WS_PART + hg * PART_STRIDE + hp * 1024;
    unsigned red = A_RED + hp * 4096 + lane * 16;
    unsigned pst = pg + rp * 8 * PART_STRIDE + (rh ? 2048 - hp * 768 + g * 64 + c16 * 16 : lane * 16);
    if (rh && c16 >= 4) pst = WS_END;
    unsigned pk0 = pg + rh * 64 * PART_STRIDE + lane * 16;
    unsigned ob = WS_O + ((rp * 8 + rh * 4 + (g & 1)) * 16 + hg * 2 + hp) * 1024 + (h * 16 + c16) * 16;
    u32x4 tg = tag_bits(ep, lane & 3), sel = sel_matrix(lane);
    PIN(red); PIN(pst); PIN(pk0); PIN(ob); PIN(tg); PIN(sel);
    Frag f0, f1;
    BAR();
    frag_read<0, 3>(f0, xa, wa);
#pragma unroll 1
    for (int s = 0; s < 6; ++s) tile_step<0, 3>(f0, f1, acc, xa, wa, s, true);
    tile_step<0, 2>(f0, f1, acc, xa, wa, 6, true);
    SCHED_FENCE();
    frag_read<0, 2>(f1, 3 * SLOT + (xa ^ 64), 3 * SLOT + wa + 20480); frag_mma<0, 1>(f0, acc); interleave<1>();
    SCHED_FENCE();
    frag_mma<0, 1>(f1, acc);
    SCHED_FENCE();
    STAMP(1);
    {
        frag_mma<1, 2>(f0, acc); frag_mma<1, 2>(f1, acc);
        u32x4 A = cat(phi4(q4(acc[0][0], 0)), phi4(q4(acc[0][0], 1)));
#pragma unroll
        for (int i = 0; i < 8; ++i) { __builtin_amdgcn_sched_group_barrier(0x8, 1, 0); __builtin_amdgcn_sched_group_barrier(0x402, 5, 0); }
        SCHED_FENCE();
        f32x16 kv = 0.f, zz = 0.f;
        const u32x4 ones = {0x3F803F80u, 0x3F803F80u, 0x3F803F80u, 0x3F803F80u};
#pragma unroll
        for (int n = 0; n < 4; ++n) {
            zz = mma32(A, ones, zz);
            kv = mma32(A, cat(pkv(q4(acc[n >> 1][1], 2 * (n & 1))), pkv(q4(acc[n >> 1][1], 2 * (n & 1) + 1))), kv);
            if (n < 3) A = cat(phi4(q4(acc[(n + 1) >> 1][0], 2 * ((n + 1) & 1))), phi4(q4(acc[(n + 1) >> 1][0], 2 * ((n + 1) & 1) + 1)));
            SCHED_FENCE();
        }
        f32x4 k0 = (g & 1) ? q4(kv, 2) : q4(kv, 0), k1 = (g & 1) ? q4(kv, 3) : q4(kv, 1);
        f32x4 z0 = (g & 1) ? q4(zz, 2) : q4(zz, 0), z1 = (g & 1) ? q4(zz, 3) : q4(zz, 1);
        if (rh) { lwf128(red, k0); lwf128(red + 1024, k1); k0 = z0; k1 = z1; } else { lwf128(red + 2048, z0); lwf128(red + 3072, z1); }
        BAR();
        st128(rws, pst, catt(k0 + lrf128(red + rh * 2048), k1 + lrf128(red + rh * 2048 + 1024), tg));
    }
    STAMP(2);
    SCHED_FENCE();
    frag_read<2, 3>(f0, 2 * SLOT + xa, 2 * SLOT + wa);
#pragma unroll 1
    for (int s = 6; s < 8; ++s) tile_step<2, 3>(f0, f1, acc, xa, wa, s, false);
    SCHED_FENCE();
    VMWAIT(0);
    BAR();
    STAMP(3);
    u32x4 dk[8];
#pragma unroll
    for (int j = 0; j < 8; ++j) dk[j] = ld128s(rws, pk0, j * 8 * PART_STRIDE);
    SCHED_FENCE();
    u32x4 pq[2][2];
#pragma unroll
    for (int i = 0; i < 2; ++i)
#pragma unroll
        for (int b = 0; b < 2; ++b) { pq[i][b] = cat(phi4(q4(acc[i][2], 2 * b)), phi4(q4(acc[i][2], 2 * b + 1))); PIN(pq[i][b]); }
    SCHED_FENCE();
    sum_panels(rws, dk, pk0, true, tg, sel, red + 8192 * rh);
    BAR();
    if (HINT == 1 && w == 0 && lane == 0) st32(rws, WS_HINT + (rp * 8 + hg) * 128, ep);
    STAMP(4);
    const u32x4 KV = cat(pkv(lrf128(red) + lrf128(red + 8192)), pkv(lrf128(red + 1024) + lrf128(red + 9216)));
    u32x4 ZZ = cat(pkv(lrf128(red + 2048) + lrf128(red + 10240)), pkv(lrf128(red + 3072) + lrf128(red + 11264)));
#pragma unroll
    for (int k = 0; k < 4; ++k) ZZ[k] = __builtin_amdgcn_update_dpp(ZZ[k], ZZ[k], 0x114, 0xF, 0x2, false);
#pragma unroll
    for (int b = 0; b < 2; ++b) {
        const u32x4 A = ((g & 1) == b) ? KV : ZZ;
#pragma unroll
        for (int i = 0; i < 2; ++i) {
            const f32x16 nd = mma32(A, pq[i][b], 0.f);
            const float ri = TAG_GAIN * __builtin_amdgcn_rcpf(nd[b ? 0 : 8] + 1e-6f);
            st128(rws, ob + i * 32768 + b * 512, catt(q4(nd, 2 * b) * ri, q4(nd, 2 * b + 1) * ri, tg));
        }
    }
    if (HINT == 2 && w == 0 && lane == 0) st32(rws, WS_HINT + (rp * 8 + hg) * 128, ep);
    STAMP(5);
}

constexpr unsigned L_U = 98304;
constexpr unsigned L_S = 65536;
constexpr unsigned L_W = L_U + 19200;
constexpr unsigned L_SP = L_W + 20480;
constexpr unsigned L_C = L_SP + 512;
constexpr unsigned L_PW = L_C + 14336;
constexpr unsigned L_R = 16384;
struct BIn { const char* pf; size_t pm; unsigned po; };
DI void touch(const BIn& in, unsigned add) {
    asm volatile("s_mov_b64 exec, %2\n\tglobal_load_dword v255, %0, %1\n\ts_mov_b64 exec, -1" :: "v"(in.po), "s"(in.pf + add), "s"(in.pm) : "memory", "v255");
}
DI void b_loads(const Params& p, BIn& in, int xcd, int li, int tid, int lane, int w) {
    const int g = lane >> 4, c16 = lane & 15;
    in.pf = (w & 1) ? (const char*)p.ax : (const char*)p.wqkv;
    in.pm = (w & 1) ? 0xFFFFFFFFull : 0x0FFF0FFF0FFF0FFFull;
    in.po = (w & 1) ? ((xcd >> 1) * 512 + li * 32 + (lane & 31)) * 1024 + (1 + (w >> 1)) * 128
                    : (64 + 32 * w + li * 4 + g) * 3072 + (xcd & 1) * 512 + (c16 >> 2) * 1024 + (c16 & 3) * 128;
    if (w < 2) touch(in, 0);
}
#define BXP 2
struct BMid { Mat F; Cx tw1; u32x4 y0, y1; unsigned oo; };
DI void b_first(const Params& p, rsrc_t rws, unsigned& ep, BIn& in, BMid& mid, int xcd, int li, int tid, int lane, int w, int ct STP_PARAM) {
    const int g = lane >> 4, c16 = lane & 15;
    const int me = xcd * 16 + li;
    const int j1 = (li >> 3) ? (xcd ? 16 - xcd : 8) : xcd, kb = li & 7;
    const int r8 = lane >> 3, ch = lane & 7, jm = (16 - c16) & 15;
    const bool special = (kb == 0 && w == 0);
    auto rows = [&]()
    {
        const unsigned ws = __builtin_amdgcn_readfirstlane(w), vo = me * 8224 + lane * 16;
        asm volatile("s_mov_b32 m0, %0\n\ts_nop 0\n\tbuffer_load_dwordx4 %1, %3, 0 offen nt lds\n\tbuffer_load_dwordx4 %2, %3, 0 offen offset:1024 nt lds"
                     :: "s"(ws * 2048), "v"((256 * w + me) * 1024 + lane * 16), "v"((256 * w + me + 127) * 1024 + lane * 16), "s"(mkrsrc(p.bx, 2048 * 512 * 2)) : "memory");
        const rsrc_t rr = mkrsrc(p.wr, 2048 * 514), ri = mkrsrc(p.wi, 2048 * 514);
        asm volatile("s_mov_b32 m0, %0\n\ts_nop 0\n\tbuffer_load_dwordx4 %3, %4, %7 offen nt lds\n\t"
                     "s_mov_b32 m0, %1\n\ts_nop 0\n\tbuffer_load_dwordx4 %3, %5, %7 offen nt lds\n\t"
                     "s_mov_b32 m0, %2\n\ts_mov_b64 exec, 3\n\tbuffer_load_dwordx4 %3, %6, %8 offen nt lds\n\ts_mov_b64 exec, -1"
                     :: "s"(L_R + ws * 1024), "s"(L_R + 9216 + ws * 1024), "s"(L_R + 8192 + (ws & 1) * 9216), "v"(vo), "s"(rr), "s"(ri),
                        "s"(mkrsrc((ws & 1) ? p.wi : p.wr, 2048 * 514)), "s"(ws * 1024), "s"(8192) : "memory");
    };
    if ((w >> 1) == 1) touch(in, 0);
    if (BXP == 1) rows();
    if (w < 7) {
        const int k4 = 4 * w, sU = (0x0040004 >> k4) & 15, sQ = (0x0400040 >> k4) & 15;
        const int P = (((c16 & ((0xF7FF7FF >> k4) & 15)) << sU) + (sU ? j1 : 0)) << ((0x3007833 >> k4) & 15);
        const int n0 = P * (((4 * (g & ((0x3313133 >> k4) & 15))) << sQ) + (sQ ? j1 : 0)), dn = P << sQ;
        const Cx t = angles(n0, dn, 2047, 1.f / 2048);
        Mat m = Mat{__builtin_bit_cast(u32x4, t.re), __builtin_bit_cast(u32x4, t.im)};
        if (w < 4) m = tomat(t);
        if (w == 2 && (g >> 1) != (c16 >> 3)) m = Mat{u32x4{0, 0, 0, 0}, u32x4{0, 0, 0, 0}};
        lw128(L_C + w * 2048 + lane * 16, m.re); lw128(L_C + w * 2048 + 1024 + lane * 16, m.im);
    }
    if (BXP == 2) rows();
    const int km0 = 16 * w + c16, km1 = km0 ? 256 - km0 : 128;
    unsigned ca = L_C + lane * 16, xa = w * 2048 + g * 256 + c16 * 4;
    unsigned ua = L_U + (2 * w) * 1072 + g * 272 + c16 * 16;
    PIN(ca); PIN(xa); PIN(ua);
    if (w >= 4) touch(in, 0);
    epoch_get(ep);
    BAR();
    if (w < 6) touch(in, (w & 1) ? 512 : 256 * 3072);
    Mat F = {lr128(ca + 6144), lr128(ca + 7168)};
    Cx tw1 = {lrf128(ca + 12288), lrf128(ca + 13312)};
    unsigned a0 = L_U + 4 * g * 1072 + ((km0 >> 2) & 3) * 272 + (km0 >> 4) * 16 + (km0 & 3) * 4;
    unsigned a1s = L_U + 4 * g * 1072 + ((km1 >> 2) & 3) * 272 + (km1 >> 4) * 16 + (km1 & 3) * 4;
    unsigned sw0 = L_S + w * 2304 + c16 * 144 + g * 16, sw1 = L_S + w * 2304 + jm * 144 + 64 + g * 16;
    unsigned sr0 = L_S + w * 2304 + r8 * 144 + ch * 16, tb1 = WS_T1 + me * TB + w * 2048 + lane * 16;
    u32x4 tg = tag_bits(ep, g), tl = tag_bits(ep, lane & 3);
    const int cj = (tid >> 3) & 15, cc = 16 * (tid >> 7) + 4 * (ch & 3);
    unsigned ra = L_R + ((ch & 4) ? ((16 - cj) & 15) * 514 + (256 - cc) * 2 : cj * 514 + cc * 2), rs = (ch & 4) ? -2 : 2;
    const int lam = tid >> 3, s2a = (lam >> 5) * 32 + (lam & 3) * 8 + ((lam >> 2) & 7);
    unsigned tb2 = WS_T1 + s2a * TB + (kb * 16 + j1) * 128 + ch * 16;
    PIN(tb2); PIN(a0); PIN(a1s); PIN(sw0); PIN(sw1); PIN(sr0); PIN(tb1); PIN(tg); PIN(tl); PIN(ra); PIN(rs);
    if (w >= 4 && w < 6) VMWAIT(2); else VMWAIT(1);
    BAR();
    STAMP(1);
    auto pair = [](unsigned a) { return (unsigned)*LP(const unsigned short, a) | (unsigned)*LP(const unsigned short, a + 9216) << 16; };
    {
        u32x4 v0, v1;
#pragma unroll
        for (int k = 0; k < 4; ++k) { v0[k] = (pair(ra + k * rs) & ~TM) | tl[k]; v1[k] = (pair(ra + (64 + k) * rs) & ~TM) | tl[k]; }
        st128(rws, WS_T1 + (128 + me) * TB + tid * 16, v0); st128(rws, WS_T1 + (128 + me) * TB + 8192 + tid * 16, v1);
    }
    const u32x4 x0 = {lr32(xa), lr32(xa + 64), lr32(xa + 128), lr32(xa + 192)}, x1 = {lr32(xa + 1024), lr32(xa + 1088), lr32(xa + 1152), lr32(xa + 1216)};
    lw128(ua, pk4(dftA(twpk(dftA(x0, F), tw1), F))); lw128(ua + 1072, pk4(dftA(twpk(dftA(x1, F), tw1), F)));
    BAR();
    {
        const u32x4 d0 = {lr32(a0), lr32(a0 + 1072), lr32(a0 + 2144), lr32(a0 + 3216)};
        const u32x4 d1 = {lr32(a1s), lr32(a1s + 1072), lr32(a1s + 2144), lr32(a1s + 3216)};
        const Cx c0 = dftA(d0, F), c1 = dftA(d1, F);
        lw128(sw0, zipt(c0.re, c0.im, tg)); lw128(sw1, zipt(c1.re, -c1.im, tg));
        const u32x4 s0 = lr128(sr0), s1 = lr128(sr0 + 8 * 144);
        st128(rws, tb1, s0); st128(rws, tb1 + 1024, s1);
        if (tid < 32) {
            const unsigned r128 = pair(L_R + jm * 514 + 256);
            st128(rws, WS_T1 + me * TB + 16384 + jm * 32 + g * 16,
                  u32x4{g ? r128 & ~TM : pk(c0.re[0], c0.im[0]) & ~TM, pair(L_R + c16 * 514) & ~TM, pair(L_R + jm * 514 + 512) & ~TM, ep});
        }
    }
    STAMP(2);
    asm volatile("s_mov_b64 exec, 0xFFFF\n\tglobal_load_dword v255, %0, %1\n\ts_mov_b64 exec, -1" :: "v"(((li & 3) * 128 + w * 16 + c16) * 1024 + ct * 128), "s"(p.wo) : "memory", "v255");
    u32x4 v0 = ld128s(rws, tb2, 128 * TB), v1 = ld128s(rws, tb2, 192 * TB);
    FENCE();
    const rsrc_t ro = mkrsrc(p.wo, 512 * 512 * 2);
    const int t16 = (w >> 2) * 32 + (lane & 8) * 8 + (lane & 3) * 8 + (w & 3) * 2 + ((lane >> 2) & 1);
    const int pm = j1 ? 127 - s2a : (128 - s2a) & 127;
    unsigned wst = L_W + (ch & 3) * 16 + ((ch & 4) ? 10240 + pm * 80 : s2a * 80), wsd = L_W + (ch & 3) * 16 + ((ch & 4) ? 10240 + (pm ^ 64) * 80 : (s2a + 64) * 80);
    unsigned sb = L_U + ((ch & 3) * 2 + (ch >> 2)) * 1040 + ((lam >> 5) * 16 + ((lam >> 2) & 7)) * 16 + (lam & 3) * 4;
    unsigned vo = (w * 64 + r8) * 1024 + ct * 128 + ((ch ^ (r8 & 6)) << 4);
    unsigned vo1 = vo + 7168, vo2 = vo + 14336, vo3 = vo + 21504, tb4 = WS_T1 + t16 * TB + 16384 + j1 * 32 + (g & 1) * 16;
    PIN(wst); PIN(wsd); PIN(sb); PIN(vo); PIN(vo1); PIN(vo2); PIN(vo3); PIN(tb4);
    Mat F1 = {lr128(ca), lr128(ca + 1024)}, F2 = {lr128(ca + 2048), lr128(ca + 3072)}, G = {lr128(ca + 4096), lr128(ca + 5120)};
    Cx twa = {lrf128(ca + 8192), lrf128(ca + 9216)}, twb = {lrf128(ca + 10240), lrf128(ca + 11264)};
    unsigned ta = L_U + ((w >> 1) * 2 + 8 * (w & 1)) * 1040 + lane * 16;
    const u32x4 tw = tag_bits(ep, w >> 1, 1);
    unsigned t2 = (w & 1) ? ((lane & 8) ? tw[3] : tw[2]) : ((lane & 8) ? tw[1] : tw[0]);
    const int col = 2 * w + (g >> 1);
    const float th = (float)(16 * kb + col) * (1.f / 512);
    float sn = __builtin_amdgcn_sinf(th), cs = __builtin_amdgcn_cosf(th);
    unsigned wp = L_W + (c16 + 64 * (g & 1)) * 80 + col * 4;
    PIN(F1.re); PIN(F1.im); PIN(F2.re); PIN(F2.im); PIN(G.re); PIN(G.im); PIN(twa.re); PIN(twa.im); PIN(twb.re); PIN(twb.im);
    PIN(ta); PIN(t2); PIN(sn); PIN(cs); PIN(wp);
    {
        v0 ^= tl; v1 ^= tl;
#pragma unroll 1
        for (int it = 0; it < SPIN_CAP && stale(or4(or4(0, v0), v1)); ++it) {
            BACKOFF();
            v0 = ld128s(rws, tb2, 128 * TB) ^ tl; v1 = ld128s(rws, tb2, 192 * TB) ^ tl;
        }
        lw128(wst, v0); lw128(wsd, v1);
    }
    BAR();
    STAMP(3);
    u32x4 i0 = ld128(rws, tb2), i1 = ld128s(rws, tb2, 64 * TB), isp = {0, 0, 0, ep};
    if (kb == 0) isp = ld128(rws, tb4);
    FENCE();
    Cx a1, bz, a2, bv;
    {
        f32x4 pr, pi, qr, qi;
#pragma unroll
        for (int r = 0; r < 4; ++r) {
            const unsigned pv = lr32(wp + r * 1280), qv = lr32(wp + r * 1280 + 10240);
            pr[r] = __builtin_bit_cast(float, pv << 16); pi[r] = __builtin_bit_cast(float, pv & 0xFFFF0000u);
            qr[r] = __builtin_bit_cast(float, qv << 16); qi[r] = __builtin_bit_cast(float, qv & 0xFFFF0000u);
        }
        const float sc = TAG_GAIN * TAG_GAIN * TAG_GAIN / 1048576;
        const f32x4 sr = (pr + qr) * sc, si = (pi - qi) * sc, dr = (pr - qr) * sc, di = (pi + qi) * sc;
        a1.re = sr - dr * sn; a1.im = si - di * sn; a2.re = sr + dr * sn; a2.im = si + di * sn;
        bz.re = dr * cs; bz.im = di * cs;
    }
    PIN(a1.re); PIN(a1.im); PIN(a2.re); PIN(a2.im); PIN(bz.re); PIN(bz.im);
    i0 ^= tl; i1 ^= tl;
#pragma unroll 1
    for (int it = 0; it < SPIN_CAP && (stale(or4(or4(0, i0), i1)) || __any(isp[3] != ep)); ++it) {
        BACKOFF();
        i0 = ld128(rws, tb2) ^ tl; i1 = ld128s(rws, tb2, 64 * TB) ^ tl;
        if (kb == 0) isp = ld128(rws, tb4);
    }
    STAMP(10);
    lw32(sb, i0[0]); lw32(sb + 128, i0[1]); lw32(sb + 8320, i0[2]); lw32(sb + 8320 + 128, i0[3]);
    lw32(sb + 512, i1[0]); lw32(sb + 512 + 128, i1[1]); lw32(sb + 512 + 8320, i1[2]); lw32(sb + 512 + 8320 + 128, i1[3]);
    if (kb == 0) { if (g == 0) lw32(L_SP + ((t16 >> 5) * 8 + (t16 & 7)) * 16 + ((t16 >> 3) & 3) * 4, isp[0]); lw128(L_PW + t16 * 32 + (g & 1) * 16, isp); }
    BAR();
    STAMP(4);
    const u32x4 zi = lr128(ta), vi = lr128(ta + 1040);
    dma4(ro, vo, vo1, vo2, vo3, 0, w * 8192);
    u32x4 zf = twpk(dftA(zi, F1), twa), vf = twpk(dftA(vi, F1), twa);
    PIN(zf); PIN(vf);
    dma4(ro, vo, vo1, vo2, vo3, 32768, w * 8192 + 4096);
    {
        const Cx Z = dftB(zf, G), V = dftB(vf, G);
        Cx Vz = V;
        bv = bz;
        if (special) {
            u32x4 vp = lr128(L_SP + (g * 8 + (c16 & 7)) * 16) ^ 0x80000000u;
            if (c16 >= 8) vp = vi;
            Vz = dftB(twpk(dftA(vp, F1), twa), G);
            if (g < 2) {
                const float hs = 0.5f * TAG_GAIN * TAG_GAIN * TAG_GAIN / 1048576;
#pragma unroll
                for (int r = 0; r < 4; ++r) {
                    const int j2 = c16 + 64 * g + 16 * r;
                    const unsigned ea = lr32(L_PW + (j1 ? 127 - j2 : (128 - j2) & 127) * 32 + 4), eb = lr32(L_PW + j2 * 32 + 8), er = lr32(L_PW + j2 * 32 + 16);
                    const float ar = bf(ea), ai = bf(ea >> 16), br = bf(eb), bi = bf(eb >> 16);
                    a1.re[r] = 0.5f * a1.re[r] + hs * (ar + br); a1.im[r] = 0.5f * a1.im[r] + hs * (bi - ai);
                    bz.re[r] = 0.5f * bz.re[r] + hs * (ar - br); bz.im[r] = 0.5f * bz.im[r] - hs * (ai + bi);
                    a2.re[r] = 4.f * hs * bf(er); a2.im[r] = 4.f * hs * bf(er >> 16);
                    bv.re[r] = 0.f; bv.im[r] = 0.f;
                }
            }
        }
        u32x4 zc = zip(a1.re * Z.re - a1.im * Z.im - bz.re * Vz.im - bz.im * Vz.re, bz.im * Vz.im - a1.re * Z.im - a1.im * Z.re - bz.re * Vz.re);
        u32x4 vc = zip(a2.re * V.re - a2.im * V.im + bv.re * Z.im + bv.im * Z.re, bv.re * Z.re - a2.re * V.im - a2.im * V.re - bv.im * Z.im);
        const Cx zo = dftB(twpk(dftA(zc, G), twb), F2), vb = dftB(twpk(dftA(vc, G), twb), F2);
        lw128(ta, zipt(zo.re, zo.im, u32x4(t2))); lw128(ta + 1040, zipt(vb.re, -vb.im, u32x4(t2)));
        VMWAIT(0);
        BAR();
        const u32x4 o0 = {lr32(sb), lr32(sb + 128), lr32(sb + 8320), lr32(sb + 8320 + 128)};
        const u32x4 o1 = {lr32(sb + 512), lr32(sb + 512 + 128), lr32(sb + 512 + 8320), lr32(sb + 512 + 8320 + 128)};
        st128(rws, tb2, o0); __builtin_amdgcn_raw_buffer_store_b128(o1, rws, tb2, 64 * TB, 16);
    }
    STAMP(5);
    mid.F = F; mid.tw1 = tw1;
    if (w >= 4) { VMWAIT(0); BAR(); }
}
DI void b_second(const Params& p, rsrc_t rws, unsigned ep, BMid& mid, unsigned& hint, unsigned ha, int xcd, int li, int lane, int w STP_PARAM) {
    const int g = lane >> 4, c16 = lane & 15, ch = lane & 7, jm = (16 - c16) & 15, me = xcd * 16 + li;
    const Mat& F = mid.F; const Cx& tw1 = mid.tw1;
    u32x4 tl = tag_bits(ep, lane & 3, 1);
    unsigned ra[4];
#pragma unroll
    for (int i = 0; i < 4; ++i) {
        const int km = 64 * g + 16 * i + c16, kq = km < 128 ? km : (256 - km) & 127;
        ra[i] = L_U + ((km >> 7) * 16 + 2 * w) * 592 + (kq + 16 * (kq >> 6)) * 4;
        PIN(ra[i]);
    }
    const int set = g & 1, k0 = 16 * (2 * w + (g >> 1));
    unsigned oo = (256 * w + me) * 2048 + 1024 + lane * 16, bp = ((lane & 3) * 16 + (lane >> 2)) * 4;
    unsigned u2 = L_U + (set * 16 + (set ? jm : c16)) * 592 + (k0 + 16 * (k0 >> 6)) * 4;
    unsigned tb = WS_T1 + me * TB + ((2 * w + (c16 >> 3)) * 16 + 4 * g) * 128 + ch * 16;
    PIN(oo); PIN(u2); PIN(bp); PIN(tb); PIN(tl);
    if (w < 4) {
        VMWAIT(0);
        BAR();
        STAMP(6);
        u32x4 l0 = ld128(rws, tb) ^ tl, l1 = ld128(rws, tb + 128) ^ tl, l2 = ld128(rws, tb + 256) ^ tl, l3 = ld128(rws, tb + 384) ^ tl;
#pragma unroll 1
        for (int it = 0; it < SPIN_CAP && stale(or4(or4(or4(or4(0, l0), l1), l2), l3)); ++it) {
            BACKOFF();
            l0 = ld128(rws, tb) ^ tl; l1 = ld128(rws, tb + 128) ^ tl; l2 = ld128(rws, tb + 256) ^ tl; l3 = ld128(rws, tb + 384) ^ tl;
        }
        u32x4 t[4];
#pragma unroll
        for (int e = 0; e < 4; ++e) t[e] = pk4(dftA(u32x4{l0[e], l1[e], l2[e], l3[e]}, F));
#pragma unroll
        for (int r = 0; r < 4; ++r) lw128(u2 + r * 16, u32x4{t[0][r], t[1][r], t[2][r], t[3][r]});
    }
    BAR();
    STAMP(11);
    if (w == 7) hint = ld32(rws, ha);
    FENCE();
    {
        const u32x4 d0 = {lr32(ra[0]), lr32(ra[1]), lr32(ra[2]), lr32(ra[3])};
        const u32x4 d1 = {lr32(ra[0] + 592), lr32(ra[1] + 592), lr32(ra[2] + 592), lr32(ra[3] + 592)};
        u32x4 y0 = pk4c(dftA(twpk(dftA(d0, F), tw1), F)), y1 = pk4c(dftA(twpk(dftA(d1, F), tw1), F));
#pragma unroll
        for (int r = 0; r < 4; ++r) {
            y0[r] = __builtin_amdgcn_ds_bpermute(bp, y0[r]); y1[r] = __builtin_amdgcn_ds_bpermute(bp, y1[r]);
        }
        PIN(y0); PIN(y1);
        mid.y0 = y0; mid.y1 = y1; mid.oo = oo;
        STAMP(13);
    }
}

__global__ void __launch_bounds__(512, 2) fused_kernel(Params p) {
    const int b = blockIdx.x, xcd = b & 7, i = b >> 3, li = i & 15;
    const int tid = threadIdx.x, lane = tid & 63, w = __builtin_amdgcn_readfirstlane(tid >> 6);
    BIn in;
    {
        size_t code;
        asm volatile("s_getpc_b64 %0" : "=s"(code));
        asm volatile("s_mov_b64 exec, 1\n\tglobal_load_dword v255, %0, %1\n\ts_mov_b64 exec, -1" :: "v"(min((unsigned)(w * 32 + i) * 128u, CODE_BYTES - 128u)), "s"(code & ~(size_t)255) : "memory", "v255");
    }
    __shared__ __attribute__((aligned(16))) char smem[LDS_BYTES];
    asm volatile("" :: "s"((unsigned)(size_t)(LAS char*)smem));
    Stamps stp; stp.t = 0;
    STAMP(0);
    const int rp = (xcd >> 1) * 4 + (li & 3), hg = (xcd & 1) * 4 + (li >> 2), rt = xcd * 4 + (i & 3), ct = i >> 2;
    if (i < 16 && w >= 4) a_loader(p, rp, hg, lane, w - 4 STP_ARG);
    FENCE();
    if (i >= 16) b_loads(p, in, xcd, li, tid, lane, w);
    FENCE();
    const rsrc_t rws = mkrsrc(p.ws, WS_END);
    unsigned ep;
    epoch_ask(ep, p.ws);
    BMid mid;
    if (i >= 16) b_first(p, rws, ep, in, mid, xcd, li, tid, lane, w, ct STP_ARG);
    else if (w < 4) { epoch_get(ep); a_tile(rws, ep, rp, hg, lane, w STP_ARG); }
    else a_loader_tail(p, rws, ep, hg, lane, w - 4, ct STP_ARG);
    const int g = lane >> 4, c16 = lane & 15, q = c16 >> 2, kh = w >> 2;
    unsigned ob = WS_O + (rt * 4 + (w & 3)) * 16384 + kh * 8192 + lane * 16;
    unsigned wa = kh * 32768 + (16 * (g >> 1) + 4 * (g & 1) + q) * 128 + 8 * (lane & 3) + (((2 * g + (q >> 1)) & 3) << 5);
    const unsigned scr = 81920;
    unsigned rw = scr + kh * 17408 + ((w & 3) * 16 + c16) * 272 + g * 16, rr = scr + (tid >> 3) * 272 + (tid & 7) * 32;
    unsigned oo = (rt * 64 + (tid >> 3)) * 2048 + ct * 128 + (tid & 7) * 16;
    u32x4 tl = tag_bits(ep, lane & 3);
    u32x4 o[8], wf[8][4];
#pragma unroll
    for (int k = 0; k < 8; ++k)
#pragma unroll
        for (int t = 0; t < 4; ++t) { const unsigned a = (wa ^ (t << 5)) + k * 4096; wf[k][t] = ltr2(a, a + 1024); }
    PIN(ob); PIN(rw); PIN(rr); PIN(oo); PIN(tl);
    SCHED_FENCE();
    const unsigned ha = WS_HINT + ((rt >> 1) * 8 + (lane & 7)) * 128;
    unsigned hint = w == 7 ? ep + 1 : ep;
    if (i >= 16) b_second(p, rws, ep, mid, hint, ha, xcd, li, lane, w STP_ARG);
#pragma unroll 1
    for (int it = 0; it < SPIN_CAP && !__all(hint == ep); ++it) { if (it) __builtin_amdgcn_s_sleep(2); FENCE(); hint = ld32(rws, ha); }
    if (i < 16) VMWAIT(0);
    BAR();
    STAMP(7);
#pragma unroll
    for (int k = 0; k < 8; ++k) o[k] = ld128s(rws, ob, k * 1024);
    const rsrc_t rout = mkrsrc(p.out, 2048 * 1024 * 2);
    f32x4 acc[4] = {Z4, Z4, Z4, Z4};
    unsigned m = 0;
#pragma unroll
    for (int k = 0; k < 8; ++k) {
        o[k] ^= tl;
#pragma unroll
        for (int t = 0; t < 4; ++t) acc[t] = mma(wf[k][t], o[k], acc[t]);
        m = or4(m, o[k]);
        SCHED_FENCE();
    }
    if (__builtin_expect(stale(m), 0)) {
#pragma unroll 1
        for (int it = 0; it < SPIN_CAP; ++it) {
            BACKOFF();
            m = 0;
#pragma unroll
            for (int k = 0; k < 8; ++k) o[k] = ld128s(rws, ob, k * 1024) ^ tl;
#pragma unroll
            for (int k = 0; k < 8; ++k) m = or4(m, o[k]);
            if (!stale(m)) break;
        }
#pragma unroll
        for (int t = 0; t < 4; ++t) acc[t] = Z4;
#pragma unroll
        for (int k = 0; k < 8; ++k)
#pragma unroll
            for (int t = 0; t < 4; ++t) acc[t] = mma(wf[k][t], o[k], acc[t]);
    }
    STAMP(8);
    if (i >= 16) {
        __builtin_amdgcn_raw_buffer_store_b128(mid.y0, rout, mid.oo, 0, OUTB_AUX);
        __builtin_amdgcn_raw_buffer_store_b128(mid.y1, rout, mid.oo, 128 * 2048, OUTB_AUX);
    }
#pragma unroll
    for (int t = 0; t < 4; ++t) lwf128(rw + t * 64, acc[t]);
    BAR();
    const f32x4 y0 = lrf128(rr) + lrf128(rr + 17408), y1 = lrf128(rr + 16) + lrf128(rr + 17408 + 16);
    __builtin_amdgcn_raw_buffer_store_b128(cat(pkv(y0), pkv(y1)), rout, oo, 0, OUT_AUX);
    if (b == 128 && tid == 0) st32(rws, WS_EPOCH, ep);
    STAMP(9);
    if ((i >= 16) == CALROLE && stp.t) while (__builtin_amdgcn_s_memrealtime() - stp.t < 4000) __builtin_amdgcn_s_sleep(1);
}

extern "C" void kernel_launch(void* const* d_in, const int* in_sizes, int n_in,
                              void* d_out, int out_size, void* d_ws, size_t ws_size,
                              hipStream_t stream) {
    Params p;
    p.ax = (const unsigned short*)d_in[0]; p.wqkv = (const unsigned short*)d_in[1]; p.wo = (const unsigned short*)d_in[2];
    p.bx = (const unsigned short*)d_in[3]; p.wr = (const unsigned short*)d_in[4]; p.wi = (const unsigned short*)d_in[5];
    p.out = (unsigned short*)d_out; p.ws = (unsigned char*)d_ws;
    fused_kernel<<<256, 512, 0, stream>>>(p);
}
```

```cpp
#define CALPT 0
#define CALROLE 0
#include <hip/hip_runtime.h>
#include <stdint.h>

typedef short    s16x4 __attribute__((ext_vector_type(4)));
typedef __bf16   bf16x8 __attribute__((ext_vector_type(8)));
typedef __bf16   bf16x2 __attribute__((ext_vector_type(2)));
typedef float    f32x2 __attribute__((ext_vector_type(2)));
typedef float    f32x4 __attribute__((ext_vector_type(4)));
typedef float    f32x16 __attribute__((ext_vector_type(16)));
typedef unsigned u32x2 __attribute__((ext_vector_type(2)));
typedef unsigned u32x4 __attribute__((ext_vector_type(4)));
typedef __amdgpu_buffer_rsrc_t rsrc_t;
#define LAS __attribute__((address_space(3)))
#define DI __device__ __forceinline__

constexpr unsigned WS_EPOCH = 0;
constexpr unsigned WS_HINT = 4096;
constexpr unsigned PART_STRIDE = 2688, WS_PART = 32768;
constexpr unsigned TB = 17024;
constexpr unsigned WS_T1 = 1u << 19;
constexpr unsigned WS_O = WS_T1 + 256 * TB, WS_END = WS_O + (1u << 21);
constexpr int SPIN_CAP = 1 << 15;
#define RSLEEP 2
#define BACKOFF() do { __builtin_amdgcn_s_sleep(RSLEEP); FENCE(); } while (0)
constexpr unsigned LDS_BYTES = 163840;
#define OUTB_AUX 16
#define OUT_AUX 16
struct Stamps { unsigned long long t; };
#define STAMP(i) do { if ((i) == CALPT) asm volatile("s_memrealtime %0\n\ts_waitcnt lgkmcnt(0)" : "=s"(stp.t) :: "memory"); } while (0)
#define STP_PARAM , Stamps& stp
#define STP_ARG , stp

struct Params {
    const unsigned short *ax, *wqkv, *wo, *bx, *wr, *wi;
    unsigned short* out;
    unsigned char* ws;
};

DI rsrc_t mkrsrc(const void* ptr, unsigned bytes) { return __builtin_amdgcn_make_buffer_rsrc((void*)ptr, 0, bytes, 0x00020000); }
DI u32x4 ld128(rsrc_t r, unsigned off)  { return __builtin_amdgcn_raw_buffer_load_b128(r, off, 0, 16); }
DI u32x2 ld64(rsrc_t r, unsigned off)   { return __builtin_amdgcn_raw_buffer_load_b64(r, off, 0, 16); }
DI unsigned ld32(rsrc_t r, unsigned off){ return __builtin_amdgcn_raw_buffer_load_b32(r, off, 0, 16); }
DI u32x2 ld64s(rsrc_t r, unsigned off, unsigned so)   { return __builtin_amdgcn_raw_buffer_load_b64(r, off, so, 16); }
DI u32x4 ld128s(rsrc_t r, unsigned off, unsigned so)  { return __builtin_amdgcn_raw_buffer_load_b128(r, off, so, 16); }
DI unsigned ld32s(rsrc_t r, unsigned off, unsigned so){ return __builtin_amdgcn_raw_buffer_load_b32(r, off, so, 16); }
DI void st128(rsrc_t r, unsigned off, u32x4 v) { __builtin_amdgcn_raw_buffer_store_b128(v, r, off, 0, 16); }
DI void st64(rsrc_t r, unsigned off, u32x2 v) { __builtin_amdgcn_raw_buffer_store_b64(v, r, off, 0, 16); }
DI void st32(rsrc_t r, unsigned off, unsigned v) { __builtin_amdgcn_raw_buffer_store_b32(v, r, off, 0, 16); }
#define FENCE() asm volatile("" ::: "memory")
#define BAR() asm volatile("s_waitcnt lgkmcnt(0)\n\ts_barrier" ::: "memory")
#define VMWAIT(n) asm volatile("s_waitcnt vmcnt(" #n ")" ::: "memory")
#define EWAIT() VMWAIT(0)
#define CWAIT() VMWAIT(0)
#define DWAIT() VMWAIT(0)
#define SWAP 0
#define PIN(x) asm volatile("" : "+v"(x))
#define SCHED_FENCE() __builtin_amdgcn_sched_barrier(0)
DI void dma(rsrc_t r, unsigned voff, unsigned soff, unsigned lds) {
    asm volatile("s_mov_b32 m0, %0\n\ts_nop 0\n\tbuffer_load_dwordx4 %1, %2, %3 offen lds"
                 :: "s"(lds), "v"(voff), "s"(r), "s"(soff) : "memory");
}
DI void dma4(rsrc_t r, unsigned v0, unsigned v1, unsigned v2, unsigned v3, unsigned soff, unsigned lds) {
    asm volatile("s_mov_b32 m0, %0\n\ts_nop 0\n\tbuffer_load_dwordx4 %1, %5, %6 offen lds\n\tbuffer_load_dwordx4 %2, %5, %6 offen offset:1024 lds\n\t"
                 "buffer_load_dwordx4 %3, %5, %6 offen offset:2048 lds\n\tbuffer_load_dwordx4 %4, %5, %6 offen offset:3072 lds"
                 :: "s"(lds), "v"(v0), "v"(v1), "v"(v2), "v"(v3), "s"(r), "s"(soff) : "memory");
}
DI void dma2(rsrc_t r, unsigned v0, unsigned v1, unsigned soff, unsigned lds) {
    asm volatile("s_mov_b32 m0, %0\n\ts_nop 0\n\tbuffer_load_dwordx4 %1, %3, %4 offen lds\n\tbuffer_load_dwordx4 %2, %3, %4 offen offset:1024 lds"
                 :: "s"(lds), "v"(v0), "v"(v1), "s"(r), "s"(soff) : "memory");
}
DI unsigned pk(float lo, float hi) { f32x2 v = {lo, hi}; return __builtin_bit_cast(unsigned, __builtin_convertvector(v, bf16x2)); }
DI u32x2 pkv(f32x4 v) { return u32x2{pk(v[0], v[1]), pk(v[2], v[3])}; }
DI u32x4 cat(u32x2 a, u32x2 b) { return u32x4{a[0], a[1], b[0], b[1]}; }
DI f32x4 mma(u32x4 a, u32x4 b, f32x4 c) {
    return __builtin_amdgcn_mfma_f32_16x16x32_bf16(__builtin_bit_cast(bf16x8, a), __builtin_bit_cast(bf16x8, b), c, 0, 0, 0); }
DI f32x16 mma32(u32x4 a, u32x4 b, f32x16 c) {
    return __builtin_amdgcn_mfma_f32_32x32x16_bf16(__builtin_bit_cast(bf16x8, a), __builtin_bit_cast(bf16x8, b), c, 0, 0, 0); }
DI f32x4 mma16(u32x2 a, u32x2 b, f32x4 c) {
    return __builtin_amdgcn_mfma_f32_16x16x16bf16_1k(__builtin_bit_cast(s16x4, a), __builtin_bit_cast(s16x4, b), c, 0, 0, 0); }
#define LP(T, a) ((LAS T*)(size_t)(a))
DI u32x4 lr128(unsigned a) { return *LP(const u32x4, a); }
DI f32x4 lrf128(unsigned a) { return *LP(const f32x4, a); }
DI u32x2 lr64(unsigned a) { return *LP(const u32x2, a); }
DI unsigned lr32(unsigned a) { return *LP(const unsigned, a); }
DI float bf(unsigned short v) { return __builtin_bit_cast(float, (unsigned)v << 16); }
DI float hi16(unsigned v) { return __builtin_bit_cast(float, v & 0xFFFF0000u); }
DI float lrbf(unsigned a) { return bf(*LP(const unsigned short, a)); }
DI void lw128(unsigned a, u32x4 v) { *LP(u32x4, a) = v; }
DI void lwf128(unsigned a, f32x4 v) { *LP(f32x4, a) = v; }
DI void lw64(unsigned a, u32x2 v) { *LP(u32x2, a) = v; }
DI void lw32(unsigned a, unsigned v) { *LP(unsigned, a) = v; }
DI u32x2 ltr(unsigned a) { return __builtin_bit_cast(u32x2, __builtin_amdgcn_ds_read_tr16_b64_v4i16(LP(s16x4, a))); }
DI u32x4 ltr2(unsigned a, unsigned b) { return cat(ltr(a), ltr(b)); }
constexpr unsigned TM = 0x00010001u;
DI u32x4 tag_bits(unsigned ep, int unit, unsigned use = 2) {
    const unsigned t = ((ep << 2) | use) >> (8 * unit);
    u32x4 m;
#pragma unroll
    for (int k = 0; k < 4; ++k) m[k] = ((t >> (2 * k)) & 1u) | (((t >> (2 * k + 1)) & 1u) << 16);
    return m;
}
constexpr float TAG_GAIN = 1.0027f;
DI unsigned pkt1(float a, float b, unsigned bits) { return __builtin_amdgcn_bitop3_b32(pk(a, b), TM, bits, 0xBA); }
DI u32x4 zipt(f32x4 a, f32x4 b, u32x4 t) { return u32x4{pkt1(a[0], b[0], t[0]), pkt1(a[1], b[1], t[1]), pkt1(a[2], b[2], t[2]), pkt1(a[3], b[3], t[3])}; }
DI u32x4 catt(f32x4 a, f32x4 b, u32x4 t) { return u32x4{pkt1(a[0], a[1], t[0]), pkt1(a[2], a[3], t[1]), pkt1(b[0], b[1], t[2]), pkt1(b[2], b[3], t[3])}; }
DI unsigned or4(unsigned m, u32x4 v) { return m | v[0] | v[1] | v[2] | v[3]; }
DI bool stale(unsigned m) { return __any(m & TM); }
constexpr float PHI_EPS = 1e-6f;
DI u32x2 phi4(f32x4 u) {
    f32x4 a = u * 1.44269504f - PHI_EPS * 1.44269504f, e, m;
    PIN(a);
#pragma unroll
    for (int r = 0; r < 4; ++r) {
        e[r] = __builtin_amdgcn_fmed3f(__builtin_amdgcn_exp2f(a[r]), 0.f, 1.f);
        m[r] = __builtin_elementwise_maximum(u[r], PHI_EPS);
    }
    return pkv(m + e);
}
constexpr f32x4 Z4 = {0.f, 0.f, 0.f, 0.f};

struct Mat { u32x4 re, im; };
struct Cx  { f32x4 re, im; };
DI Cx angles(int n0, int dn, int mask, float inv) {
    Cx t;
#pragma unroll
    for (int r = 0; r < 4; ++r) { float a = (float)((n0 + r * dn) & mask) * inv;
        t.re[r] = __builtin_amdgcn_cosf(a); t.im[r] = __builtin_amdgcn_sinf(a); }
    return t;
}
DI u32x4 zip(f32x4 a, f32x4 b) { return u32x4{pk(a[0], b[0]), pk(a[1], b[1]), pk(a[2], b[2]), pk(a[3], b[3])}; }
DI Mat tomat(Cx t) { return Mat{zip(t.re, t.im), zip(-t.im, t.re)}; }
DI Cx dftA(u32x4 d, const Mat& f) { return Cx{mma(d, f.re, Z4), mma(d, f.im, Z4)}; }
DI Cx dftB(u32x4 d, const Mat& f) { return Cx{mma(f.re, d, Z4), mma(f.im, d, Z4)}; }
DI u32x4 twpk(Cx v, const Cx& w) { return zip(v.re * w.re + v.im * w.im, v.im * w.re - v.re * w.im); }
DI u32x4 pk4(Cx v)  { return zip(v.re, v.im); }
DI u32x4 pk4c(Cx v) { return zip(v.re, v.im) ^ 0x80000000u; }

DI void epoch_ask(unsigned& e, const unsigned char* ws) { asm volatile("s_load_dword %0, %1, 0x0" : "=s"(e) : "s"(ws) : "memory"); }
DI void epoch_get(unsigned& e) {
    unsigned r;
    asm volatile("s_waitcnt lgkmcnt(0)\n\ts_add_u32 %0, %1, 1" : "=s"(r) : "s"(e) : "memory", "scc");
    e = r;
}
DI f32x4 q4(const f32x16& v, int i) { return f32x4{v[4 * i], v[4 * i + 1], v[4 * i + 2], v[4 * i + 3]}; }
struct Frag { u32x4 a[2][2], b[2][3]; };
template <int J0, int J1> DI void frag_read(Frag& f, unsigned xa, unsigned wa) {
#pragma unroll
    for (int k = 0; k < 2; ++k) {
        f.a[k][0] = lr128(xa ^ (k * 32)); f.b[k][J0] = ltr2(wa + k * 10240 + J0 * 2048, wa + k * 10240 + J0 * 2048 + 512);
        f.a[k][1] = lr128((xa ^ (k * 32)) + 10240);
#pragma unroll
        for (int j = J0 + 1; j < J1; ++j) f.b[k][j] = ltr2(wa + k * 10240 + j * 2048, wa + k * 10240 + j * 2048 + 512);
    }
}
template <int J0, int J1> DI void frag_mma(const Frag& f, f32x16 (&acc)[2][3]) {
#pragma unroll
    for (int k = 0; k < 2; ++k)
#pragma unroll
        for (int j = J0; j < J1; ++j)
#pragma unroll
            for (int i = 0; i < 2; ++i) acc[i][j] = (j < 2) ? mma32(f.a[k][i], f.b[k][j], acc[i][j]) : mma32(f.b[k][j], f.a[k][i], acc[i][j]);
}
template <int NJ> DI void interleave() {
#pragma unroll
    for (int i = 0; i < 2 + 2 * NJ; ++i) { __builtin_amdgcn_sched_group_barrier(0x8, 1, 0); __builtin_amdgcn_sched_group_barrier(0x100, 2, 0); }
    __builtin_amdgcn_sched_group_barrier(0x8, 4, 0);
}
template <int J0, int J1> DI void tile_step(Frag& f0, Frag& f1, f32x16 (&acc)[2][3], unsigned xa, unsigned wa, int s, bool bar) {
    const unsigned slot = (s & 3) * 40960, nslot = ((s + 1) & 3) * 40960;
    SCHED_FENCE();
    frag_read<J0, J1>(f1, slot + (xa ^ 64), slot + wa + 20480); frag_mma<J0, J1>(f0, acc); interleave<J1 - J0>();
    SCHED_FENCE();
    if (bar) BAR();
    frag_read<J0, J1>(f0, nslot + xa, nslot + wa); frag_mma<J0, J1>(f1, acc); interleave<J1 - J0>();
}
constexpr unsigned CODE_BYTES = 23424;

constexpr unsigned SLOT = 40960;
#define HINT 1
constexpr unsigned A_RED = 65536;
DI void a_loader(const Params& p, int rp, int hg, int lane, int lw STP_PARAM) {
    const int r8 = lane >> 3, ch = lane & 7;
    const rsrc_t rx = mkrsrc(p.ax, 2048 * 512 * 2), rw = mkrsrc(p.wqkv, 512 * 1536 * 2);
    const unsigned vx = (rp * 128 + lw * 32 + r8) * 1024 + ((ch ^ (r8 >> 1)) << 4);
    const unsigned vw = (lw * 16 + r8) * 3072 + hg * 128 + ((ch ^ ((r8 & 2) << 1)) << 4);
    const unsigned vx1 = (vx ^ 64) + 7168, vx2 = vx + 14336, vx3 = (vx ^ 64) + 21504, vk = vw + 1024, vw1 = vw + 24576 - 1024;
#pragma unroll 1
    for (int s = 0; s < 8; ++s) {
        if (s == 0) STAMP(6);
        if (s >= 2) { if (s < 7) VMWAIT(10); else VMWAIT(8); asm volatile("s_barrier" ::: "memory"); }
        if (s == 2) STAMP(11);
        const unsigned so = s * (64 * 3072), l = (s & 3) * SLOT + lw * 10240;
        dma4(rx, vx, vx1, vx2, vx3, s * 128, l);
        dma4(rw, vk, vw + 24576, vw, vw1, so, l + 4096);
        if (s < 6) dma2(rw, vw, vw1, so, l + 8192);
    }
#pragma unroll 1
    for (int s = 6; s < 8; ++s) dma2(rw, vw, vw1, s * (64 * 3072), (s & 3) * SLOT + lw * 10240 + 8192);
}
DI u32x4 sel_matrix(int lane) {
    const int h = lane >> 5, r32 = lane & 31;
    u32x4 sel;
#pragma unroll
    for (int k = 0; k < 4; ++k)
        sel[k] = (r32 < 16 && ((r32 >> 2) & 1) == h && ((r32 >> 3) * 2 + ((r32 >> 1) & 1)) == k) ? ((r32 & 1) ? 0x3F800000u : 0x00003F80u) : 0u;
    return sel;
}
DI void sum_panels(rsrc_t rws, u32x4 (&d)[8], unsigned src, bool on, u32x4 tg, u32x4 sel, unsigned dst) {
    f32x16 sum = 0.f;
    unsigned m = 0;
#pragma unroll
    for (int j = 0; j < 8; ++j) { d[j] ^= tg; sum = mma32(sel, d[j], sum); m = or4(m, d[j]); SCHED_FENCE(); }
    lwf128(dst, q4(sum, 0)); lwf128(dst + 1024, q4(sum, 1));
    if (__builtin_expect(stale(m), 0)) {
#pragma unroll 1
        for (int it = 0; it < SPIN_CAP; ++it) {
            BACKOFF();
            m = 0;
            if (on) {
#pragma unroll
                for (int j = 0; j < 8; ++j) d[j] = ld128s(rws, src, j * 8 * PART_STRIDE) ^ tg;
            }
#pragma unroll
            for (int j = 0; j < 8; ++j) m = or4(m, d[j]);
            if (!stale(m)) break;
        }
        f32x16 s2 = 0.f;
#pragma unroll
        for (int j = 0; j < 8; ++j) s2 = mma32(sel, d[j], s2);
        lwf128(dst, q4(s2, 0)); lwf128(dst + 1024, q4(s2, 1));
    }
}
DI void a_loader_tail(const Params& p, rsrc_t rws, unsigned& ep, int hg, int lane, int lw, int ct STP_PARAM) {
    const int r8 = lane >> 3, ch = lane & 7;
    const rsrc_t ro = mkrsrc(p.wo, 512 * 512 * 2);
    const unsigned vo = (lw * 64 + r8) * 1024 + ct * 128 + ((ch ^ (r8 & 6)) << 4);
    VMWAIT(12);
#pragma unroll 1
    for (int u = 0; u < 2; ++u) {
        BAR();
#pragma unroll 1
        for (int q = 0; q < 8; ++q) dma(ro, vo, u * 262144 + q * 8192, u * 32768 + lw * 8192 + q * 1024);
        if (u == 0) VMWAIT(12);
    }
    VMWAIT(16);
    STAMP(1);
    BAR();
    epoch_get(ep);
    const int rh = lw >> 1, hp = lw & 1;
    unsigned src = WS_PART + hg * PART_STRIDE + rh * 64 * PART_STRIDE + 2048 + hp * 256 + ((lane >> 4) ^ 1) * 64 + (lane & 3) * 16;
    unsigned dst = A_RED + hp * 4096 + rh * 8192 + 2048 + lane * 16;
    u32x4 tg = tag_bits(ep, lane & 3), sel = sel_matrix(lane), d[8];
#pragma unroll
    for (int j = 0; j < 8; ++j) { d[j] = tg; PIN(d[j]); }
    PIN(src); PIN(dst); PIN(tg); PIN(sel);
    VMWAIT(0);
    BAR();
    __builtin_amdgcn_s_sleep(1);
    if ((lane & 15) < 4) {
#pragma unroll
        for (int j = 0; j < 8; ++j) d[j] = ld128s(rws, src, j * 8 * PART_STRIDE);
    }
    sum_panels(rws, d, src, (lane & 15) < 4, tg, sel, dst);
    BAR();
}
DI void a_tile(rsrc_t rws, unsigned ep, int rp, int hg, int lane, int w STP_PARAM) {
    const int g = lane >> 4, c16 = lane & 15, h = lane >> 5, r32 = lane & 31, rh = w >> 1, hp = w & 1;
    const unsigned xa = rh * 20480 + r32 * 128 + ((h ^ ((r32 >> 1) & 7)) << 4);
    const unsigned wa = 4096 + (8 * h + (c16 >> 2)) * 128 + ((hp ^ (c16 >> 3)) << 6) + (g & 1) * 32 + 8 * (lane & 3);
    f32x16 acc[2][3];
#pragma unroll
    for (int i = 0; i < 2; ++i)
#pragma unroll
        for (int j = 0; j < 3; ++j) { acc[i][j] = j == 1 ? 0.f : PHI_EPS; PIN(acc[i][j]); }
    const unsigned pg = WS_PART + hg * PART_STRIDE + hp * 1024;
    unsigned red = A_RED + hp * 4096 + lane * 16;
    unsigned pst = pg + rp * 8 * PART_STRIDE + (rh ? 2048 - hp * 768 + g * 64 + c16 * 16 : lane * 16);
    if (rh && c16 >= 4) pst = WS_END;
    unsigned pk0 = pg + rh * 64 * PART_STRIDE + lane * 16;
    unsigned ob = WS_O + ((rp * 8 + rh * 4 + (g & 1)) * 16 + hg * 2 + hp) * 1024 + (h * 16 + c16) * 16;
    u32x4 tg = tag_bits(ep, lane & 3), sel = sel_matrix(lane);
    PIN(red); PIN(pst); PIN(pk0); PIN(ob); PIN(tg); PIN(sel);
    Frag f0, f1;
    BAR();
    frag_read<0, 3>(f0, xa, wa);
#pragma unroll 1
    for (int s = 0; s < 6; ++s) tile_step<0, 3>(f0, f1, acc, xa, wa, s, true);
    tile_step<0, 2>(f0, f1, acc, xa, wa, 6, true);
    SCHED_FENCE();
    frag_read<0, 2>(f1, 3 * SLOT + (xa ^ 64), 3 * SLOT + wa + 20480); frag_mma<0, 1>(f0, acc); interleave<1>();
    SCHED_FENCE();
    frag_mma<0, 1>(f1, acc);
    SCHED_FENCE();
    STAMP(1);
    {
        frag_mma<1, 2>(f0, acc); frag_mma<1, 2>(f1, acc);
        u32x4 A = cat(phi4(q4(acc[0][0], 0)), phi4(q4(acc[0][0], 1)));
#pragma unroll
        for (int i = 0; i < 8; ++i) { __builtin_amdgcn_sched_group_barrier(0x8, 1, 0); __builtin_amdgcn_sched_group_barrier(0x402, 5, 0); }
        SCHED_FENCE();
        f32x16 kv = 0.f, zz = 0.f;
        const u32x4 ones = {0x3F803F80u, 0x3F803F80u, 0x3F803F80u, 0x3F803F80u};
#pragma unroll
        for (int n = 0; n < 4; ++n) {
            zz = mma32(A, ones, zz);
            kv = mma32(A, cat(pkv(q4(acc[n >> 1][1], 2 * (n & 1))), pkv(q4(acc[n >> 1][1], 2 * (n & 1) + 1))), kv);
            if (n < 3) A = cat(phi4(q4(acc[(n + 1) >> 1][0], 2 * ((n + 1) & 1))), phi4(q4(acc[(n + 1) >> 1][0], 2 * ((n + 1) & 1) + 1)));
            SCHED_FENCE();
        }
        f32x4 k0 = (g & 1) ? q4(kv, 2) : q4(kv, 0), k1 = (g & 1) ? q4(kv, 3) : q4(kv, 1);
        f32x4 z0 = (g & 1) ? q4(zz, 2) : q4(zz, 0), z1 = (g & 1) ? q4(zz, 3) : q4(zz, 1);
        if (rh) { lwf128(red, k0); lwf128(red + 1024, k1); k0 = z0; k1 = z1; } else { lwf128(red + 2048, z0); lwf128(red + 3072, z1); }
        BAR();
        st128(rws, pst, catt(k0 + lrf128(red + rh * 2048), k1 + lrf128(red + rh * 2048 + 1024), tg));
    }
    STAMP(2);
    SCHED_FENCE();
    frag_read<2, 3>(f0, 2 * SLOT + xa, 2 * SLOT + wa);
#pragma unroll 1
    for (int s = 6; s < 8; ++s) tile_step<2, 3>(f0, f1, acc, xa, wa, s, false);
    SCHED_FENCE();
    CWAIT();
    BAR();
    STAMP(3);
    u32x4 dk[8];
#pragma unroll
    for (int j = 0; j < 8; ++j) dk[j] = ld128s(rws, pk0, j * 8 * PART_STRIDE);
    SCHED_FENCE();
    u32x4 pq[2][2];
#pragma unroll
    for (int i = 0; i < 2; ++i)
#pragma unroll
        for (int b = 0; b < 2; ++b) { pq[i][b] = cat(phi4(q4(acc[i][2], 2 * b)), phi4(q4(acc[i][2], 2 * b + 1))); PIN(pq[i][b]); }
    SCHED_FENCE();
    sum_panels(rws, dk, pk0, true, tg, sel, red + 8192 * rh);
    BAR();
    if (HINT == 1 && w == 0 && lane == 0) st32(rws, WS_HINT + (rp * 8 + hg) * 128, ep);
    STAMP(4);
    const u32x4 KV = cat(pkv(lrf128(red) + lrf128(red + 8192)), pkv(lrf128(red + 1024) + lrf128(red + 9216)));
    u32x4 ZZ = cat(pkv(lrf128(red + 2048) + lrf128(red + 10240)), pkv(lrf128(red + 3072) + lrf128(red + 11264)));
#pragma unroll
    for (int k = 0; k < 4; ++k) ZZ[k] = __builtin_amdgcn_update_dpp(ZZ[k], ZZ[k], 0x114, 0xF, 0x2, false);
#pragma unroll
    for (int b = 0; b < 2; ++b) {
        const u32x4 A = ((g & 1) == b) ? KV : ZZ;
#pragma unroll
        for (int i = 0; i < 2; ++i) {
            const f32x16 nd = mma32(A, pq[i][b], 0.f);
            const float ri = TAG_GAIN * __builtin_amdgcn_rcpf(nd[b ? 0 : 8] + 1e-6f);
            st128(rws, ob + i * 32768 + b * 512, catt(q4(nd, 2 * b) * ri, q4(nd, 2 * b + 1) * ri, tg));
        }
    }
    if (HINT == 2 && w == 0 && lane == 0) st32(rws, WS_HINT + (rp * 8 + hg) * 128, ep);
    STAMP(5);
}

constexpr unsigned L_U = 98304;
constexpr unsigned L_S = 65536;
constexpr unsigned L_W = L_U + 19200;
constexpr unsigned L_SP = L_W + 20480;
constexpr unsigned L_C = L_SP + 512;
constexpr unsigned L_PW = L_C + 14336;
constexpr unsigned L_R = 16384;
struct BIn { const char* pf; size_t pm; unsigned po; };
DI void touch(const BIn& in, unsigned add) {
    asm volatile("s_mov_b64 exec, %2\n\tglobal_load_dword v255, %0, %1\n\ts_mov_b64 exec, -1" :: "v"(in.po), "s"(in.pf + add), "s"(in.pm) : "memory", "v255");
}
DI void b_loads(const Params& p, BIn& in, int xcd, int li, int tid, int lane, int w) {
    const int g = lane >> 4, c16 = lane & 15;
    in.pf = (w & 1) ? (const char*)p.ax : (const char*)p.wqkv;
    in.pm = (w & 1) ? 0xFFFFFFFFull : 0x0FFF0FFF0FFF0FFFull;
    in.po = (w & 1) ? ((xcd >> 1) * 512 + li * 32 + (lane & 31)) * 1024 + (1 + (w >> 1)) * 128
                    : (64 + 32 * w + li * 4 + g) * 3072 + (xcd & 1) * 512 + (c16 >> 2) * 1024 + (c16 & 3) * 128;
    if (w < 2) touch(in, 0);
}
#define BXP 1
struct BMid { Mat F; Cx tw1; u32x4 y0, y1; unsigned ra[4], oo, bp, u2, tb; };
DI void b_first(const Params& p, rsrc_t rws, unsigned& ep, BIn& in, BMid& mid, int xcd, int li, int tid, int lane, int w, int ct STP_PARAM) {
    const int g = lane >> 4, c16 = lane & 15;
    const int me = xcd * 16 + li;
    const int j1 = (li >> 3) ? (xcd ? 16 - xcd : 8) : xcd, kb = li & 7;
    const int r8 = lane >> 3, ch = lane & 7, jm = (16 - c16) & 15;
    const bool special = (kb == 0 && w == 0);
    auto rows = [&]()
    {
        const unsigned ws = __builtin_amdgcn_readfirstlane(w), vo = me * 8224 + lane * 16;
        asm volatile("s_mov_b32 m0, %0\n\ts_nop 0\n\tbuffer_load_dwordx4 %1, %3, 0 offen nt lds\n\tbuffer_load_dwordx4 %2, %3, 0 offen offset:1024 nt lds"
                     :: "s"(ws * 2048), "v"((256 * w + me) * 1024 + lane * 16), "v"((256 * w + me + 127) * 1024 + lane * 16), "s"(mkrsrc(p.bx, 2048 * 512 * 2)) : "memory");
        const rsrc_t rr = mkrsrc(p.wr, 2048 * 514), ri = mkrsrc(p.wi, 2048 * 514);
        asm volatile("s_mov_b32 m0, %0\n\ts_nop 0\n\tbuffer_load_dwordx4 %3, %4, %7 offen nt lds\n\t"
                     "s_mov_b32 m0, %1\n\ts_nop 0\n\tbuffer_load_dwordx4 %3, %5, %7 offen nt lds\n\t"
                     "s_mov_b32 m0, %2\n\ts_mov_b64 exec, 3\n\tbuffer_load_dwordx4 %3, %6, %8 offen nt lds\n\ts_mov_b64 exec, -1"
                     :: "s"(L_R + ws * 1024), "s"(L_R + 9216 + ws * 1024), "s"(L_R + 8192 + (ws & 1) * 9216), "v"(vo), "s"(rr), "s"(ri),
                        "s"(mkrsrc((ws & 1) ? p.wi : p.wr, 2048 * 514)), "s"(ws * 1024), "s"(8192) : "memory");
    };
    if ((w >> 1) == 1) touch(in, 0);
    if (BXP == 1) rows();
    if (w < 7) {
        const int k4 = 4 * w, sU = (0x0040004 >> k4) & 15, sQ = (0x0400040 >> k4) & 15;
        const int P = (((c16 & ((0xF7FF7FF >> k4) & 15)) << sU) + (sU ? j1 : 0)) << ((0x3007833 >> k4) & 15);
        const int n0 = P * (((4 * (g & ((0x3313133 >> k4) & 15))) << sQ) + (sQ ? j1 : 0)), dn = P << sQ;
        const Cx t = angles(n0, dn, 2047, 1.f / 2048);
        Mat m = Mat{__builtin_bit_cast(u32x4, t.re), __builtin_bit_cast(u32x4, t.im)};
        if (w < 4) m = tomat(t);
        if (w == 2 && (g >> 1) != (c16 >> 3)) m = Mat{u32x4{0, 0, 0, 0}, u32x4{0, 0, 0, 0}};
        lw128(L_C + w * 2048 + lane * 16, m.re); lw128(L_C + w * 2048 + 1024 + lane * 16, m.im);
    }
    if (BXP == 2) rows();
    const int km0 = 16 * w + c16, km1 = km0 ? 256 - km0 : 128;
    unsigned ca = L_C + lane * 16, xa = w * 2048 + g * 256 + c16 * 4;
    unsigned ua = L_U + (2 * w) * 1072 + g * 272 + c16 * 16;
    PIN(ca); PIN(xa); PIN(ua);
    if (w >= 4) touch(in, 0);
    epoch_get(ep);
    BAR();
    if (w < 6) touch(in, (w & 1) ? 512 : 256 * 3072);
    Mat F = {lr128(ca + 6144), lr128(ca + 7168)};
    Cx tw1 = {lrf128(ca + 12288), lrf128(ca + 13312)};
    unsigned a0 = L_U + 4 * g * 1072 + ((km0 >> 2) & 3) * 272 + (km0 >> 4) * 16 + (km0 & 3) * 4;
    unsigned a1s = L_U + 4 * g * 1072 + ((km1 >> 2) & 3) * 272 + (km1 >> 4) * 16 + (km1 & 3) * 4;
    unsigned sw0 = L_S + w * 2304 + c16 * 144 + g * 16, sw1 = L_S + w * 2304 + jm * 144 + 64 + g * 16;
    unsigned sr0 = L_S + w * 2304 + r8 * 144 + ch * 16, tb1 = WS_T1 + me * TB + w * 2048 + lane * 16;
    u32x4 tg = tag_bits(ep, g), tl = tag_bits(ep, lane & 3);
    const int cj = (tid >> 3) & 15, cc = 16 * (tid >> 7) + 4 * (ch & 3);
    unsigned ra = L_R + ((ch & 4) ? ((16 - cj) & 15) * 514 + (256 - cc) * 2 : cj * 514 + cc * 2), rs = (ch & 4) ? -2 : 2;
    const int lam = tid >> 3, s2a = (lam >> 5) * 32 + (lam & 3) * 8 + ((lam >> 2) & 7);
    unsigned tb2 = WS_T1 + s2a * TB + (kb * 16 + j1) * 128 + ch * 16;
    const int t16 = (w >> 2) * 32 + (lane & 8) * 8 + (lane & 3) * 8 + (w & 3) * 2 + ((lane >> 2) & 1);
    unsigned tb4 = WS_T1 + t16 * TB + 16384 + j1 * 16;
    PIN(tb2); PIN(tb4); PIN(a0); PIN(a1s); PIN(sw0); PIN(sw1); PIN(sr0); PIN(tb1); PIN(tg); PIN(tl); PIN(ra); PIN(rs);
    if (w >= 4 && w < 6) VMWAIT(2); else VMWAIT(1);
    BAR();
    STAMP(1);
    auto pair = [](unsigned a) { return (unsigned)*LP(const unsigned short, a) | (unsigned)*LP(const unsigned short, a + 9216) << 16; };
    {
        u32x4 v0, v1;
#pragma unroll
        for (int k = 0; k < 4; ++k) { v0[k] = (pair(ra + k * rs) & ~TM) | tl[k]; v1[k] = (pair(ra + (64 + k) * rs) & ~TM) | tl[k]; }
        st128(rws, WS_T1 + (128 + me) * TB + tid * 16, v0); st128(rws, WS_T1 + (128 + me) * TB + 8192 + tid * 16, v1);
        if (tid >= 496)
            st128(rws, WS_T1 + (128 + me) * TB + 16384 + c16 * 16,
                  u32x4{pair(L_R + jm * 514) & ~TM, pair(L_R + c16 * 514 + 512) & ~TM, pair(L_R + c16 * 514 + 256) & ~TM, ep});
    }
    const u32x4 x0 = {lr32(xa), lr32(xa + 64), lr32(xa + 128), lr32(xa + 192)}, x1 = {lr32(xa + 1024), lr32(xa + 1088), lr32(xa + 1152), lr32(xa + 1216)};
    lw128(ua, pk4(dftA(twpk(dftA(x0, F), tw1), F))); lw128(ua + 1072, pk4(dftA(twpk(dftA(x1, F), tw1), F)));
    BAR();
    {
        const u32x4 d0 = {lr32(a0), lr32(a0 + 1072), lr32(a0 + 2144), lr32(a0 + 3216)};
        const u32x4 d1 = {lr32(a1s), lr32(a1s + 1072), lr32(a1s + 2144), lr32(a1s + 3216)};
        const Cx c0 = dftA(d0, F), c1 = dftA(d1, F);
        lw128(sw0, zipt(c0.re, c0.im, tg)); lw128(sw1, zipt(c1.re, -c1.im, tg));
        const u32x4 s0 = lr128(sr0), s1 = lr128(sr0 + 8 * 144);
        st128(rws, tb1, s0); st128(rws, tb1 + 1024, s1);
        if (tid < 16) st128(rws, WS_T1 + me * TB + 16384 + jm * 16, u32x4{pk(c0.re[0], c0.im[0]) & ~TM, 0, 0, ep});
    }
    STAMP(2);
    asm volatile("s_mov_b64 exec, 0xFFFF\n\tglobal_load_dword v255, %0, %1\n\ts_mov_b64 exec, -1" :: "v"(((li & 3) * 128 + w * 16 + c16) * 1024 + ct * 128), "s"(p.wo) : "memory", "v255");
    u32x4 v0 = ld128s(rws, tb2, 128 * TB), v1 = ld128s(rws, tb2, 192 * TB), isp = {0, 0, 0, ep};
    if (kb == 0) isp = ld128s(rws, tb4, 128 * TB);
    FENCE();
    const rsrc_t ro = mkrsrc(p.wo, 512 * 512 * 2);
    const int pm = j1 ? 127 - s2a : (128 - s2a) & 127;
    unsigned wst = L_W + (ch & 3) * 16 + ((ch & 4) ? 10240 + pm * 80 : s2a * 80), wsd = L_W + (ch & 3) * 16 + ((ch & 4) ? 10240 + (pm ^ 64) * 80 : (s2a + 64) * 80);
    unsigned sb = L_U + ((ch & 3) * 2 + (ch >> 2)) * 1040 + ((lam >> 5) * 16 + ((lam >> 2) & 7)) * 16 + (lam & 3) * 4;
    unsigned vo = (w * 64 + r8) * 1024 + ct * 128 + ((ch ^ (r8 & 6)) << 4);
    unsigned vo1 = vo + 7168, vo2 = vo + 14336, vo3 = vo + 21504;
    PIN(wst); PIN(wsd); PIN(sb); PIN(vo); PIN(vo1); PIN(vo2); PIN(vo3);
    Mat F1 = {lr128(ca), lr128(ca + 1024)}, F2 = {lr128(ca + 2048), lr128(ca + 3072)}, G = {lr128(ca + 4096), lr128(ca + 5120)};
    Cx twa = {lrf128(ca + 8192), lrf128(ca + 9216)}, twb = {lrf128(ca + 10240), lrf128(ca + 11264)};
    unsigned ta = L_U + ((w >> 1) * 2 + 8 * (w & 1)) * 1040 + lane * 16;
    const u32x4 tw = tag_bits(ep, w >> 1, 1);
    unsigned t2 = (w & 1) ? ((lane & 8) ? tw[3] : tw[2]) : ((lane & 8) ? tw[1] : tw[0]);
    const int col = 2 * w + (g >> 1);
    const float th = (float)(16 * kb + col) * (1.f / 512);
    float sn = __builtin_amdgcn_sinf(th), cs = __builtin_amdgcn_cosf(th);
    unsigned wp = L_W + (c16 + 64 * (g & 1)) * 80 + col * 4;
    PIN(F1.re); PIN(F1.im); PIN(F2.re); PIN(F2.im); PIN(G.re); PIN(G.im); PIN(twa.re); PIN(twa.im); PIN(twb.re); PIN(twb.im);
    PIN(ta); PIN(t2); PIN(sn); PIN(cs); PIN(wp);
#pragma unroll
    for (int i = 0; i < 4; ++i) {
        const int km = 64 * g + 16 * i + c16, kq = km < 128 ? km : (256 - km) & 127;
        mid.ra[i] = L_U + ((km >> 7) * 16 + 2 * w) * 592 + (kq + 16 * (kq >> 6)) * 4;
        PIN(mid.ra[i]);
    }
    const int set = g & 1, k0 = 16 * (2 * w + (g >> 1));
    mid.oo = (256 * w + me) * 2048 + 1024 + lane * 16; mid.bp = ((lane & 3) * 16 + (lane >> 2)) * 4;
    mid.u2 = L_U + (set * 16 + (set ? jm : c16)) * 592 + (k0 + 16 * (k0 >> 6)) * 4;
    mid.tb = WS_T1 + me * TB + ((2 * w + (c16 >> 3)) * 16 + 4 * g) * 128 + ch * 16;
    PIN(mid.oo); PIN(mid.u2); PIN(mid.bp); PIN(mid.tb);
    {
        v0 ^= tl; v1 ^= tl;
#pragma unroll 1
        for (int it = 0; it < SPIN_CAP && (stale(or4(or4(0, v0), v1)) || __any(isp[3] != ep)); ++it) {
            BACKOFF();
            v0 = ld128s(rws, tb2, 128 * TB) ^ tl; v1 = ld128s(rws, tb2, 192 * TB) ^ tl;
            if (kb == 0) isp = ld128s(rws, tb4, 128 * TB);
        }
        lw128(wst, v0); lw128(wsd, v1);
        if (kb == 0) { lw32(L_PW + (j1 ? 127 - t16 : (128 - t16) & 127) * 16, isp[0]); lw64(L_PW + t16 * 16 + 8, u32x2{isp[1], isp[2]}); }
    }
    BAR();
    STAMP(3);
    u32x4 i0 = ld128(rws, tb2), i1 = ld128s(rws, tb2, 64 * TB);
    if (kb == 0) isp = ld128(rws, tb4);
    FENCE();
    Cx a1, bz, a2, bv;
    {
        f32x4 pr, pi, qr, qi;
#pragma unroll
        for (int r = 0; r < 4; ++r) {
            const unsigned pv = lr32(wp + r * 1280), qv = lr32(wp + r * 1280 + 10240);
            pr[r] = __builtin_bit_cast(float, pv << 16); pi[r] = __builtin_bit_cast(float, pv & 0xFFFF0000u);
            qr[r] = __builtin_bit_cast(float, qv << 16); qi[r] = __builtin_bit_cast(float, qv & 0xFFFF0000u);
        }
        const float sc = TAG_GAIN * TAG_GAIN * TAG_GAIN / 1048576;
        f32x4 rr, ri;
        if (special && g < 2) {
            f32x4 ar, ai, br, bi;
#pragma unroll
            for (int r = 0; r < 4; ++r) {
                const u32x4 e = lr128(L_PW + (c16 + 64 * g + 16 * r) * 16);
                ar[r] = bf(e[0]); ai[r] = hi16(e[0]); br[r] = bf(e[2]); bi[r] = hi16(e[2]); rr[r] = bf(e[3]); ri[r] = hi16(e[3]);
            }
            pr = 0.5f * (pr + ar); pi = 0.5f * (pi - ai); qr = 0.5f * (qr + br); qi = 0.5f * (qi - bi);
        }
        const f32x4 sr = (pr + qr) * sc, si = (pi - qi) * sc, dr = (pr - qr) * sc, di = (pi + qi) * sc;
        a1.re = sr - dr * sn; a1.im = si - di * sn; a2.re = sr + dr * sn; a2.im = si + di * sn;
        bz.re = dr * cs; bz.im = di * cs; bv = bz;
        if (special && g < 2) { a2.re = rr * (2.f * sc); a2.im = ri * (2.f * sc); bv.re = Z4; bv.im = Z4; }
    }
    PIN(a1.re); PIN(a1.im); PIN(a2.re); PIN(a2.im); PIN(bz.re); PIN(bz.im); PIN(bv.re); PIN(bv.im);
    i0 ^= tl; i1 ^= tl;
#pragma unroll 1
    for (int it = 0; it < SPIN_CAP && (stale(or4(or4(0, i0), i1)) || __any(isp[3] != ep)); ++it) {
        BACKOFF();
        i0 = ld128(rws, tb2) ^ tl; i1 = ld128s(rws, tb2, 64 * TB) ^ tl;
        if (kb == 0) isp = ld128(rws, tb4);
    }
    STAMP(10);
    lw32(sb, i0[0]); lw32(sb + 128, i0[1]); lw32(sb + 8320, i0[2]); lw32(sb + 8320 + 128, i0[3]);
    lw32(sb + 512, i1[0]); lw32(sb + 512 + 128, i1[1]); lw32(sb + 512 + 8320, i1[2]); lw32(sb + 512 + 8320 + 128, i1[3]);
    if (kb == 0) lw32(L_SP + ((t16 >> 5) * 8 + (t16 & 7)) * 16 + ((t16 >> 3) & 3) * 4, isp[0]);
    BAR();
    STAMP(4);
    const u32x4 zi = lr128(ta), vi = lr128(ta + 1040);
    dma4(ro, vo, vo1, vo2, vo3, 0, w * 8192);
    u32x4 zf = twpk(dftA(zi, F1), twa), vf = twpk(dftA(vi, F1), twa);
    u32x4 vp = vf;
    if (special) {
        vp = lr128(L_SP + (g * 8 + (c16 & 7)) * 16) ^ 0x80000000u;
        if (c16 >= 8) vp = vi;
        vp = twpk(dftA(vp, F1), twa);
    }
    PIN(zf); PIN(vf);
    dma4(ro, vo, vo1, vo2, vo3, 32768, w * 8192 + 4096);
    {
        const Cx Z = dftB(zf, G), V = dftB(vf, G);
        Cx Vz = V;
        if (special) Vz = dftB(vp, G);
        u32x4 zc = zip(a1.re * Z.re - a1.im * Z.im - bz.re * Vz.im - bz.im * Vz.re, bz.im * Vz.im - a1.re * Z.im - a1.im * Z.re - bz.re * Vz.re);
        u32x4 vc = zip(a2.re * V.re - a2.im * V.im + bv.re * Z.im + bv.im * Z.re, bv.re * Z.re - a2.re * V.im - a2.im * V.re - bv.im * Z.im);
        const Cx zo = dftB(twpk(dftA(zc, G), twb), F2), vb = dftB(twpk(dftA(vc, G), twb), F2);
        lw128(ta, zipt(zo.re, zo.im, u32x4(t2))); lw128(ta + 1040, zipt(vb.re, -vb.im, u32x4(t2)));
        VMWAIT(0);
        BAR();
        const u32x4 o0 = {lr32(sb), lr32(sb + 128), lr32(sb + 8320), lr32(sb + 8320 + 128)};
        const u32x4 o1 = {lr32(sb + 512), lr32(sb + 512 + 128), lr32(sb + 512 + 8320), lr32(sb + 512 + 8320 + 128)};
        st128(rws, tb2, o0); __builtin_amdgcn_raw_buffer_store_b128(o1, rws, tb2, 64 * TB, 16);
    }
    STAMP(5);
    mid.F = F; mid.tw1 = tw1;
    if (w >= 4) { EWAIT(); BAR(); }
}
DI void b_second(const Params& p, rsrc_t rws, unsigned ep, BMid& mid, unsigned& hint, unsigned ha, int xcd, int li, int lane, int w STP_PARAM) {
    const int g = lane >> 4, c16 = lane & 15, ch = lane & 7, jm = (16 - c16) & 15, me = xcd * 16 + li;
    const Mat& F = mid.F; const Cx& tw1 = mid.tw1;
    u32x4 tl = tag_bits(ep, lane & 3, 1);
    const unsigned (&ra)[4] = mid.ra; const unsigned bp = mid.bp, u2 = mid.u2, tb = mid.tb;
    PIN(tl);
    if (w < 4) {
        EWAIT();
        BAR();
        STAMP(6);
        u32x4 l0 = ld128(rws, tb) ^ tl, l1 = ld128(rws, tb + 128) ^ tl, l2 = ld128(rws, tb + 256) ^ tl, l3 = ld128(rws, tb + 384) ^ tl;
#pragma unroll 1
        for (int it = 0; it < SPIN_CAP && stale(or4(or4(or4(or4(0, l0), l1), l2), l3)); ++it) {
            BACKOFF();
            l0 = ld128(rws, tb) ^ tl; l1 = ld128(rws, tb + 128) ^ tl; l2 = ld128(rws, tb + 256) ^ tl; l3 = ld128(rws, tb + 384) ^ tl;
        }
        u32x4 t[4];
#pragma unroll
        for (int e = 0; e < 4; ++e) t[e] = pk4(dftA(u32x4{l0[e], l1[e], l2[e], l3[e]}, F));
#pragma unroll
        for (int r = 0; r < 4; ++r) lw128(u2 + r * 16, u32x4{t[0][r], t[1][r], t[2][r], t[3][r]});
    }
    BAR();
    STAMP(11);
    if (w == 7) hint = ld32(rws, ha);
    FENCE();
    {
        const u32x4 d0 = {lr32(ra[0]), lr32(ra[1]), lr32(ra[2]), lr32(ra[3])};
        const u32x4 d1 = {lr32(ra[0] + 592), lr32(ra[1] + 592), lr32(ra[2] + 592), lr32(ra[3] + 592)};
        u32x4 y0 = pk4c(dftA(twpk(dftA(d0, F), tw1), F)), y1 = pk4c(dftA(twpk(dftA(d1, F), tw1), F));
#pragma unroll
        for (int r = 0; r < 4; ++r) {
            y0[r] = __builtin_amdgcn_ds_bpermute(bp, y0[r]); y1[r] = __builtin_amdgcn_ds_bpermute(bp, y1[r]);
        }
        PIN(y0); PIN(y1);
        mid.y0 = y0; mid.y1 = y1;
        STAMP(13);
    }
}

__global__ void __launch_bounds__(512, 2) fused_kernel(Params p) {
    const int b = blockIdx.x ^ (SWAP * 128), xcd = b & 7, i = b >> 3, li = i & 15;
    const int tid = threadIdx.x, lane = tid & 63, w = __builtin_amdgcn_readfirstlane(tid >> 6);
    BIn in;
    {
        size_t code;
        asm volatile("s_getpc_b64 %0" : "=s"(code));
        asm volatile("s_mov_b64 exec, 1\n\tglobal_load_dword v255, %0, %1\n\ts_mov_b64 exec, -1" :: "v"(min((unsigned)(w * 32 + i) * 128u, CODE_BYTES - 128u)), "s"(code & ~(size_t)255) : "memory", "v255");
    }
    __shared__ __attribute__((aligned(16))) char smem[LDS_BYTES];
    asm volatile("" :: "s"((unsigned)(size_t)(LAS char*)smem));
    Stamps stp; stp.t = 0;
    STAMP(0);
    const int rp = (xcd >> 1) * 4 + (li & 3), hg = (xcd & 1) * 4 + (li >> 2), rt = xcd * 4 + (i & 3), ct = i >> 2;
    if (i < 16 && w >= 4) a_loader(p, rp, hg, lane, w - 4 STP_ARG);
    FENCE();
    if (i >= 16) b_loads(p, in, xcd, li, tid, lane, w);
    FENCE();
    const rsrc_t rws = mkrsrc(p.ws, WS_END);
    unsigned ep;
    epoch_ask(ep, p.ws);
    BMid mid;
    if (i >= 16) b_first(p, rws, ep, in, mid, xcd, li, tid, lane, w, ct STP_ARG);
    else if (w < 4) { epoch_get(ep); a_tile(rws, ep, rp, hg, lane, w STP_ARG); }
    else a_loader_tail(p, rws, ep, hg, lane, w - 4, ct STP_ARG);
    const int g = lane >> 4, c16 = lane & 15, q = c16 >> 2, kh = w >> 2;
    unsigned ob = WS_O + (rt * 4 + (w & 3)) * 16384 + kh * 8192 + lane * 16;
    unsigned wa = kh * 32768 + (16 * (g >> 1) + 4 * (g & 1) + q) * 128 + 8 * (lane & 3) + (((2 * g + (q >> 1)) & 3) << 5);
    const unsigned scr = 81920;
    unsigned rw = scr + kh * 17408 + ((w & 3) * 16 + c16) * 272 + g * 16, rr = scr + (tid >> 3) * 272 + (tid & 7) * 32;
    unsigned oo = (rt * 64 + (tid >> 3)) * 2048 + ct * 128 + (tid & 7) * 16;
    u32x4 tl = tag_bits(ep, lane & 3);
    u32x4 o[8], wf[8][4];
#pragma unroll
    for (int k = 0; k < 8; ++k)
#pragma unroll
        for (int t = 0; t < 4; ++t) { const unsigned a = (wa ^ (t << 5)) + k * 4096; wf[k][t] = ltr2(a, a + 1024); }
    PIN(ob); PIN(rw); PIN(rr); PIN(oo); PIN(tl);
    SCHED_FENCE();
    const unsigned ha = WS_HINT + ((rt >> 1) * 8 + (lane & 7)) * 128;
    unsigned hint = ep + 1;
    if (i >= 16) b_second(p, rws, ep, mid, hint, ha, xcd, li, lane, w STP_ARG);
    if (w == 7) {
#pragma unroll 1
        for (int it = 0; it < SPIN_CAP && !__all(hint == ep); ++it) { if (it) __builtin_amdgcn_s_sleep(2); FENCE(); hint = ld32(rws, ha); }
    }
    if (i < 16) DWAIT();
    BAR();
    STAMP(7);
#pragma unroll
    for (int k = 0; k < 8; ++k) o[k] = ld128s(rws, ob, k * 1024);
    const rsrc_t rout = mkrsrc(p.out, 2048 * 1024 * 2);
    f32x4 acc[4] = {Z4, Z4, Z4, Z4};
    unsigned m = 0;
#pragma unroll
    for (int k = 0; k < 8; ++k) {
        o[k] ^= tl;
#pragma unroll
        for (int t = 0; t < 4; ++t) acc[t] = mma(wf[k][t], o[k], acc[t]);
        m = or4(m, o[k]);
        SCHED_FENCE();
    }
    if (__builtin_expect(stale(m), 0)) {
#pragma unroll 1
        for (int it = 0; it < SPIN_CAP; ++it) {
            BACKOFF();
            m = 0;
#pragma unroll
            for (int k = 0; k < 8; ++k) o[k] = ld128s(rws, ob, k * 1024) ^ tl;
#pragma unroll
            for (int k = 0; k < 8; ++k) m = or4(m, o[k]);
            if (!stale(m)) break;
        }
#pragma unroll
        for (int t = 0; t < 4; ++t) acc[t] = Z4;
#pragma unroll
        for (int k = 0; k < 8; ++k)
#pragma unroll
            for (int t = 0; t < 4; ++t) acc[t] = mma(wf[k][t], o[k], acc[t]);
    }
    STAMP(8);
#pragma unroll
    for (int t = 0; t < 4; ++t) lwf128(rw + t * 64, acc[t]);
    if (i >= 16) {
        __builtin_amdgcn_raw_buffer_store_b128(mid.y0, rout, mid.oo, 0, OUTB_AUX);
        __builtin_amdgcn_raw_buffer_store_b128(mid.y1, rout, mid.oo, 128 * 2048, OUTB_AUX);
    }
    BAR();
    const f32x4 y0 = lrf128(rr) + lrf128(rr + 17408), y1 = lrf128(rr + 16) + lrf128(rr + 17408 + 16);
    __builtin_amdgcn_raw_buffer_store_b128(cat(pkv(y0), pkv(y1)), rout, oo, 0, OUT_AUX);
    if (b == 128 && tid == 256) st32(rws, WS_EPOCH, ep);
    STAMP(9);
    if ((i >= 16) == CALROLE && stp.t) while (__builtin_amdgcn_s_memrealtime() - stp.t < 4000) __builtin_amdgcn_s_sleep(1);
}

extern "C" void kernel_launch(void* const* d_in, const int* in_sizes, int n_in,
                              void* d_out, int out_size, void* d_ws, size_t ws_size,
                              hipStream_t stream) {
    Params p;
    p.ax = (const unsigned short*)d_in[0]; p.wqkv = (const unsigned short*)d_in[1]; p.wo = (const unsigned short*)d_in[2];
    p.bx = (const unsigned short*)d_in[3]; p.wr = (const unsigned short*)d_in[4]; p.wi = (const unsigned short*)d_in[5];
    p.out = (unsigned short*)d_out; p.ws = (unsigned char*)d_ws;
    fused_kernel<<<256, 512, 0, stream>>>(p);
}
```

```cpp
#define AT 2
#define TL 1
#include <hip/hip_runtime.h>
#include <stdint.h>

typedef short    s16x4 __attribute__((ext_vector_type(4)));
typedef __bf16   bf16x8 __attribute__((ext_vector_type(8)));
typedef __bf16   bf16x2 __attribute__((ext_vector_type(2)));
typedef float    f32x2 __attribute__((ext_vector_type(2)));
typedef float    f32x4 __attribute__((ext_vector_type(4)));
typedef float    f32x16 __attribute__((ext_vector_type(16)));
typedef unsigned u32x2 __attribute__((ext_vector_type(2)));
typedef unsigned u32x4 __attribute__((ext_vector_type(4)));
typedef __amdgpu_buffer_rsrc_t rsrc_t;
#define LAS __attribute__((address_space(3)))
#define DI __device__ __forceinline__

constexpr unsigned WS_EPOCH = 0;
constexpr unsigned WS_HINT = 4096;
constexpr unsigned PART_STRIDE = 2688, WS_PART = 32768;
constexpr unsigned TB = 17024;
constexpr unsigned WS_T1 = 1u << 19;
constexpr unsigned WS_O = WS_T1 + 256 * TB, WS_END = WS_O + (1u << 21);
constexpr int SPIN_CAP = 1 << 15;
#define RSLEEP 2
#define BACKOFF() do { __builtin_amdgcn_s_sleep(RSLEEP); FENCE(); } while (0)
constexpr unsigned LDS_BYTES = 163840;
#define OUTB_AUX 16
#define OUT_AUX 16
#define STAMP(i) do {} while (0)
#define STP_PARAM
#define STP_ARG

struct Params {
    const unsigned short *ax, *wqkv, *wo, *bx, *wr, *wi;
    unsigned short* out;
    unsigned char* ws;
};

DI rsrc_t mkrsrc(const void* ptr, unsigned bytes) { return __builtin_amdgcn_make_buffer_rsrc((void*)ptr, 0, bytes, 0x00020000); }
DI u32x4 ld128(rsrc_t r, unsigned off)  { return __builtin_amdgcn_raw_buffer_load_b128(r, off, 0, 16); }
DI u32x2 ld64(rsrc_t r, unsigned off)   { return __builtin_amdgcn_raw_buffer_load_b64(r, off, 0, 16); }
DI unsigned ld32(rsrc_t r, unsigned off){ return __builtin_amdgcn_raw_buffer_load_b32(r, off, 0, 16); }
DI u32x2 ld64s(rsrc_t r, unsigned off, unsigned so)   { return __builtin_amdgcn_raw_buffer_load_b64(r, off, so, 16); }
DI u32x4 ld128s(rsrc_t r, unsigned off, unsigned so)  { return __builtin_amdgcn_raw_buffer_load_b128(r, off, so, 16); }
DI unsigned ld32s(rsrc_t r, unsigned off, unsigned so){ return __builtin_amdgcn_raw_buffer_load_b32(r, off, so, 16); }
DI void st128(rsrc_t r, unsigned off, u32x4 v) { __builtin_amdgcn_raw_buffer_store_b128(v, r, off, 0, 16); }
DI void st64(rsrc_t r, unsigned off, u32x2 v) { __builtin_amdgcn_raw_buffer_store_b64(v, r, off, 0, 16); }
DI void st32(rsrc_t r, unsigned off, unsigned v) { __builtin_amdgcn_raw_buffer_store_b32(v, r, off, 0, 16); }
#define FENCE() asm volatile("" ::: "memory")
#define BAR() asm volatile("s_waitcnt lgkmcnt(0)\n\ts_barrier" ::: "memory")
#define VMWAIT(n) asm volatile("s_waitcnt vmcnt(" #n ")" ::: "memory")
#define EWAIT() VMWAIT(0)
#define CWAIT() VMWAIT(0)
#define DWAIT() VMWAIT(0)
#define SWAP 0
#define PIN(x) asm volatile("" : "+v"(x))
#define SCHED_FENCE() __builtin_amdgcn_sched_barrier(0)
DI void dma(rsrc_t r, unsigned voff, unsigned soff, unsigned lds) {
    asm volatile("s_mov_b32 m0, %0\n\ts_nop 0\n\tbuffer_load_dwordx4 %1, %2, %3 offen lds"
                 :: "s"(lds), "v"(voff), "s"(r), "s"(soff) : "memory");
}
DI void dma4(rsrc_t r, unsigned v0, unsigned v1, unsigned v2, unsigned v3, unsigned soff, unsigned lds) {
    asm volatile("s_mov_b32 m0, %0\n\ts_nop 0\n\tbuffer_load_dwordx4 %1, %5, %6 offen lds\n\tbuffer_load_dwordx4 %2, %5, %6 offen offset:1024 lds\n\t"
                 "buffer_load_dwordx4 %3, %5, %6 offen offset:2048 lds\n\tbuffer_load_dwordx4 %4, %5, %6 offen offset:3072 lds"
                 :: "s"(lds), "v"(v0), "v"(v1), "v"(v2), "v"(v3), "s"(r), "s"(soff) : "memory");
}
DI void dma2(rsrc_t r, unsigned v0, unsigned v1, unsigned soff, unsigned lds) {
    asm volatile("s_mov_b32 m0, %0\n\ts_nop 0\n\tbuffer_load_dwordx4 %1, %3, %4 offen lds\n\tbuffer_load_dwordx4 %2, %3, %4 offen offset:1024 lds"
                 :: "s"(lds), "v"(v0), "v"(v1), "s"(r), "s"(soff) : "memory");
}
DI unsigned pk(float lo, float hi) { f32x2 v = {lo, hi}; return __builtin_bit_cast(unsigned, __builtin_convertvector(v, bf16x2)); }
DI u32x2 pkv(f32x4 v) { return u32x2{pk(v[0], v[1]), pk(v[2], v[3])}; }
DI u32x4 cat(u32x2 a, u32x2 b) { return u32x4{a[0], a[1], b[0], b[1]}; }
DI f32x4 mma(u32x4 a, u32x4 b, f32x4 c) {
    return __builtin_amdgcn_mfma_f32_16x16x32_bf16(__builtin_bit_cast(bf16x8, a), __builtin_bit_cast(bf16x8, b), c, 0, 0, 0); }
DI f32x16 mma32(u32x4 a, u32x4 b, f32x16 c) {
    return __builtin_amdgcn_mfma_f32_32x32x16_bf16(__builtin_bit_cast(bf16x8, a), __builtin_bit_cast(bf16x8, b), c, 0, 0, 0); }
DI f32x4 mma16(u32x2 a, u32x2 b, f32x4 c) {
    return __builtin_amdgcn_mfma_f32_16x16x16bf16_1k(__builtin_bit_cast(s16x4, a), __builtin_bit_cast(s16x4, b), c, 0, 0, 0); }
#define LP(T, a) ((LAS T*)(size_t)(a))
DI u32x4 lr128(unsigned a) { return *LP(const u32x4, a); }
DI f32x4 lrf128(unsigned a) { return *LP(const f32x4, a); }
DI u32x2 lr64(unsigned a) { return *LP(const u32x2, a); }
DI unsigned lr32(unsigned a) { return *LP(const unsigned, a); }
DI float bf(unsigned short v) { return __builtin_bit_cast(float, (unsigned)v << 16); }
DI float hi16(unsigned v) { return __builtin_bit_cast(float, v & 0xFFFF0000u); }
DI float lrbf(unsigned a) { return bf(*LP(const unsigned short, a)); }
DI void lw128(unsigned a, u32x4 v) { *LP(u32x4, a) = v; }
DI void lwf128(unsigned a, f32x4 v) { *LP(f32x4, a) = v; }
DI void lw64(unsigned a, u32x2 v) { *LP(u32x2, a) = v; }
DI void lw32(unsigned a, unsigned v) { *LP(unsigned, a) = v; }
DI u32x2 ltr(unsigned a) { return __builtin_bit_cast(u32x2, __builtin_amdgcn_ds_read_tr16_b64_v4i16(LP(s16x4, a))); }
DI u32x4 ltr2(unsigned a, unsigned b) { return cat(ltr(a), ltr(b)); }
constexpr unsigned TM = 0x00010001u;
DI u32x4 tag_bits(unsigned ep, int unit, unsigned use = 2) {
    const unsigned t = ((ep << 2) | use) >> (8 * unit);
    u32x4 m;
#pragma unroll
    for (int k = 0; k < 4; ++k) m[k] = ((t >> (2 * k)) & 1u) | (((t >> (2 * k + 1)) & 1u) << 16);
    return m;
}
constexpr float TAG_GAIN = 1.0027f;
DI unsigned pkt1(float a, float b, unsigned bits) { return __builtin_amdgcn_bitop3_b32(pk(a, b), TM, bits, 0xBA); }
DI u32x4 zipt(f32x4 a, f32x4 b, u32x4 t) { return u32x4{pkt1(a[0], b[0], t[0]), pkt1(a[1], b[1], t[1]), pkt1(a[2], b[2], t[2]), pkt1(a[3], b[3], t[3])}; }
DI u32x4 catt(f32x4 a, f32x4 b, u32x4 t) { return u32x4{pkt1(a[0], a[1], t[0]), pkt1(a[2], a[3], t[1]), pkt1(b[0], b[1], t[2]), pkt1(b[2], b[3], t[3])}; }
DI unsigned or4(unsigned m, u32x4 v) { return m | v[0] | v[1] | v[2] | v[3]; }
DI bool stale(unsigned m) { return __any(m & TM); }
constexpr float PHI_EPS = 1e-6f;
DI u32x2 phi4(f32x4 u) {
    f32x4 a = u * 1.44269504f - PHI_EPS * 1.44269504f, e, m;
    PIN(a);
#pragma unroll
    for (int r = 0; r < 4; ++r) {
        e[r] = __builtin_amdgcn_fmed3f(__builtin_amdgcn_exp2f(a[r]), 0.f, 1.f);
        m[r] = __builtin_elementwise_maximum(u[r], PHI_EPS);
    }
    return pkv(m + e);
}
constexpr f32x4 Z4 = {0.f, 0.f, 0.f, 0.f};

struct Mat { u32x4 re, im; };
struct Cx  { f32x4 re, im; };
DI Cx angles(int n0, int dn, int mask, float inv) {
    Cx t;
#pragma unroll
    for (int r = 0; r < 4; ++r) { float a = (float)((n0 + r * dn) & mask) * inv;
        t.re[r] = __builtin_amdgcn_cosf(a); t.im[r] = __builtin_amdgcn_sinf(a); }
    return t;
}
DI u32x4 zip(f32x4 a, f32x4 b) { return u32x4{pk(a[0], b[0]), pk(a[1], b[1]), pk(a[2], b[2]), pk(a[3], b[3])}; }
DI Mat tomat(Cx t) { return Mat{zip(t.re, t.im), zip(-t.im, t.re)}; }
DI Cx dftA(u32x4 d, const Mat& f) { return Cx{mma(d, f.re, Z4), mma(d, f.im, Z4)}; }
DI Cx dftB(u32x4 d, const Mat& f) { return Cx{mma(f.re, d, Z4), mma(f.im, d, Z4)}; }
DI u32x4 twpk(Cx v, const Cx& w) { return zip(v.re * w.re + v.im * w.im, v.im * w.re - v.re * w.im); }
DI u32x4 pk4(Cx v)  { return zip(v.re, v.im); }
DI u32x4 pk4c(Cx v) { return zip(v.re, v.im) ^ 0x80000000u; }

DI void epoch_ask(unsigned& e, const unsigned char* ws) { asm volatile("s_load_dword %0, %1, 0x0" : "=s"(e) : "s"(ws) : "memory"); }
DI void epoch_get(unsigned& e) {
    unsigned r;
    asm volatile("s_waitcnt lgkmcnt(0)\n\ts_add_u32 %0, %1, 1" : "=s"(r) : "s"(e) : "memory", "scc");
    e = r;
}
DI f32x4 q4(const f32x16& v, int i) { return f32x4{v[4 * i], v[4 * i + 1], v[4 * i + 2], v[4 * i + 3]}; }
struct Frag { u32x4 a[2][2], b[2][3]; };
template <int J0, int J1> DI void frag_read(Frag& f, unsigned xa, unsigned wa) {
#pragma unroll
    for (int k = 0; k < 2; ++k) {
        f.a[k][0] = lr128(xa ^ (k * 32)); f.b[k][J0] = ltr2(wa + k * 10240 + J0 * 2048, wa + k * 10240 + J0 * 2048 + 512);
        f.a[k][1] = lr128((xa ^ (k * 32)) + 10240);
#pragma unroll
        for (int j = J0 + 1; j < J1; ++j) f.b[k][j] = ltr2(wa + k * 10240 + j * 2048, wa + k * 10240 + j * 2048 + 512);
    }
}
template <int J0, int J1> DI void frag_mma(const Frag& f, f32x16 (&acc)[2][3]) {
#pragma unroll
    for (int k = 0; k < 2; ++k)
#pragma unroll
        for (int j = J0; j < J1; ++j)
#pragma unroll
            for (int i = 0; i < 2; ++i) acc[i][j] = (j < 2) ? mma32(f.a[k][i], f.b[k][j], acc[i][j]) : mma32(f.b[k][j], f.a[k][i], acc[i][j]);
}
template <int NJ> DI void interleave() {
#pragma unroll
    for (int i = 0; i < 2 + 2 * NJ; ++i) { __builtin_amdgcn_sched_group_barrier(0x8, 1, 0); __builtin_amdgcn_sched_group_barrier(0x100, 2, 0); }
    __builtin_amdgcn_sched_group_barrier(0x8, 4, 0);
}
template <int J0, int J1> DI void tile_step(Frag& f0, Frag& f1, f32x16 (&acc)[2][3], unsigned xa, unsigned wa, int s, bool bar) {
    const unsigned slot = (s & 3) * 40960, nslot = ((s + 1) & 3) * 40960;
    SCHED_FENCE();
    frag_read<J0, J1>(f1, slot + (xa ^ 64), slot + wa + 20480); frag_mma<J0, J1>(f0, acc); interleave<J1 - J0>();
    SCHED_FENCE();
    if (bar) BAR();
    frag_read<J0, J1>(f0, nslot + xa, nslot + wa); frag_mma<J0, J1>(f1, acc); interleave<J1 - J0>();
}
constexpr unsigned CODE_BYTES = 23424;

constexpr unsigned SLOT = 40960;
#define HINT 1
constexpr unsigned A_RED = 65536;
DI void a_loader(const Params& p, int rp, int hg, int lane, int lw STP_PARAM) {
    const int r8 = lane >> 3, ch = lane & 7;
    if (AT && lw < AT) {
        const int c16 = lane & 15;
        asm volatile("s_mov_b64 exec, 0xFFFFFFFF\n\tglobal_load_dword v255, %0, %2\n\ts_mov_b64 exec, %4\n\tglobal_load_dword v255, %1, %3\n\ts_mov_b64 exec, -1"
                     :: "v"((rp * 128 + (hg & 3) * 32 + (lane & 31)) * 1024 + lw * 128),
                        "v"((lw * 64 + (rp & 3) * 16 + c16) * 3072 + (lane >> 4) * 1024 + hg * 128), "s"(p.ax), "s"(p.wqkv), "s"(0xFFFFFFFFFFFFull) : "memory", "v255");
    }
    const rsrc_t rx = mkrsrc(p.ax, 2048 * 512 * 2), rw = mkrsrc(p.wqkv, 512 * 1536 * 2);
    const unsigned vx = (rp * 128 + lw * 32 + r8) * 1024 + ((ch ^ (r8 >> 1)) << 4);
    const unsigned vw = (lw * 16 + r8) * 3072 + hg * 128 + ((ch ^ ((r8 & 2) << 1)) << 4);
    const unsigned vx1 = (vx ^ 64) + 7168, vx2 = vx + 14336, vx3 = (vx ^ 64) + 21504, vk = vw + 1024, vw1 = vw + 24576 - 1024;
#pragma unroll 1
    for (int s = 0; s < 8; ++s) {
        if (s == 0) STAMP(6);
        if (s >= 2) { if (s < 7) VMWAIT(10); else VMWAIT(8); asm volatile("s_barrier" ::: "memory"); }
        if (s == 2) STAMP(11);
        const unsigned so = s * (64 * 3072), l = (s & 3) * SLOT + lw * 10240;
        dma4(rx, vx, vx1, vx2, vx3, s * 128, l);
        dma4(rw, vk, vw + 24576, vw, vw1, so, l + 4096);
        if (s < 6) dma2(rw, vw, vw1, so, l + 8192);
    }
#pragma unroll 1
    for (int s = 6; s < 8; ++s) dma2(rw, vw, vw1, s * (64 * 3072), (s & 3) * SLOT + lw * 10240 + 8192);
}
DI u32x4 sel_matrix(int lane) {
    const int h = lane >> 5, r32 = lane & 31;
    u32x4 sel;
#pragma unroll
    for (int k = 0; k < 4; ++k)
        sel[k] = (r32 < 16 && ((r32 >> 2) & 1) == h && ((r32 >> 3) * 2 + ((r32 >> 1) & 1)) == k) ? ((r32 & 1) ? 0x3F800000u : 0x00003F80u) : 0u;
    return sel;
}
DI void sum_panels(rsrc_t rws, u32x4 (&d)[8], unsigned src, bool on, u32x4 tg, u32x4 sel, unsigned dst) {
    f32x16 sum = 0.f;
    unsigned m = 0;
#pragma unroll
    for (int j = 0; j < 8; ++j) { d[j] ^= tg; sum = mma32(sel, d[j], sum); m = or4(m, d[j]); SCHED_FENCE(); }
    lwf128(dst, q4(sum, 0)); lwf128(dst + 1024, q4(sum, 1));
    if (__builtin_expect(stale(m), 0)) {
#pragma unroll 1
        for (int it = 0; it < SPIN_CAP; ++it) {
            BACKOFF();
            m = 0;
            if (on) {
#pragma unroll
                for (int j = 0; j < 8; ++j) d[j] = ld128s(rws, src, j * 8 * PART_STRIDE) ^ tg;
            }
#pragma unroll
            for (int j = 0; j < 8; ++j) m = or4(m, d[j]);
            if (!stale(m)) break;
        }
        f32x16 s2 = 0.f;
#pragma unroll
        for (int j = 0; j < 8; ++j) s2 = mma32(sel, d[j], s2);
        lwf128(dst, q4(s2, 0)); lwf128(dst + 1024, q4(s2, 1));
    }
}
DI void a_loader_tail(const Params& p, rsrc_t rws, unsigned& ep, int hg, int lane, int lw, int ct STP_PARAM) {
    const int r8 = lane >> 3, ch = lane & 7;
    const rsrc_t ro = mkrsrc(p.wo, 512 * 512 * 2);
    const unsigned vo = (lw * 64 + r8) * 1024 + ct * 128 + ((ch ^ (r8 & 6)) << 4);
    VMWAIT(12);
#pragma unroll 1
    for (int u = 0; u < 2; ++u) {
        BAR();
#pragma unroll 1
        for (int q = 0; q < 8; ++q) dma(ro, vo, u * 262144 + q * 8192, u * 32768 + lw * 8192 + q * 1024);
        if (u == 0) VMWAIT(12);
    }
    VMWAIT(16);
    STAMP(1);
    BAR();
    epoch_get(ep);
    const int rh = lw >> 1, hp = lw & 1;
    unsigned src = WS_PART + hg * PART_STRIDE + rh * 64 * PART_STRIDE + 2048 + hp * 256 + ((lane >> 4) ^ 1) * 64 + (lane & 3) * 16;
    unsigned dst = A_RED + hp * 4096 + rh * 8192 + 2048 + lane * 16;
    u32x4 tg = tag_bits(ep, lane & 3), sel = sel_matrix(lane), d[8];
#pragma unroll
    for (int j = 0; j < 8; ++j) { d[j] = tg; PIN(d[j]); }
    PIN(src); PIN(dst); PIN(tg); PIN(sel);
    VMWAIT(0);
    BAR();
    __builtin_amdgcn_s_sleep(1);
    if ((lane & 15) < 4) {
#pragma unroll
        for (int j = 0; j < 8; ++j) d[j] = ld128s(rws, src, j * 8 * PART_STRIDE);
    }
    sum_panels(rws, d, src, (lane & 15) < 4, tg, sel, dst);
    BAR();
}
DI void a_tile(rsrc_t rws, unsigned ep, int rp, int hg, int lane, int w STP_PARAM) {
    const int g = lane >> 4, c16 = lane & 15, h = lane >> 5, r32 = lane & 31, rh = w >> 1, hp = w & 1;
    const unsigned xa = rh * 20480 + r32 * 128 + ((h ^ ((r32 >> 1) & 7)) << 4);
    const unsigned wa = 4096 + (8 * h + (c16 >> 2)) * 128 + ((hp ^ (c16 >> 3)) << 6) + (g & 1) * 32 + 8 * (lane & 3);
    f32x16 acc[2][3];
#pragma unroll
    for (int i = 0; i < 2; ++i)
#pragma unroll
        for (int j = 0; j < 3; ++j) { acc[i][j] = j == 1 ? 0.f : PHI_EPS; PIN(acc[i][j]); }
    const unsigned pg = WS_PART + hg * PART_STRIDE + hp * 1024;
    unsigned red = A_RED + hp * 4096 + lane * 16;
    unsigned pst = pg + rp * 8 * PART_STRIDE + (rh ? 2048 - hp * 768 + g * 64 + c16 * 16 : lane * 16);
    if (rh && c16 >= 4) pst = WS_END;
    unsigned pk0 = pg + rh * 64 * PART_STRIDE + lane * 16;
    unsigned ob = WS_O + ((rp * 8 + rh * 4 + (g & 1)) * 16 + hg * 2 + hp) * 1024 + (h * 16 + c16) * 16;
    u32x4 tg = tag_bits(ep, lane & 3), sel = sel_matrix(lane);
    PIN(red); PIN(pst); PIN(pk0); PIN(ob); PIN(tg); PIN(sel);
    Frag f0, f1;
    BAR();
    frag_read<0, 3>(f0, xa, wa);
#pragma unroll 1
    for (int s = 0; s < 6; ++s) tile_step<0, 3>(f0, f1, acc, xa, wa, s, true);
    tile_step<0, 2>(f0, f1, acc, xa, wa, 6, true);
    SCHED_FENCE();
    frag_read<0, 2>(f1, 3 * SLOT + (xa ^ 64), 3 * SLOT + wa + 20480); frag_mma<0, 1>(f0, acc); interleave<1>();
    SCHED_FENCE();
    frag_mma<0, 1>(f1, acc);
    SCHED_FENCE();
    STAMP(1);
    {
        frag_mma<1, 2>(f0, acc); frag_mma<1, 2>(f1, acc);
        u32x4 A = cat(phi4(q4(acc[0][0], 0)), phi4(q4(acc[0][0], 1)));
#pragma unroll
        for (int i = 0; i < 8; ++i) { __builtin_amdgcn_sched_group_barrier(0x8, 1, 0); __builtin_amdgcn_sched_group_barrier(0x402, 5, 0); }
        SCHED_FENCE();
        f32x16 kv = 0.f, zz = 0.f;
        const u32x4 ones = {0x3F803F80u, 0x3F803F80u, 0x3F803F80u, 0x3F803F80u};
#pragma unroll
        for (int n = 0; n < 4; ++n) {
            zz = mma32(A, ones, zz);
            kv = mma32(A, cat(pkv(q4(acc[n >> 1][1], 2 * (n & 1))), pkv(q4(acc[n >> 1][1], 2 * (n & 1) + 1))), kv);
            if (n < 3) A = cat(phi4(q4(acc[(n + 1) >> 1][0], 2 * ((n + 1) & 1))), phi4(q4(acc[(n + 1) >> 1][0], 2 * ((n + 1) & 1) + 1)));
            SCHED_FENCE();
        }
        f32x4 k0 = (g & 1) ? q4(kv, 2) : q4(kv, 0), k1 = (g & 1) ? q4(kv, 3) : q4(kv, 1);
        f32x4 z0 = (g & 1) ? q4(zz, 2) : q4(zz, 0), z1 = (g & 1) ? q4(zz, 3) : q4(zz, 1);
        if (rh) { lwf128(red, k0); lwf128(red + 1024, k1); k0 = z0; k1 = z1; } else { lwf128(red + 2048, z0); lwf128(red + 3072, z1); }
        BAR();
        st128(rws, pst, catt(k0 + lrf128(red + rh * 2048), k1 + lrf128(red + rh * 2048 + 1024), tg));
    }
    STAMP(2);
    SCHED_FENCE();
    frag_read<2, 3>(f0, 2 * SLOT + xa, 2 * SLOT + wa);
#pragma unroll 1
    for (int s = 6; s < 8; ++s) tile_step<2, 3>(f0, f1, acc, xa, wa, s, false);
    SCHED_FENCE();
    CWAIT();
    BAR();
    STAMP(3);
    u32x4 dk[8];
#pragma unroll
    for (int j = 0; j < 8; ++j) dk[j] = ld128s(rws, pk0, j * 8 * PART_STRIDE);
    SCHED_FENCE();
    u32x4 pq[2][2];
#pragma unroll
    for (int i = 0; i < 2; ++i)
#pragma unroll
        for (int b = 0; b < 2; ++b) { pq[i][b] = cat(phi4(q4(acc[i][2], 2 * b)), phi4(q4(acc[i][2], 2 * b + 1))); PIN(pq[i][b]); }
    SCHED_FENCE();
    sum_panels(rws, dk, pk0, true, tg, sel, red + 8192 * rh);
    BAR();
    if (HINT == 1 && w == 0 && lane == 0) st32(rws, WS_HINT + (rp * 8 + hg) * 128, ep);
    STAMP(4);
    const u32x4 KV = cat(pkv(lrf128(red) + lrf128(red + 8192)), pkv(lrf128(red + 1024) + lrf128(red + 9216)));
    u32x4 ZZ = cat(pkv(lrf128(red + 2048) + lrf128(red + 10240)), pkv(lrf128(red + 3072) + lrf128(red + 11264)));
#pragma unroll
    for (int k = 0; k < 4; ++k) ZZ[k] = __builtin_amdgcn_update_dpp(ZZ[k], ZZ[k], 0x114, 0xF, 0x2, false);
#pragma unroll
    for (int b = 0; b < 2; ++b) {
        const u32x4 A = ((g & 1) == b) ? KV : ZZ;
#pragma unroll
        for (int i = 0; i < 2; ++i) {
            const f32x16 nd = mma32(A, pq[i][b], 0.f);
            const float ri = TAG_GAIN * __builtin_amdgcn_rcpf(nd[b ? 0 : 8] + 1e-6f);
            st128(rws, ob + i * 32768 + b * 512, catt(q4(nd, 2 * b) * ri, q4(nd, 2 * b + 1) * ri, tg));
        }
    }
    if (HINT == 2 && w == 0 && lane == 0) st32(rws, WS_HINT + (rp * 8 + hg) * 128, ep);
    STAMP(5);
}

constexpr unsigned L_U = 98304;
constexpr unsigned L_S = 65536;
constexpr unsigned L_W = L_U + 19200;
constexpr unsigned L_SP = L_W + 20480;
constexpr unsigned L_C = L_SP + 512;
constexpr unsigned L_PW = L_C + 14336;
constexpr unsigned L_R = 16384;
struct BIn { const char* pf; size_t pm; unsigned po; };
DI void touch(const BIn& in, unsigned add) {
    asm volatile("s_mov_b64 exec, %2\n\tglobal_load_dword v255, %0, %1\n\ts_mov_b64 exec, -1" :: "v"(in.po), "s"(in.pf + add), "s"(in.pm) : "memory", "v255");
}
DI void b_loads(const Params& p, BIn& in, int xcd, int li, int tid, int lane, int w) {
    const int g = lane >> 4, c16 = lane & 15;
    in.pf = (w & 1) ? (const char*)p.ax : (const char*)p.wqkv;
    in.pm = (w & 1) ? 0xFFFFFFFFull : 0x0FFF0FFF0FFF0FFFull;
    in.po = (w & 1) ? ((xcd >> 1) * 512 + li * 32 + (lane & 31)) * 1024 + ((AT ? AT : 1) + (w >> 1)) * 128
                    : (64 * (AT ? AT : 1) + 32 * w + li * 4 + g) * 3072 + (xcd & 1) * 512 + (c16 >> 2) * 1024 + (c16 & 3) * 128;
    if (w < 2 && !TL) touch(in, 0);
}
#define BXP 1
struct BMid { Mat F; Cx tw1; u32x4 y0, y1; unsigned ra[4], oo, bp, u2, tb; };
DI void b_first(const Params& p, rsrc_t rws, unsigned& ep, BIn& in, BMid& mid, int xcd, int li, int tid, int lane, int w, int ct STP_PARAM) {
    const int g = lane >> 4, c16 = lane & 15;
    const int me = xcd * 16 + li;
    const int j1 = (li >> 3) ? (xcd ? 16 - xcd : 8) : xcd, kb = li & 7;
    const int r8 = lane >> 3, ch = lane & 7, jm = (16 - c16) & 15;
    const bool special = (kb == 0 && w == 0);
    auto rows = [&]()
    {
        const unsigned ws = __builtin_amdgcn_readfirstlane(w), vo = me * 8224 + lane * 16;
        asm volatile("s_mov_b32 m0, %0\n\ts_nop 0\n\tbuffer_load_dwordx4 %1, %3, 0 offen nt lds\n\tbuffer_load_dwordx4 %2, %3, 0 offen offset:1024 nt lds"
                     :: "s"(ws * 2048), "v"((256 * w + me) * 1024 + lane * 16), "v"((256 * w + me + 127) * 1024 + lane * 16), "s"(mkrsrc(p.bx, 2048 * 512 * 2)) : "memory");
        const rsrc_t rr = mkrsrc(p.wr, 2048 * 514), ri = mkrsrc(p.wi, 2048 * 514);
        asm volatile("s_mov_b32 m0, %0\n\ts_nop 0\n\tbuffer_load_dwordx4 %3, %4, %7 offen nt lds\n\t"
                     "s_mov_b32 m0, %1\n\ts_nop 0\n\tbuffer_load_dwordx4 %3, %5, %7 offen nt lds\n\t"
                     "s_mov_b32 m0, %2\n\ts_mov_b64 exec, 3\n\tbuffer_load_dwordx4 %3, %6, %8 offen nt lds\n\ts_mov_b64 exec, -1"
                     :: "s"(L_R + ws * 1024), "s"(L_R + 9216 + ws * 1024), "s"(L_R + 8192 + (ws & 1) * 9216), "v"(vo), "s"(rr), "s"(ri),
                        "s"(mkrsrc((ws & 1) ? p.wi : p.wr, 2048 * 514)), "s"(ws * 1024), "s"(8192) : "memory");
    };
    if ((w >> 1) == 1 && !TL) touch(in, 0);
    if (BXP == 1) rows();
    if (w < 7) {
        const int k4 = 4 * w, sU = (0x0040004 >> k4) & 15, sQ = (0x0400040 >> k4) & 15;
        const int P = (((c16 & ((0xF7FF7FF >> k4) & 15)) << sU) + (sU ? j1 : 0)) << ((0x3007833 >> k4) & 15);
        const int n0 = P * (((4 * (g & ((0x3313133 >> k4) & 15))) << sQ) + (sQ ? j1 : 0)), dn = P << sQ;
        const Cx t = angles(n0, dn, 2047, 1.f / 2048);
        Mat m = Mat{__builtin_bit_cast(u32x4, t.re), __builtin_bit_cast(u32x4, t.im)};
        if (w < 4) m = tomat(t);
        if (w == 2 && (g >> 1) != (c16 >> 3)) m = Mat{u32x4{0, 0, 0, 0}, u32x4{0, 0, 0, 0}};
        lw128(L_C + w * 2048 + lane * 16, m.re); lw128(L_C + w * 2048 + 1024 + lane * 16, m.im);
    }
    if (BXP == 2) rows();
    const int km0 = 16 * w + c16, km1 = km0 ? 256 - km0 : 128;
    unsigned ca = L_C + lane * 16, xa = w * 2048 + g * 256 + c16 * 4;
    unsigned ua = L_U + (2 * w) * 1072 + g * 272 + c16 * 16;
    PIN(ca); PIN(xa); PIN(ua);
    if (w >= 4 || TL) touch(in, 0);
    epoch_get(ep);
    BAR();
    if (w < 2 * (4 - (AT ? AT : 1))) touch(in, (w & 1) ? 512 : 256 * 3072);
    Mat F = {lr128(ca + 6144), lr128(ca + 7168)};
    Cx tw1 = {lrf128(ca + 12288), lrf128(ca + 13312)};
    unsigned a0 = L_U + 4 * g * 1072 + ((km0 >> 2) & 3) * 272 + (km0 >> 4) * 16 + (km0 & 3) * 4;
    unsigned a1s = L_U + 4 * g * 1072 + ((km1 >> 2) & 3) * 272 + (km1 >> 4) * 16 + (km1 & 3) * 4;
    unsigned sw0 = L_S + w * 2304 + c16 * 144 + g * 16, sw1 = L_S + w * 2304 + jm * 144 + 64 + g * 16;
    unsigned sr0 = L_S + w * 2304 + r8 * 144 + ch * 16, tb1 = WS_T1 + me * TB + w * 2048 + lane * 16;
    u32x4 tg = tag_bits(ep, g), tl = tag_bits(ep, lane & 3);
    const int cj = (tid >> 3) & 15, cc = 16 * (tid >> 7) + 4 * (ch & 3);
    unsigned ra = L_R + ((ch & 4) ? ((16 - cj) & 15) * 514 + (256 - cc) * 2 : cj * 514 + cc * 2), rs = (ch & 4) ? -2 : 2;
    const int lam = tid >> 3, s2a = (lam >> 5) * 32 + (lam & 3) * 8 + ((lam >> 2) & 7);
    unsigned tb2 = WS_T1 + s2a * TB + (kb * 16 + j1) * 128 + ch * 16;
    const int t16 = (w >> 2) * 32 + (lane & 8) * 8 + (lane & 3) * 8 + (w & 3) * 2 + ((lane >> 2) & 1);
    unsigned tb4 = WS_T1 + t16 * TB + 16384 + j1 * 16;
    PIN(tb2); PIN(tb4); PIN(a0); PIN(a1s); PIN(sw0); PIN(sw1); PIN(sr0); PIN(tb1); PIN(tg); PIN(tl); PIN(ra); PIN(rs);
    { const int n = (w >= 4 || TL) + (w < 2 * (4 - (AT ? AT : 1))); if (n == 2) VMWAIT(2); else if (n == 1) VMWAIT(1); else VMWAIT(0); }
    BAR();
    STAMP(1);
    auto pair = [](unsigned a) { return (unsigned)*LP(const unsigned short, a) | (unsigned)*LP(const unsigned short, a + 9216) << 16; };
    {
        u32x4 v0, v1;
#pragma unroll
        for (int k = 0; k < 4; ++k) { v0[k] = (pair(ra + k * rs) & ~TM) | tl[k]; v1[k] = (pair(ra + (64 + k) * rs) & ~TM) | tl[k]; }
        st128(rws, WS_T1 + (128 + me) * TB + tid * 16, v0); st128(rws, WS_T1 + (128 + me) * TB + 8192 + tid * 16, v1);
        if (tid >= 496)
            st128(rws, WS_T1 + (128 + me) * TB + 16384 + c16 * 16,
                  u32x4{pair(L_R + jm * 514) & ~TM, pair(L_R + c16 * 514 + 512) & ~TM, pair(L_R + c16 * 514 + 256) & ~TM, ep});
    }
    const u32x4 x0 = {lr32(xa), lr32(xa + 64), lr32(xa + 128), lr32(xa + 192)}, x1 = {lr32(xa + 1024), lr32(xa + 1088), lr32(xa + 1152), lr32(xa + 1216)};
    lw128(ua, pk4(dftA(twpk(dftA(x0, F), tw1), F))); lw128(ua + 1072, pk4(dftA(twpk(dftA(x1, F), tw1), F)));
    BAR();
    {
        const u32x4 d0 = {lr32(a0), lr32(a0 + 1072), lr32(a0 + 2144), lr32(a0 + 3216)};
        const u32x4 d1 = {lr32(a1s), lr32(a1s + 1072), lr32(a1s + 2144), lr32(a1s + 3216)};
        const Cx c0 = dftA(d0, F), c1 = dftA(d1, F);
        lw128(sw0, zipt(c0.re, c0.im, tg)); lw128(sw1, zipt(c1.re, -c1.im, tg));
        const u32x4 s0 = lr128(sr0), s1 = lr128(sr0 + 8 * 144);
        st128(rws, tb1, s0); st128(rws, tb1 + 1024, s1);
        if (tid < 16) st128(rws, WS_T1 + me * TB + 16384 + jm * 16, u32x4{pk(c0.re[0], c0.im[0]) & ~TM, 0, 0, ep});
    }
    STAMP(2);
    asm volatile("s_mov_b64 exec, 0xFFFF\n\tglobal_load_dword v255, %0, %1\n\ts_mov_b64 exec, -1" :: "v"(((li & 3) * 128 + w * 16 + c16) * 1024 + ct * 128), "s"(p.wo) : "memory", "v255");
    u32x4 v0 = ld128s(rws, tb2, 128 * TB), v1 = ld128s(rws, tb2, 192 * TB), isp = {0, 0, 0, ep};
    if (kb == 0) isp = ld128s(rws, tb4, 128 * TB);
    FENCE();
    const rsrc_t ro = mkrsrc(p.wo, 512 * 512 * 2);
    const int pm = j1 ? 127 - s2a : (128 - s2a) & 127;
    unsigned wst = L_W + (ch & 3) * 16 + ((ch & 4) ? 10240 + pm * 80 : s2a * 80), wsd = L_W + (ch & 3) * 16 + ((ch & 4) ? 10240 + (pm ^ 64) * 80 : (s2a + 64) * 80);
    unsigned sb = L_U + ((ch & 3) * 2 + (ch >> 2)) * 1040 + ((lam >> 5) * 16 + ((lam >> 2) & 7)) * 16 + (lam & 3) * 4;
    unsigned vo = (w * 64 + r8) * 1024 + ct * 128 + ((ch ^ (r8 & 6)) << 4);
    unsigned vo1 = vo + 7168, vo2 = vo + 14336, vo3 = vo + 21504;
    PIN(wst); PIN(wsd); PIN(sb); PIN(vo); PIN(vo1); PIN(vo2); PIN(vo3);
    Mat F1 = {lr128(ca), lr128(ca + 1024)}, F2 = {lr128(ca + 2048), lr128(ca + 3072)}, G = {lr128(ca + 4096), lr128(ca + 5120)};
    Cx twa = {lrf128(ca + 8192), lrf128(ca + 9216)}, twb = {lrf128(ca + 10240), lrf128(ca + 11264)};
    unsigned ta = L_U + ((w >> 1) * 2 + 8 * (w & 1)) * 1040 + lane * 16;
    const u32x4 tw = tag_bits(ep, w >> 1, 1);
    unsigned t2 = (w & 1) ? ((lane & 8) ? tw[3] : tw[2]) : ((lane & 8) ? tw[1] : tw[0]);
    const int col = 2 * w + (g >> 1);
    const float th = (float)(16 * kb + col) * (1.f / 512);
    float sn = __builtin_amdgcn_sinf(th), cs = __builtin_amdgcn_cosf(th);
    unsigned wp = L_W + (c16 + 64 * (g & 1)) * 80 + col * 4;
    PIN(F1.re); PIN(F1.im); PIN(F2.re); PIN(F2.im); PIN(G.re); PIN(G.im); PIN(twa.re); PIN(twa.im); PIN(twb.re); PIN(twb.im);
    PIN(ta); PIN(t2); PIN(sn); PIN(cs); PIN(wp);
#pragma unroll
    for (int i = 0; i < 4; ++i) {
        const int km = 64 * g + 16 * i + c16, kq = km < 128 ? km : (256 - km) & 127;
        mid.ra[i] = L_U + ((km >> 7) * 16 + 2 * w) * 592 + (kq + 16 * (kq >> 6)) * 4;
        PIN(mid.ra[i]);
    }
    const int set = g & 1, k0 = 16 * (2 * w + (g >> 1));
    mid.oo = (256 * w + me) * 2048 + 1024 + lane * 16; mid.bp = ((lane & 3) * 16 + (lane >> 2)) * 4;
    mid.u2 = L_U + (set * 16 + (set ? jm : c16)) * 592 + (k0 + 16 * (k0 >> 6)) * 4;
    mid.tb = WS_T1 + me * TB + ((2 * w + (c16 >> 3)) * 16 + 4 * g) * 128 + ch * 16;
    PIN(mid.oo); PIN(mid.u2); PIN(mid.bp); PIN(mid.tb);
    {
        v0 ^= tl; v1 ^= tl;
#pragma unroll 1
        for (int it = 0; it < SPIN_CAP && (stale(or4(or4(0, v0), v1)) || __any(isp[3] != ep)); ++it) {
            BACKOFF();
            v0 = ld128s(rws, tb2, 128 * TB) ^ tl; v1 = ld128s(rws, tb2, 192 * TB) ^ tl;
            if (kb == 0) isp = ld128s(rws, tb4, 128 * TB);
        }
        lw128(wst, v0); lw128(wsd, v1);
        if (kb == 0) { lw32(L_PW + (j1 ? 127 - t16 : (128 - t16) & 127) * 16, isp[0]); lw64(L_PW + t16 * 16 + 8, u32x2{isp[1], isp[2]}); }
    }
    BAR();
    STAMP(3);
    u32x4 i0 = ld128(rws, tb2), i1 = ld128s(rws, tb2, 64 * TB);
    if (kb == 0) isp = ld128(rws, tb4);
    FENCE();
    Cx a1, bz, a2, bv;
    {
        f32x4 pr, pi, qr, qi;
#pragma unroll
        for (int r = 0; r < 4; ++r) {
            const unsigned pv = lr32(wp + r * 1280), qv = lr32(wp + r * 1280 + 10240);
            pr[r] = __builtin_bit_cast(float, pv << 16); pi[r] = __builtin_bit_cast(float, pv & 0xFFFF0000u);
            qr[r] = __builtin_bit_cast(float, qv << 16); qi[r] = __builtin_bit_cast(float, qv & 0xFFFF0000u);
        }
        const float sc = TAG_GAIN * TAG_GAIN * TAG_GAIN / 1048576;
        f32x4 rr, ri;
        if (special && g < 2) {
            f32x4 ar, ai, br, bi;
#pragma unroll
            for (int r = 0; r < 4; ++r) {
                const u32x4 e = lr128(L_PW + (c16 + 64 * g + 16 * r) * 16);
                ar[r] = bf(e[0]); ai[r] = hi16(e[0]); br[r] = bf(e[2]); bi[r] = hi16(e[2]); rr[r] = bf(e[3]); ri[r] = hi16(e[3]);
            }
            pr = 0.5f * (pr + ar); pi = 0.5f * (pi - ai); qr = 0.5f * (qr + br); qi = 0.5f * (qi - bi);
        }
        const f32x4 sr = (pr + qr) * sc, si = (pi - qi) * sc, dr = (pr - qr) * sc, di = (pi + qi) * sc;
        a1.re = sr - dr * sn; a1.im = si - di * sn; a2.re = sr + dr * sn; a2.im = si + di * sn;
        bz.re = dr * cs; bz.im = di * cs; bv = bz;
        if (special && g < 2) { a2.re = rr * (2.f * sc); a2.im = ri * (2.f * sc); bv.re = Z4; bv.im = Z4; }
    }
    PIN(a1.re); PIN(a1.im); PIN(a2.re); PIN(a2.im); PIN(bz.re); PIN(bz.im); PIN(bv.re); PIN(bv.im);
    i0 ^= tl; i1 ^= tl;
#pragma unroll 1
    for (int it = 0; it < SPIN_CAP && (stale(or4(or4(0, i0), i1)) || __any(isp[3] != ep)); ++it) {
        BACKOFF();
        i0 = ld128(rws, tb2) ^ tl; i1 = ld128s(rws, tb2, 64 * TB) ^ tl;
        if (kb == 0) isp = ld128(rws, tb4);
    }
    STAMP(10);
    lw32(sb, i0[0]); lw32(sb + 128, i0[1]); lw32(sb + 8320, i0[2]); lw32(sb + 8320 + 128, i0[3]);
    lw32(sb + 512, i1[0]); lw32(sb + 512 + 128, i1[1]); lw32(sb + 512 + 8320, i1[2]); lw32(sb + 512 + 8320 + 128, i1[3]);
    if (kb == 0) lw32(L_SP + ((t16 >> 5) * 8 + (t16 & 7)) * 16 + ((t16 >> 3) & 3) * 4, isp[0]);
    BAR();
    STAMP(4);
    const u32x4 zi = lr128(ta), vi = lr128(ta + 1040);
    dma4(ro, vo, vo1, vo2, vo3, 0, w * 8192);
    u32x4 zf = twpk(dftA(zi, F1), twa), vf = twpk(dftA(vi, F1), twa);
    u32x4 vp = vf;
    if (special) {
        vp = lr128(L_SP + (g * 8 + (c16 & 7)) * 16) ^ 0x80000000u;
        if (c16 >= 8) vp = vi;
        vp = twpk(dftA(vp, F1), twa);
    }
    PIN(zf); PIN(vf);
    dma4(ro, vo, vo1, vo2, vo3, 32768, w * 8192 + 4096);
    {
        const Cx Z = dftB(zf, G), V = dftB(vf, G);
        Cx Vz = V;
        if (special) Vz = dftB(vp, G);
        u32x4 zc = zip(a1.re * Z.re - a1.im * Z.im - bz.re * Vz.im - bz.im * Vz.re, bz.im * Vz.im - a1.re * Z.im - a1.im * Z.re - bz.re * Vz.re);
        u32x4 vc = zip(a2.re * V.re - a2.im * V.im + bv.re * Z.im + bv.im * Z.re, bv.re * Z.re - a2.re * V.im - a2.im * V.re - bv.im * Z.im);
        const Cx zo = dftB(twpk(dftA(zc, G), twb), F2), vb = dftB(twpk(dftA(vc, G), twb), F2);
        lw128(ta, zipt(zo.re, zo.im, u32x4(t2))); lw128(ta + 1040, zipt(vb.re, -vb.im, u32x4(t2)));
        VMWAIT(0);
        BAR();
        const u32x4 o0 = {lr32(sb), lr32(sb + 128), lr32(sb + 8320), lr32(sb + 8320 + 128)};
        const u32x4 o1 = {lr32(sb + 512), lr32(sb + 512 + 128), lr32(sb + 512 + 8320), lr32(sb + 512 + 8320 + 128)};
        st128(rws, tb2, o0); __builtin_amdgcn_raw_buffer_store_b128(o1, rws, tb2, 64 * TB, 16);
    }
    STAMP(5);
    mid.F = F; mid.tw1 = tw1;
    if (w >= 4) { EWAIT(); BAR(); }
}
DI void b_second(const Params& p, rsrc_t rws, unsigned ep, BMid& mid, unsigned& hint, unsigned ha, int xcd, int li, int lane, int w STP_PARAM) {
    const int g = lane >> 4, c16 = lane & 15, ch = lane & 7, jm = (16 - c16) & 15, me = xcd * 16 + li;
    const Mat& F = mid.F; const Cx& tw1 = mid.tw1;
    u32x4 tl = tag_bits(ep, lane & 3, 1);
    const unsigned (&ra)[4] = mid.ra; const unsigned bp = mid.bp, u2 = mid.u2, tb = mid.tb;
    PIN(tl);
    if (w < 4) {
        EWAIT();
        BAR();
        STAMP(6);
        u32x4 l0 = ld128(rws, tb) ^ tl, l1 = ld128(rws, tb + 128) ^ tl, l2 = ld128(rws, tb + 256) ^ tl, l3 = ld128(rws, tb + 384) ^ tl;
#pragma unroll 1
        for (int it = 0; it < SPIN_CAP && stale(or4(or4(or4(or4(0, l0), l1), l2), l3)); ++it) {
            BACKOFF();
            l0 = ld128(rws, tb) ^ tl; l1 = ld128(rws, tb + 128) ^ tl; l2 = ld128(rws, tb + 256) ^ tl; l3 = ld128(rws, tb + 384) ^ tl;
        }
        u32x4 t[4];
#pragma unroll
        for (int e = 0; e < 4; ++e) t[e] = pk4(dftA(u32x4{l0[e], l1[e], l2[e], l3[e]}, F));
#pragma unroll
        for (int r = 0; r < 4; ++r) lw128(u2 + r * 16, u32x4{t[0][r], t[1][r], t[2][r], t[3][r]});
    }
    BAR();
    STAMP(11);
    if (w == 7) hint = ld32(rws, ha);
    FENCE();
    {
        const u32x4 d0 = {lr32(ra[0]), lr32(ra[1]), lr32(ra[2]), lr32(ra[3])};
        const u32x4 d1 = {lr32(ra[0] + 592), lr32(ra[1] + 592), lr32(ra[2] + 592), lr32(ra[3] + 592)};
        u32x4 y0 = pk4c(dftA(twpk(dftA(d0, F), tw1), F)), y1 = pk4c(dftA(twpk(dftA(d1, F), tw1), F));
#pragma unroll
        for (int r = 0; r < 4; ++r) {
            y0[r] = __builtin_amdgcn_ds_bpermute(bp, y0[r]); y1[r] = __builtin_amdgcn_ds_bpermute(bp, y1[r]);
        }
        PIN(y0); PIN(y1);
        mid.y0 = y0; mid.y1 = y1;
        STAMP(13);
    }
}

__global__ void __launch_bounds__(512, 2) fused_kernel(Params p) {
    const int b = blockIdx.x ^ (SWAP * 128), xcd = b & 7, i = b >> 3, li = i & 15;
    const int tid = threadIdx.x, lane = tid & 63, w = __builtin_amdgcn_readfirstlane(tid >> 6);
    BIn in;
    {
        size_t code;
        asm volatile("s_getpc_b64 %0" : "=s"(code));
        asm volatile("s_mov_b64 exec, 1\n\tglobal_load_dword v255, %0, %1\n\ts_mov_b64 exec, -1" :: "v"(min((unsigned)(w * 32 + i) * 128u, CODE_BYTES - 128u)), "s"(code & ~(size_t)255) : "memory", "v255");
    }
    __shared__ __attribute__((aligned(16))) char smem[LDS_BYTES];
    asm volatile("" :: "s"((unsigned)(size_t)(LAS char*)smem));
    STAMP(0);
    const int rp = (xcd >> 1) * 4 + (li & 3), hg = (xcd & 1) * 4 + (li >> 2), rt = xcd * 4 + (i & 3), ct = i >> 2;
    if (i < 16 && w >= 4) a_loader(p, rp, hg, lane, w - 4 STP_ARG);
    FENCE();
    if (i >= 16) b_loads(p, in, xcd, li, tid, lane, w);
    FENCE();
    const rsrc_t rws = mkrsrc(p.ws, WS_END);
    unsigned ep;
    epoch_ask(ep, p.ws);
    BMid mid;
    if (i >= 16) b_first(p, rws, ep, in, mid, xcd, li, tid, lane, w, ct STP_ARG);
    else if (w < 4) { epoch_get(ep); a_tile(rws, ep, rp, hg, lane, w STP_ARG); }
    else a_loader_tail(p, rws, ep, hg, lane, w - 4, ct STP_ARG);
    const int g = lane >> 4, c16 = lane & 15, q = c16 >> 2, kh = w >> 2;
    unsigned ob = WS_O + (rt * 4 + (w & 3)) * 16384 + kh * 8192 + lane * 16;
    unsigned wa = kh * 32768 + (16 * (g >> 1) + 4 * (g & 1) + q) * 128 + 8 * (lane & 3) + (((2 * g + (q >> 1)) & 3) << 5);
    const unsigned scr = 81920;
    unsigned rw = scr + kh * 17408 + ((w & 3) * 16 + c16) * 272 + g * 16, rr = scr + (tid >> 3) * 272 + (tid & 7) * 32;
    unsigned oo = (rt * 64 + (tid >> 3)) * 2048 + ct * 128 + (tid & 7) * 16;
    u32x4 tl = tag_bits(ep, lane & 3);
    u32x4 o[8], wf[8][4];
#pragma unroll
    for (int k = 0; k < 8; ++k)
#pragma unroll
        for (int t = 0; t < 4; ++t) { const unsigned a = (wa ^ (t << 5)) + k * 4096; wf[k][t] = ltr2(a, a + 1024); }
    PIN(ob); PIN(rw); PIN(rr); PIN(oo); PIN(tl);
    SCHED_FENCE();
    const unsigned ha = WS_HINT + ((rt >> 1) * 8 + (lane & 7)) * 128;
    unsigned hint = ep + 1;
    if (i >= 16) b_second(p, rws, ep, mid, hint, ha, xcd, li, lane, w STP_ARG);
    if (w == 7) {
#pragma unroll 1
        for (int it = 0; it < SPIN_CAP && !__all(hint == ep); ++it) { if (it) __builtin_amdgcn_s_sleep(2); FENCE(); hint = ld32(rws, ha); }
    }
    if (i < 16) DWAIT();
    BAR();
    STAMP(7);
#pragma unroll
    for (int k = 0; k < 8; ++k) o[k] = ld128s(rws, ob, k * 1024);
    const rsrc_t rout = mkrsrc(p.out, 2048 * 1024 * 2);
    f32x4 acc[4] = {Z4, Z4, Z4, Z4};
    unsigned m = 0;
#pragma unroll
    for (int k = 0; k < 8; ++k) {
        o[k] ^= tl;
#pragma unroll
        for (int t = 0; t < 4; ++t) acc[t] = mma(wf[k][t], o[k], acc[t]);
        m = or4(m, o[k]);
        SCHED_FENCE();
    }
    if (__builtin_expect(stale(m), 0)) {
#pragma unroll 1
        for (int it = 0; it < SPIN_CAP; ++it) {
            BACKOFF();
            m = 0;
#pragma unroll
            for (int k = 0; k < 8; ++k) o[k] = ld128s(rws, ob, k * 1024) ^ tl;
#pragma unroll
            for (int k = 0; k < 8; ++k) m = or4(m, o[k]);
            if (!stale(m)) break;
        }
#pragma unroll
        for (int t = 0; t < 4; ++t) acc[t] = Z4;
#pragma unroll
        for (int k = 0; k < 8; ++k)
#pragma unroll
            for (int t = 0; t < 4; ++t) acc[t] = mma(wf[k][t], o[k], acc[t]);
    }
    STAMP(8);
#pragma unroll
    for (int t = 0; t < 4; ++t) lwf128(rw + t * 64, acc[t]);
    if (i >= 16) {
        __builtin_amdgcn_raw_buffer_store_b128(mid.y0, rout, mid.oo, 0, OUTB_AUX);
        __builtin_amdgcn_raw_buffer_store_b128(mid.y1, rout, mid.oo, 128 * 2048, OUTB_AUX);
    }
    BAR();
    const f32x4 y0 = lrf128(rr) + lrf128(rr + 17408), y1 = lrf128(rr + 16) + lrf128(rr + 17408 + 16);
    __builtin_amdgcn_raw_buffer_store_b128(cat(pkv(y0), pkv(y1)), rout, oo, 0, OUT_AUX);
    if (b == 128 && tid == 256) st32(rws, WS_EPOCH, ep);
    STAMP(9);
}

extern "C" void kernel_launch(void* const* d_in, const int* in_sizes, int n_in,
                              void* d_out, int out_size, void* d_ws, size_t ws_size,
                              hipStream_t stream) {
    Params p;
    p.ax = (const unsigned short*)d_in[0]; p.wqkv = (const unsigned short*)d_in[1]; p.wo = (const unsigned short*)d_in[2];
    p.bx = (const unsigned short*)d_in[3]; p.wr = (const unsigned short*)d_in[4]; p.wi = (const unsigned short*)d_in[5];
    p.out = (unsigned short*)d_out; p.ws = (unsigned char*)d_ws;
    fused_kernel<<<256, 512, 0, stream>>>(p);
}
```

```cpp
#include <hip/hip_runtime.h>
#include <stdint.h>

typedef short    s16x4 __attribute__((ext_vector_type(4)));
typedef __bf16   bf16x8 __attribute__((ext_vector_type(8)));
typedef __bf16   bf16x2 __attribute__((ext_vector_type(2)));
typedef float    f32x2 __attribute__((ext_vector_type(2)));
typedef float    f32x4 __attribute__((ext_vector_type(4)));
typedef float    f32x16 __attribute__((ext_vector_type(16)));
typedef unsigned u32x2 __attribute__((ext_vector_type(2)));
typedef unsigned u32x4 __attribute__((ext_vector_type(4)));
typedef __amdgpu_buffer_rsrc_t rsrc_t;
#define LAS __attribute__((address_space(3)))
#define DI __device__ __forceinline__

constexpr unsigned WS_EPOCH = 0;
constexpr unsigned WS_HINT = 4096;
constexpr unsigned PART_STRIDE = 2688, WS_PART = 32768;
constexpr unsigned TB = 17024;
constexpr unsigned WS_T1 = 1u << 19;
constexpr unsigned WS_O = WS_T1 + 128 * TB, WS_END = WS_O + (1u << 21);
constexpr int SPIN_CAP = 1 << 15;
#define RSLEEP 2
#define BACKOFF() do { __builtin_amdgcn_s_sleep(RSLEEP); FENCE(); } while (0)
constexpr unsigned LDS_BYTES = 163840;
#define OUTB_AUX 16
#define OUT_AUX 16
#define STAMP(i) do {} while (0)
#define STP_PARAM
#define STP_ARG

struct Params {
    const unsigned short *ax, *wqkv, *wo, *bx, *wr, *wi;
    unsigned short* out;
    unsigned char* ws;
};

DI rsrc_t mkrsrc(const void* ptr, unsigned bytes) { return __builtin_amdgcn_make_buffer_rsrc((void*)ptr, 0, bytes, 0x00020000); }
DI u32x4 ld128(rsrc_t r, unsigned off)  { return __builtin_amdgcn_raw_buffer_load_b128(r, off, 0, 16); }
DI u32x2 ld64(rsrc_t r, unsigned off)   { return __builtin_amdgcn_raw_buffer_load_b64(r, off, 0, 16); }
DI unsigned ld32(rsrc_t r, unsigned off){ return __builtin_amdgcn_raw_buffer_load_b32(r, off, 0, 16); }
DI u32x2 ld64s(rsrc_t r, unsigned off, unsigned so)   { return __builtin_amdgcn_raw_buffer_load_b64(r, off, so, 16); }
DI u32x4 ld128s(rsrc_t r, unsigned off, unsigned so)  { return __builtin_amdgcn_raw_buffer_load_b128(r, off, so, 16); }
DI unsigned ld32s(rsrc_t r, unsigned off, unsigned so){ return __builtin_amdgcn_raw_buffer_load_b32(r, off, so, 16); }
DI void st128(rsrc_t r, unsigned off, u32x4 v) { __builtin_amdgcn_raw_buffer_store_b128(v, r, off, 0, 16); }
DI void st64(rsrc_t r, unsigned off, u32x2 v) { __builtin_amdgcn_raw_buffer_store_b64(v, r, off, 0, 16); }
DI void st32(rsrc_t r, unsigned off, unsigned v) { __builtin_amdgcn_raw_buffer_store_b32(v, r, off, 0, 16); }
#define FENCE() asm volatile("" ::: "memory")
#define BAR() asm volatile("s_waitcnt lgkmcnt(0)\n\ts_barrier" ::: "memory")
#define VMWAIT(n) asm volatile("s_waitcnt vmcnt(" #n ")" ::: "memory")
#define EWAIT() VMWAIT(0)
#define CWAIT() VMWAIT(0)
#define DWAIT() VMWAIT(0)
#define SWAP 0
#define PIN(x) asm volatile("" : "+v"(x))
#define SCHED_FENCE() __builtin_amdgcn_sched_barrier(0)
DI void dma(rsrc_t r, unsigned voff, unsigned soff, unsigned lds) {
    asm volatile("s_mov_b32 m0, %0\n\ts_nop 0\n\tbuffer_load_dwordx4 %1, %2, %3 offen lds"
                 :: "s"(lds), "v"(voff), "s"(r), "s"(soff) : "memory");
}
DI void dma4(rsrc_t r, unsigned v0, unsigned v1, unsigned v2, unsigned v3, unsigned soff, unsigned lds) {
    asm volatile("s_mov_b32 m0, %0\n\ts_nop 0\n\tbuffer_load_dwordx4 %1, %5, %6 offen lds\n\tbuffer_load_dwordx4 %2, %5, %6 offen offset:1024 lds\n\t"
                 "buffer_load_dwordx4 %3, %5, %6 offen offset:2048 lds\n\tbuffer_load_dwordx4 %4, %5, %6 offen offset:3072 lds"
                 :: "s"(lds), "v"(v0), "v"(v1), "v"(v2), "v"(v3), "s"(r), "s"(soff) : "memory");
}
DI void dma2(rsrc_t r, unsigned v0, unsigned v1, unsigned soff, unsigned lds) {
    asm volatile("s_mov_b32 m0, %0\n\ts_nop 0\n\tbuffer_load_dwordx4 %1, %3, %4 offen lds\n\tbuffer_load_dwordx4 %2, %3, %4 offen offset:1024 lds"
                 :: "s"(lds), "v"(v0), "v"(v1), "s"(r), "s"(soff) : "memory");
}
DI unsigned pk(float lo, float hi) { f32x2 v = {lo, hi}; return __builtin_bit_cast(unsigned, __builtin_convertvector(v, bf16x2)); }
DI u32x2 pkv(f32x4 v) { return u32x2{pk(v[0], v[1]), pk(v[2], v[3])}; }
DI u32x4 cat(u32x2 a, u32x2 b) { return u32x4{a[0], a[1], b[0], b[1]}; }
DI f32x4 mma(u32x4 a, u32x4 b, f32x4 c) {
    return __builtin_amdgcn_mfma_f32_16x16x32_bf16(__builtin_bit_cast(bf16x8, a), __builtin_bit_cast(bf16x8, b), c, 0, 0, 0); }
DI f32x16 mma32(u32x4 a, u32x4 b, f32x16 c) {
    return __builtin_amdgcn_mfma_f32_32x32x16_bf16(__builtin_bit_cast(bf16x8, a), __builtin_bit_cast(bf16x8, b), c, 0, 0, 0); }
DI f32x4 mma16(u32x2 a, u32x2 b, f32x4 c) {
    return __builtin_amdgcn_mfma_f32_16x16x16bf16_1k(__builtin_bit_cast(s16x4, a), __builtin_bit_cast(s16x4, b), c, 0, 0, 0); }
#define LP(T, a) ((LAS T*)(size_t)(a))
DI u32x4 lr128(unsigned a) { return *LP(const u32x4, a); }
DI f32x4 lrf128(unsigned a) { return *LP(const f32x4, a); }
DI u32x2 lr64(unsigned a) { return *LP(const u32x2, a); }
DI unsigned lr32(unsigned a) { return *LP(const unsigned, a); }
DI float bf(unsigned short v) { return __builtin_bit_cast(float, (unsigned)v << 16); }
DI float hi16(unsigned v) { return __builtin_bit_cast(float, v & 0xFFFF0000u); }
DI float lrbf(unsigned a) { return bf(*LP(const unsigned short, a)); }
DI void lw128(unsigned a, u32x4 v) { *LP(u32x4, a) = v; }
DI void lwf128(unsigned a, f32x4 v) { *LP(f32x4, a) = v; }
DI void lw64(unsigned a, u32x2 v) { *LP(u32x2, a) = v; }
DI void lw32(unsigned a, unsigned v) { *LP(unsigned, a) = v; }
DI u32x2 ltr(unsigned a) { return __builtin_bit_cast(u32x2, __builtin_amdgcn_ds_read_tr16_b64_v4i16(LP(s16x4, a))); }
DI u32x4 ltr2(unsigned a, unsigned b) { return cat(ltr(a), ltr(b)); }
constexpr unsigned TM = 0x00010001u;
DI u32x4 tag_bits(unsigned ep, int unit, unsigned use = 2) {
    const unsigned t = ((ep << 2) | use) >> (8 * unit);
    u32x4 m;
#pragma unroll
    for (int k = 0; k < 4; ++k) m[k] = ((t >> (2 * k)) & 1u) | (((t >> (2 * k + 1)) & 1u) << 16);
    return m;
}
constexpr float TAG_GAIN = 1.0027f;
DI unsigned pkt1(float a, float b, unsigned bits) { return __builtin_amdgcn_bitop3_b32(pk(a, b), TM, bits, 0xBA); }
DI u32x4 zipt(f32x4 a, f32x4 b, u32x4 t) { return u32x4{pkt1(a[0], b[0], t[0]), pkt1(a[1], b[1], t[1]), pkt1(a[2], b[2], t[2]), pkt1(a[3], b[3], t[3])}; }
DI u32x4 catt(f32x4 a, f32x4 b, u32x4 t) { return u32x4{pkt1(a[0], a[1], t[0]), pkt1(a[2], a[3], t[1]), pkt1(b[0], b[1], t[2]), pkt1(b[2], b[3], t[3])}; }
DI unsigned or4(unsigned m, u32x4 v) { return m | v[0] | v[1] | v[2] | v[3]; }
DI bool stale(unsigned m) { return __any(m & TM); }
constexpr float PHI_EPS = 1e-6f;
DI u32x2 phi4(f32x4 u) {
    f32x4 a = u * 1.44269504f - PHI_EPS * 1.44269504f, e, m;
    PIN(a);
#pragma unroll
    for (int r = 0; r < 4; ++r) {
        e[r] = __builtin_amdgcn_fmed3f(__builtin_amdgcn_exp2f(a[r]), 0.f, 1.f);
        m[r] = __builtin_elementwise_maximum(u[r], PHI_EPS);
    }
    return pkv(m + e);
}
constexpr f32x4 Z4 = {0.f, 0.f, 0.f, 0.f};

struct Mat { u32x4 re, im; };
struct Cx  { f32x4 re, im; };
DI Cx angles(int n0, int dn, int mask, float inv) {
    Cx t;
#pragma unroll
    for (int r = 0; r < 4; ++r) { float a = (float)((n0 + r * dn) & mask) * inv;
        t.re[r] = __builtin_amdgcn_cosf(a); t.im[r] = __builtin_amdgcn_sinf(a); }
    return t;
}
DI u32x4 zip(f32x4 a, f32x4 b) { return u32x4{pk(a[0], b[0]), pk(a[1], b[1]), pk(a[2], b[2]), pk(a[3], b[3])}; }
DI Mat tomat(Cx t) { return Mat{zip(t.re, t.im), zip(-t.im, t.re)}; }
DI Cx dftA(u32x4 d, const Mat& f) { return Cx{mma(d, f.re, Z4), mma(d, f.im, Z4)}; }
DI Cx dftB(u32x4 d, const Mat& f) { return Cx{mma(f.re, d, Z4), mma(f.im, d, Z4)}; }
DI u32x4 twpk(Cx v, const Cx& w) { return zip(v.re * w.re + v.im * w.im, v.im * w.re - v.re * w.im); }
DI u32x4 pk4(Cx v)  { return zip(v.re, v.im); }
DI u32x4 pk4c(Cx v) { return zip(v.re, v.im) ^ 0x80000000u; }

DI void epoch_ask(unsigned& e, const unsigned char* ws) { asm volatile("s_load_dword %0, %1, 0x0" : "=s"(e) : "s"(ws) : "memory"); }
DI void epoch_get(unsigned& e) {
    unsigned r;
    asm volatile("s_waitcnt lgkmcnt(0)\n\ts_add_u32 %0, %1, 1" : "=s"(r) : "s"(e) : "memory", "scc");
    e = r;
}
DI f32x4 q4(const f32x16& v, int i) { return f32x4{v[4 * i], v[4 * i + 1], v[4 * i + 2], v[4 * i + 3]}; }
struct Frag { u32x4 a[2][2], b[2][3]; };
template <int J0, int J1> DI void frag_read(Frag& f, unsigned xa, unsigned wa) {
#pragma unroll
    for (int k = 0; k < 2; ++k) {
        f.a[k][0] = lr128(xa ^ (k * 32)); f.b[k][J0] = ltr2(wa + k * 10240 + J0 * 2048, wa + k * 10240 + J0 * 2048 + 512);
        f.a[k][1] = lr128((xa ^ (k * 32)) + 10240);
#pragma unroll
        for (int j = J0 + 1; j < J1; ++j) f.b[k][j] = ltr2(wa + k * 10240 + j * 2048, wa + k * 10240 + j * 2048 + 512);
    }
}
template <int J0, int J1> DI void frag_mma(const Frag& f, f32x16 (&acc)[2][3]) {
#pragma unroll
    for (int k = 0; k < 2; ++k)
#pragma unroll
        for (int j = J0; j < J1; ++j)
#pragma unroll
            for (int i = 0; i < 2; ++i) acc[i][j] = (j < 2) ? mma32(f.a[k][i], f.b[k][j], acc[i][j]) : mma32(f.b[k][j], f.a[k][i], acc[i][j]);
}
template <int NJ> DI void interleave() {
#pragma unroll
    for (int i = 0; i < 2 + 2 * NJ; ++i) { __builtin_amdgcn_sched_group_barrier(0x8, 1, 0); __builtin_amdgcn_sched_group_barrier(0x100, 2, 0); }
    __builtin_amdgcn_sched_group_barrier(0x8, 4, 0);
}
template <int J0, int J1> DI void tile_step(Frag& f0, Frag& f1, f32x16 (&acc)[2][3], unsigned xa, unsigned wa, int s, bool bar) {
    const unsigned slot = (s & 3) * 40960, nslot = ((s + 1) & 3) * 40960;
    SCHED_FENCE();
    frag_read<J0, J1>(f1, slot + (xa ^ 64), slot + wa + 20480); frag_mma<J0, J1>(f0, acc); interleave<J1 - J0>();
    SCHED_FENCE();
    if (bar) BAR();
    frag_read<J0, J1>(f0, nslot + xa, nslot + wa); frag_mma<J0, J1>(f1, acc); interleave<J1 - J0>();
}
constexpr unsigned CODE_BYTES = 23040;

constexpr unsigned SLOT = 40960;
#define HINT 1
constexpr unsigned A_RED = 65536;
DI void a_loader(const Params& p, int rp, int hg, int lane, int lw STP_PARAM) {
    const int r8 = lane >> 3, ch = lane & 7;
    const rsrc_t rx = mkrsrc(p.ax, 2048 * 512 * 2), rw = mkrsrc(p.wqkv, 512 * 1536 * 2);
    const unsigned vx = (rp * 128 + lw * 32 + r8) * 1024 + ((ch ^ (r8 >> 1)) << 4);
    const unsigned vw = (lw * 16 + r8) * 3072 + hg * 128 + ((ch ^ ((r8 & 2) << 1)) << 4);
    const unsigned vx1 = (vx ^ 64) + 7168, vx2 = vx + 14336, vx3 = (vx ^ 64) + 21504, vk = vw + 1024, vw1 = vw + 24576 - 1024;
#pragma unroll 1
    for (int s = 0; s < 8; ++s) {
        if (s == 0) STAMP(6);
        if (s >= 2) { if (s < 7) VMWAIT(10); else VMWAIT(8); asm volatile("s_barrier" ::: "memory"); }
        if (s == 2) STAMP(11);
        const unsigned so = s * (64 * 3072), l = (s & 3) * SLOT + lw * 10240;
        dma4(rx, vx, vx1, vx2, vx3, s * 128, l);
        dma4(rw, vk, vw + 24576, vw, vw1, so, l + 4096);
        if (s < 6) dma2(rw, vw, vw1, so, l + 8192);
    }
#pragma unroll 1
    for (int s = 6; s < 8; ++s) dma2(rw, vw, vw1, s * (64 * 3072), (s & 3) * SLOT + lw * 10240 + 8192);
}
DI u32x4 sel_matrix(int lane) {
    const int h = lane >> 5, r32 = lane & 31;
    u32x4 sel;
#pragma unroll
    for (int k = 0; k < 4; ++k)
        sel[k] = (r32 < 16 && ((r32 >> 2) & 1) == h && ((r32 >> 3) * 2 + ((r32 >> 1) & 1)) == k) ? ((r32 & 1) ? 0x3F800000u : 0x00003F80u) : 0u;
    return sel;
}
DI void sum_panels(rsrc_t rws, u32x4 (&d)[8], unsigned src, bool on, u32x4 tg, u32x4 sel, unsigned dst) {
    f32x16 sum = 0.f;
    unsigned m = 0;
#pragma unroll
    for (int j = 0; j < 8; ++j) { d[j] ^= tg; sum = mma32(sel, d[j], sum); m = or4(m, d[j]); SCHED_FENCE(); }
    lwf128(dst, q4(sum, 0)); lwf128(dst + 1024, q4(sum, 1));
    if (__builtin_expect(stale(m), 0)) {
#pragma unroll 1
        for (int it = 0; it < SPIN_CAP; ++it) {
            BACKOFF();
            m = 0;
            if (on) {
#pragma unroll
                for (int j = 0; j < 8; ++j) d[j] = ld128s(rws, src, j * 8 * PART_STRIDE) ^ tg;
            }
#pragma unroll
            for (int j = 0; j < 8; ++j) m = or4(m, d[j]);
            if (!stale(m)) break;
        }
        f32x16 s2 = 0.f;
#pragma unroll
        for (int j = 0; j < 8; ++j) s2 = mma32(sel, d[j], s2);
        lwf128(dst, q4(s2, 0)); lwf128(dst + 1024, q4(s2, 1));
    }
}
DI void a_loader_tail(const Params& p, rsrc_t rws, unsigned& ep, int hg, int lane, int lw, int ct STP_PARAM) {
    const int r8 = lane >> 3, ch = lane & 7;
    const rsrc_t ro = mkrsrc(p.wo, 512 * 512 * 2);
    const unsigned vo = (lw * 64 + r8) * 1024 + ct * 128 + ((ch ^ (r8 & 6)) << 4);
    VMWAIT(12);
#pragma unroll 1
    for (int u = 0; u < 2; ++u) {
        BAR();
#pragma unroll 1
        for (int q = 0; q < 8; ++q) dma(ro, vo, u * 262144 + q * 8192, u * 32768 + lw * 8192 + q * 1024);
        if (u == 0) VMWAIT(12);
    }
    VMWAIT(16);
    STAMP(1);
    BAR();
    epoch_get(ep);
    const int rh = lw >> 1, hp = lw & 1;
    unsigned src = WS_PART + hg * PART_STRIDE + rh * 64 * PART_STRIDE + 2048 + hp * 256 + ((lane >> 4) ^ 1) * 64 + (lane & 3) * 16;
    unsigned dst = A_RED + hp * 4096 + rh * 8192 + 2048 + lane * 16;
    u32x4 tg = tag_bits(ep, lane & 3), sel = sel_matrix(lane), d[8];
#pragma unroll
    for (int j = 0; j < 8; ++j) { d[j] = tg; PIN(d[j]); }
    PIN(src); PIN(dst); PIN(tg); PIN(sel);
    VMWAIT(0);
    BAR();
    __builtin_amdgcn_s_sleep(1);
    if ((lane & 15) < 4) {
#pragma unroll
        for (int j = 0; j < 8; ++j) d[j] = ld128s(rws, src, j * 8 * PART_STRIDE);
    }
    sum_panels(rws, d, src, (lane & 15) < 4, tg, sel, dst);
    BAR();
}
DI void a_tile(rsrc_t rws, unsigned ep, int rp, int hg, int lane, int w STP_PARAM) {
    const int g = lane >> 4, c16 = lane & 15, h = lane >> 5, r32 = lane & 31, rh = w >> 1, hp = w & 1;
    const unsigned xa = rh * 20480 + r32 * 128 + ((h ^ ((r32 >> 1) & 7)) << 4);
    const unsigned wa = 4096 + (8 * h + (c16 >> 2)) * 128 + ((hp ^ (c16 >> 3)) << 6) + (g & 1) * 32 + 8 * (lane & 3);
    f32x16 acc[2][3];
#pragma unroll
    for (int i = 0; i < 2; ++i)
#pragma unroll
        for (int j = 0; j < 3; ++j) { acc[i][j] = j == 1 ? 0.f : PHI_EPS; PIN(acc[i][j]); }
    const unsigned pg = WS_PART + hg * PART_STRIDE + hp * 1024;
    unsigned red = A_RED + hp * 4096 + lane * 16;
    unsigned pst = pg + rp * 8 * PART_STRIDE + (rh ? 2048 - hp * 768 + g * 64 + c16 * 16 : lane * 16);
    if (rh && c16 >= 4) pst = WS_END;
    unsigned pk0 = pg + rh * 64 * PART_STRIDE + lane * 16;
    unsigned ob = WS_O + ((rp * 8 + rh * 4 + (g & 1)) * 16 + hg * 2 + hp) * 1024 + (h * 16 + c16) * 16;
    u32x4 tg = tag_bits(ep, lane & 3), sel = sel_matrix(lane);
    PIN(red); PIN(pst); PIN(pk0); PIN(ob); PIN(tg); PIN(sel);
    Frag f0, f1;
    BAR();
    frag_read<0, 3>(f0, xa, wa);
#pragma unroll 1
    for (int s = 0; s < 6; ++s) tile_step<0, 3>(f0, f1, acc, xa, wa, s, true);
    tile_step<0, 2>(f0, f1, acc, xa, wa, 6, true);
    SCHED_FENCE();
    frag_read<0, 2>(f1, 3 * SLOT + (xa ^ 64), 3 * SLOT + wa + 20480); frag_mma<0, 1>(f0, acc); interleave<1>();
    SCHED_FENCE();
    frag_mma<0, 1>(f1, acc);
    SCHED_FENCE();
    STAMP(1);
    {
        frag_mma<1, 2>(f0, acc); frag_mma<1, 2>(f1, acc);
        u32x4 A = cat(phi4(q4(acc[0][0], 0)), phi4(q4(acc[0][0], 1)));
#pragma unroll
        for (int i = 0; i < 8; ++i) { __builtin_amdgcn_sched_group_barrier(0x8, 1, 0); __builtin_amdgcn_sched_group_barrier(0x402, 5, 0); }
        SCHED_FENCE();
        f32x16 kv = 0.f, zz = 0.f;
        const u32x4 ones = {0x3F803F80u, 0x3F803F80u, 0x3F803F80u, 0x3F803F80u};
#pragma unroll
        for (int n = 0; n < 4; ++n) {
            zz = mma32(A, ones, zz);
            kv = mma32(A, cat(pkv(q4(acc[n >> 1][1], 2 * (n & 1))), pkv(q4(acc[n >> 1][1], 2 * (n & 1) + 1))), kv);
            if (n < 3) A = cat(phi4(q4(acc[(n + 1) >> 1][0], 2 * ((n + 1) & 1))), phi4(q4(acc[(n + 1) >> 1][0], 2 * ((n + 1) & 1) + 1)));
            SCHED_FENCE();
        }
        f32x4 k0 = (g & 1) ? q4(kv, 2) : q4(kv, 0), k1 = (g & 1) ? q4(kv, 3) : q4(kv, 1);
        f32x4 z0 = (g & 1) ? q4(zz, 2) : q4(zz, 0), z1 = (g & 1) ? q4(zz, 3) : q4(zz, 1);
        if (rh) { lwf128(red, k0); lwf128(red + 1024, k1); k0 = z0; k1 = z1; } else { lwf128(red + 2048, z0); lwf128(red + 3072, z1); }
        BAR();
        st128(rws, pst, catt(k0 + lrf128(red + rh * 2048), k1 + lrf128(red + rh * 2048 + 1024), tg));
    }
    STAMP(2);
    SCHED_FENCE();
    frag_read<2, 3>(f0, 2 * SLOT + xa, 2 * SLOT + wa);
#pragma unroll 1
    for (int s = 6; s < 8; ++s) tile_step<2, 3>(f0, f1, acc, xa, wa, s, false);
    SCHED_FENCE();
    CWAIT();
    BAR();
    STAMP(3);
    u32x4 dk[8];
#pragma unroll
    for (int j = 0; j < 8; ++j) dk[j] = ld128s(rws, pk0, j * 8 * PART_STRIDE);
    SCHED_FENCE();
    u32x4 pq[2][2];
#pragma unroll
    for (int i = 0; i < 2; ++i)
#pragma unroll
        for (int b = 0; b < 2; ++b) { pq[i][b] = cat(phi4(q4(acc[i][2], 2 * b)), phi4(q4(acc[i][2], 2 * b + 1))); PIN(pq[i][b]); }
    SCHED_FENCE();
    sum_panels(rws, dk, pk0, true, tg, sel, red + 8192 * rh);
    BAR();
    if (HINT == 1 && w == 0 && lane == 0) st32(rws, WS_HINT + (rp * 8 + hg) * 128, ep);
    STAMP(4);
    const u32x4 KV = cat(pkv(lrf128(red) + lrf128(red + 8192)), pkv(lrf128(red + 1024) + lrf128(red + 9216)));
    u32x4 ZZ = cat(pkv(lrf128(red + 2048) + lrf128(red + 10240)), pkv(lrf128(red + 3072) + lrf128(red + 11264)));
#pragma unroll
    for (int k = 0; k < 4; ++k) ZZ[k] = __builtin_amdgcn_update_dpp(ZZ[k], ZZ[k], 0x114, 0xF, 0x2, false);
#pragma unroll
    for (int b = 0; b < 2; ++b) {
        const u32x4 A = ((g & 1) == b) ? KV : ZZ;
#pragma unroll
        for (int i = 0; i < 2; ++i) {
            const f32x16 nd = mma32(A, pq[i][b], 0.f);
            const float ri = TAG_GAIN * __builtin_amdgcn_rcpf(nd[b ? 0 : 8] + 1e-6f);
            st128(rws, ob + i * 32768 + b * 512, catt(q4(nd, 2 * b) * ri, q4(nd, 2 * b + 1) * ri, tg));
        }
    }
    if (HINT == 2 && w == 0 && lane == 0) st32(rws, WS_HINT + (rp * 8 + hg) * 128, ep);
    STAMP(5);
}

constexpr unsigned L_U = 98304;
constexpr unsigned L_S = 65536;
constexpr unsigned L_W = L_U + 19200;
constexpr unsigned L_SP = L_W + 24576;
constexpr unsigned L_C = L_SP + 512;
constexpr unsigned L_PW = L_C + 14336;
struct BIn { const char* pf; size_t pm; unsigned po; };
DI void touch(const BIn& in, unsigned add) {
    asm volatile("s_mov_b64 exec, %2\n\tglobal_load_dword v255, %0, %1\n\ts_mov_b64 exec, -1" :: "v"(in.po), "s"(in.pf + add), "s"(in.pm) : "memory", "v255");
}
DI void b_loads(const Params& p, BIn& in, int xcd, int li, int tid, int lane, int w) {
    const int g = lane >> 4, c16 = lane & 15;
    in.pf = (w & 1) ? (const char*)p.ax : (const char*)p.wqkv;
    in.pm = (w & 1) ? 0xFFFFFFFFull : 0x0FFF0FFF0FFF0FFFull;
    in.po = (w & 1) ? ((xcd >> 1) * 512 + li * 32 + (lane & 31)) * 1024 + (1 + (w >> 1)) * 128
                    : (64 + 32 * w + li * 4 + g) * 3072 + (xcd & 1) * 512 + (c16 >> 2) * 1024 + (c16 & 3) * 128;
    if (w < 2) touch(in, 0);
}
#define BXP 1
#define WG 1
#define WF_POL " nt"
struct BMid { Mat F; Cx tw1; u32x4 y0, y1; unsigned ra[4], oo, bp, u2, tb; };
DI void b_first(const Params& p, rsrc_t rws, unsigned& ep, BIn& in, BMid& mid, int xcd, int li, int tid, int lane, int w, int ct STP_PARAM) {
    const int g = lane >> 4, c16 = lane & 15;
    const int me = xcd * 16 + li;
    const int j1 = (li >> 3) ? (xcd ? 16 - xcd : 8) : xcd, kb = li & 7;
    const int r8 = lane >> 3, ch = lane & 7, jm = (16 - c16) & 15;
    const bool special = (kb == 0 && w == 0);
    const unsigned ws = __builtin_amdgcn_readfirstlane(w);
    const rsrc_t rf = mkrsrc((ws & 2) ? p.wi : p.wr, 2048 * 514);
    unsigned spr, spi;
    auto rows = [&]() {
        asm volatile("s_mov_b32 m0, %0\n\ts_nop 0\n\tbuffer_load_dwordx4 %1, %3, 0 offen nt lds\n\tbuffer_load_dwordx4 %2, %3, 0 offen offset:1024 nt lds"
                     :: "s"(ws * 2048), "v"((256 * w + me) * 1024 + lane * 16), "v"((256 * w + me + 127) * 1024 + lane * 16), "s"(mkrsrc(p.bx, 2048 * 512 * 2)) : "memory");
        if (w == 7) {
            const unsigned o = ((16 * me + (g ? c16 : jm)) * 257 + (g == 0 ? 0 : g == 1 ? 256 : 128)) * 2;
            asm volatile("global_load_ushort %0, %2, %3\n\tglobal_load_ushort %1, %2, %4" : "=&v"(spr), "=&v"(spi) : "v"(o), "s"(p.wr), "s"(p.wi) : "memory");
        }
        const int kr = ((lane & 8) ? (xcd ? 16 - xcd : 8) : xcd) + 16 * (li * 8 + (w >> 2) * 4 + (w & 1) * 2 + (lane >> 4 & 1));
        asm volatile("s_mov_b64 exec, 0x1F1F1F1F\n\tbuffer_load_dword v255, %0, %1, 0 offen" WF_POL "\n\ts_mov_b64 exec, -1"
                     :: "v"((kr * 514 + (lane & 7) * 128) & ~3u), "s"(rf) : "memory", "v255");
    };
    if ((w >> 1) == 1) touch(in, 0);
    if (BXP == 1) rows();
    if (w < 7) {
        const int k4 = 4 * w, sU = (0x0040004 >> k4) & 15, sQ = (0x0400040 >> k4) & 15;
        const int P = (((c16 & ((0xF7FF7FF >> k4) & 15)) << sU) + (sU ? j1 : 0)) << ((0x3007833 >> k4) & 15);
        const int n0 = P * (((4 * (g & ((0x3313133 >> k4) & 15))) << sQ) + (sQ ? j1 : 0)), dn = P << sQ;
        const Cx t = angles(n0, dn, 2047, 1.f / 2048);
        Mat m = Mat{__builtin_bit_cast(u32x4, t.re), __builtin_bit_cast(u32x4, t.im)};
        if (w < 4) m = tomat(t);
        if (w == 2 && (g >> 1) != (c16 >> 3)) m = Mat{u32x4{0, 0, 0, 0}, u32x4{0, 0, 0, 0}};
        lw128(L_C + w * 2048 + lane * 16, m.re); lw128(L_C + w * 2048 + 1024 + lane * 16, m.im);
    }
    if (BXP == 2) rows();
    const int km0 = 16 * w + c16, km1 = km0 ? 256 - km0 : 128;
    unsigned ca = L_C + lane * 16, xa = w * 2048 + g * 256 + c16 * 4;
    unsigned ua = L_U + (2 * w) * 1072 + g * 272 + c16 * 16;
    PIN(ca); PIN(xa); PIN(ua);
    if (w >= 4) touch(in, 0);
    epoch_get(ep);
    BAR();
    if (w < 6) touch(in, (w & 1) ? 512 : 256 * 3072);
    Mat F = {lr128(ca + 6144), lr128(ca + 7168)};
    Cx tw1 = {lrf128(ca + 12288), lrf128(ca + 13312)};
    unsigned a0 = L_U + 4 * g * 1072 + ((km0 >> 2) & 3) * 272 + (km0 >> 4) * 16 + (km0 & 3) * 4;
    unsigned a1s = L_U + 4 * g * 1072 + ((km1 >> 2) & 3) * 272 + (km1 >> 4) * 16 + (km1 & 3) * 4;
    unsigned sw0 = L_S + w * 2304 + c16 * 144 + g * 16, sw1 = L_S + w * 2304 + jm * 144 + 64 + g * 16;
    unsigned sr0 = L_S + w * 2304 + r8 * 144 + ch * 16, tb1 = WS_T1 + me * TB + w * 2048 + lane * 16;
    u32x4 tg = tag_bits(ep, g), tl = tag_bits(ep, lane & 3);
    unsigned wv[3];
#pragma unroll
    for (int k = 0; k < 3; ++k) {
        const int u = (w & 1) * 192 + k * 64 + lane, j2 = u / 3, ks = j1 + 16 * j2;
        wv[k] = (((w < 4 ? ks * 257 + 16 * kb : ((2048 - ks) & 2047) * 257 + 241 - 16 * kb) * 2) & ~15u) + (u - 3 * j2) * 16;
        PIN(wv[k]);
    }
    const int lam = tid >> 3, s2a = (lam >> 5) * 32 + (lam & 3) * 8 + ((lam >> 2) & 7);
    unsigned tb2 = WS_T1 + s2a * TB + (kb * 16 + j1) * 128 + ch * 16;
    const int t16 = (w >> 2) * 32 + (lane & 8) * 8 + (lane & 3) * 8 + (w & 3) * 2 + ((lane >> 2) & 1);
    unsigned tb4 = WS_T1 + t16 * TB + 16384 + j1 * 16;
    PIN(tb2); PIN(tb4); PIN(a0); PIN(a1s); PIN(sw0); PIN(sw1); PIN(sr0); PIN(tb1); PIN(tg); PIN(tl);
    if (w >= 4 && w < 6) VMWAIT(3); else VMWAIT(2);
    STAMP(1);
    if (w == 7) {
        PIN(spr); PIN(spi);
        const unsigned v = spr | spi << 16, vb = __builtin_amdgcn_ds_bpermute((c16 + 16) * 4, v), vr = __builtin_amdgcn_ds_bpermute((c16 + 32) * 4, v);
        if (lane < 16) st128(rws, WS_T1 + me * TB + 16640 + c16 * 16, u32x4{v, vb, vr, ep});
    }
    const u32x4 x0 = {lr32(xa), lr32(xa + 64), lr32(xa + 128), lr32(xa + 192)}, x1 = {lr32(xa + 1024), lr32(xa + 1088), lr32(xa + 1152), lr32(xa + 1216)};
    lw128(ua, pk4(dftA(twpk(dftA(x0, F), tw1), F))); lw128(ua + 1072, pk4(dftA(twpk(dftA(x1, F), tw1), F)));
    BAR();
    {
        const u32x4 d0 = {lr32(a0), lr32(a0 + 1072), lr32(a0 + 2144), lr32(a0 + 3216)};
        const u32x4 d1 = {lr32(a1s), lr32(a1s + 1072), lr32(a1s + 2144), lr32(a1s + 3216)};
        const Cx c0 = dftA(d0, F), c1 = dftA(d1, F);
        lw128(sw0, zipt(c0.re, c0.im, tg)); lw128(sw1, zipt(c1.re, -c1.im, tg));
        const u32x4 s0 = lr128(sr0), s1 = lr128(sr0 + 8 * 144);
        st128(rws, tb1, s0); st128(rws, tb1 + 1024, s1);
        if (tid < 16) st128(rws, WS_T1 + me * TB + 16384 + jm * 16, u32x4{pk(c0.re[0], c0.im[0]) & ~TM, 0, 0, ep});
    }
    STAMP(2);
#pragma unroll
    for (int k = 0; k < 3; ++k) dma(rf, wv[k], 0, L_W + ws * 3072 + k * 1024);
    asm volatile("s_mov_b64 exec, 0xFFFF\n\tglobal_load_dword v255, %0, %1\n\ts_mov_b64 exec, -1" :: "v"(((li & 3) * 128 + w * 16 + c16) * 1024 + ct * 128), "s"(p.wo) : "memory", "v255");
    u32x4 isp = {0, 0, 0, ep};
    if (kb == 0 && lane < 16) isp = ld128(rws, tb4 + 256);
    FENCE();
    const rsrc_t ro = mkrsrc(p.wo, 512 * 512 * 2);
    unsigned sb = L_U + ((ch & 3) * 2 + (ch >> 2)) * 1040 + ((lam >> 5) * 16 + ((lam >> 2) & 7)) * 16 + (lam & 3) * 4;
    unsigned vo = (w * 64 + r8) * 1024 + ct * 128 + ((ch ^ (r8 & 6)) << 4);
    unsigned vo1 = vo + 7168, vo2 = vo + 14336, vo3 = vo + 21504;
    PIN(sb); PIN(vo); PIN(vo1); PIN(vo2); PIN(vo3);
    Mat F1 = {lr128(ca), lr128(ca + 1024)}, F2 = {lr128(ca + 2048), lr128(ca + 3072)}, G = {lr128(ca + 4096), lr128(ca + 5120)};
    Cx twa = {lrf128(ca + 8192), lrf128(ca + 9216)}, twb = {lrf128(ca + 10240), lrf128(ca + 11264)};
    unsigned ta = L_U + ((w >> 1) * 2 + 8 * (w & 1)) * 1040 + lane * 16;
    const u32x4 tw = tag_bits(ep, w >> 1, 1);
    unsigned t2 = (w & 1) ? ((lane & 8) ? tw[3] : tw[2]) : ((lane & 8) ? tw[1] : tw[0]);
    const int col = 2 * w + (g >> 1);
    const float th = (float)(16 * kb + col) * (1.f / 512);
    float sn = __builtin_amdgcn_sinf(th), cs = __builtin_amdgcn_cosf(th);
    unsigned wp = L_W + (c16 + 64 * (g & 1)) * 48 + ((2 * j1) & 15) + col * 2, wq = L_W + 12288 + (c16 + 64 * (g & 1)) * 48 + ((2 - 2 * j1) & 15) + 30 - col * 2;
    PIN(F1.re); PIN(F1.im); PIN(F2.re); PIN(F2.im); PIN(G.re); PIN(G.im); PIN(twa.re); PIN(twa.im); PIN(twb.re); PIN(twb.im);
    PIN(ta); PIN(t2); PIN(sn); PIN(cs); PIN(wp); PIN(wq);
#pragma unroll
    for (int i = 0; i < 4; ++i) {
        const int km = 64 * g + 16 * i + c16, kq = km < 128 ? km : (256 - km) & 127;
        mid.ra[i] = L_U + ((km >> 7) * 16 + 2 * w) * 592 + (kq + 16 * (kq >> 6)) * 4;
        PIN(mid.ra[i]);
    }
    const int set = g & 1, k0 = 16 * (2 * w + (g >> 1));
    mid.oo = (256 * w + me) * 2048 + 1024 + lane * 16; mid.bp = ((lane & 3) * 16 + (lane >> 2)) * 4;
    mid.u2 = L_U + (set * 16 + (set ? jm : c16)) * 592 + (k0 + 16 * (k0 >> 6)) * 4;
    mid.tb = WS_T1 + me * TB + ((2 * w + (c16 >> 3)) * 16 + 4 * g) * 128 + ch * 16;
    PIN(mid.oo); PIN(mid.u2); PIN(mid.bp); PIN(mid.tb);
    if (kb == 0) {
#pragma unroll 1
        for (int it = 0; it < SPIN_CAP && __any(isp[3] != ep); ++it) { BACKOFF(); if (lane < 16) isp = ld128(rws, tb4 + 256); }
        if (lane < 16) { lw32(L_PW + (j1 ? 127 - t16 : (128 - t16) & 127) * 16, isp[0]); lw64(L_PW + t16 * 16 + 8, u32x2{isp[1], isp[2]}); }
    }
    VMWAIT(1);
    BAR();
    STAMP(3);
    u32x4 i0 = ld128(rws, tb2), i1 = ld128s(rws, tb2, 64 * TB);
    if (kb == 0 && lane < 16) isp = ld128(rws, tb4);
    FENCE();
    Cx a1, bz, a2, bv;
    {
        f32x4 pr, pi, qr, qi;
#pragma unroll
        for (int r = 0; r < 4; ++r) { pr[r] = lrbf(wp + r * 768); pi[r] = lrbf(wp + r * 768 + 6144); qr[r] = lrbf(wq + r * 768); qi[r] = lrbf(wq + r * 768 + 6144); }
        const float sc = TAG_GAIN * TAG_GAIN / 1048576;
        f32x4 rr, ri;
        if (special && g < 2) {
            f32x4 ar, ai, br, bi;
#pragma unroll
            for (int r = 0; r < 4; ++r) {
                const u32x4 e = lr128(L_PW + (c16 + 64 * g + 16 * r) * 16);
                ar[r] = bf(e[0]); ai[r] = hi16(e[0]); br[r] = bf(e[2]); bi[r] = hi16(e[2]); rr[r] = bf(e[3]); ri[r] = hi16(e[3]);
            }
            pr = 0.5f * (pr + ar); pi = 0.5f * (pi - ai); qr = 0.5f * (qr + br); qi = 0.5f * (qi - bi);
        }
        const f32x4 sr = (pr + qr) * sc, si = (pi - qi) * sc, dr = (pr - qr) * sc, di = (pi + qi) * sc;
        a1.re = sr - dr * sn; a1.im = si - di * sn; a2.re = sr + dr * sn; a2.im = si + di * sn;
        bz.re = dr * cs; bz.im = di * cs; bv = bz;
        if (special && g < 2) { a2.re = rr * (2.f * sc); a2.im = ri * (2.f * sc); bv.re = Z4; bv.im = Z4; }
    }
    PIN(a1.re); PIN(a1.im); PIN(a2.re); PIN(a2.im); PIN(bz.re); PIN(bz.im); PIN(bv.re); PIN(bv.im);
    i0 ^= tl; i1 ^= tl;
#pragma unroll 1
    for (int it = 0; it < SPIN_CAP && (stale(or4(or4(0, i0), i1)) || __any(isp[3] != ep)); ++it) {
        BACKOFF();
        i0 = ld128(rws, tb2) ^ tl; i1 = ld128s(rws, tb2, 64 * TB) ^ tl;
        if (kb == 0 && lane < 16) isp = ld128(rws, tb4);
    }
    STAMP(10);
    lw32(sb, i0[0]); lw32(sb + 128, i0[1]); lw32(sb + 8320, i0[2]); lw32(sb + 8320 + 128, i0[3]);
    lw32(sb + 512, i1[0]); lw32(sb + 512 + 128, i1[1]); lw32(sb + 512 + 8320, i1[2]); lw32(sb + 512 + 8320 + 128, i1[3]);
    if (kb == 0 && lane < 16) lw32(L_SP + ((t16 >> 5) * 8 + (t16 & 7)) * 16 + ((t16 >> 3) & 3) * 4, isp[0]);
    BAR();
    STAMP(4);
    const u32x4 zi = lr128(ta), vi = lr128(ta + 1040);
    dma4(ro, vo, vo1, vo2, vo3, 0, w * 8192);
    u32x4 zf = twpk(dftA(zi, F1), twa), vf = twpk(dftA(vi, F1), twa);
    u32x4 vp = vf;
    if (special) {
        vp = lr128(L_SP + (g * 8 + (c16 & 7)) * 16) ^ 0x80000000u;
        if (c16 >= 8) vp = vi;
        vp = twpk(dftA(vp, F1), twa);
    }
    PIN(zf); PIN(vf);
    dma4(ro, vo, vo1, vo2, vo3, 32768, w * 8192 + 4096);
    {
        const Cx Z = dftB(zf, G), V = dftB(vf, G);
        Cx Vz = V;
        if (special) Vz = dftB(vp, G);
        u32x4 zc = zip(a1.re * Z.re - a1.im * Z.im - bz.re * Vz.im - bz.im * Vz.re, bz.im * Vz.im - a1.re * Z.im - a1.im * Z.re - bz.re * Vz.re);
        u32x4 vc = zip(a2.re * V.re - a2.im * V.im + bv.re * Z.im + bv.im * Z.re, bv.re * Z.re - a2.re * V.im - a2.im * V.re - bv.im * Z.im);
        const Cx zo = dftB(twpk(dftA(zc, G), twb), F2), vb = dftB(twpk(dftA(vc, G), twb), F2);
        lw128(ta, zipt(zo.re, zo.im, u32x4(t2))); lw128(ta + 1040, zipt(vb.re, -vb.im, u32x4(t2)));
        VMWAIT(0);
        BAR();
        const u32x4 o0 = {lr32(sb), lr32(sb + 128), lr32(sb + 8320), lr32(sb + 8320 + 128)};
        const u32x4 o1 = {lr32(sb + 512), lr32(sb + 512 + 128), lr32(sb + 512 + 8320), lr32(sb + 512 + 8320 + 128)};
        st128(rws, tb2, o0); __builtin_amdgcn_raw_buffer_store_b128(o1, rws, tb2, 64 * TB, 16);
    }
    STAMP(5);
    mid.F = F; mid.tw1 = tw1;
    if (w >= 4) { EWAIT(); BAR(); }
}
DI void b_second(const Params& p, rsrc_t rws, unsigned ep, BMid& mid, unsigned& hint, unsigned ha, int xcd, int li, int lane, int w STP_PARAM) {
    const int g = lane >> 4, c16 = lane & 15, ch = lane & 7, jm = (16 - c16) & 15, me = xcd * 16 + li;
    const Mat& F = mid.F; const Cx& tw1 = mid.tw1;
    u32x4 tl = tag_bits(ep, lane & 3, 1);
    const unsigned (&ra)[4] = mid.ra; const unsigned bp = mid.bp, u2 = mid.u2, tb = mid.tb;
    PIN(tl);
    if (w < 4) {
        EWAIT();
        BAR();
        STAMP(6);
        u32x4 l0 = ld128(rws, tb) ^ tl, l1 = ld128(rws, tb + 128) ^ tl, l2 = ld128(rws, tb + 256) ^ tl, l3 = ld128(rws, tb + 384) ^ tl;
#pragma unroll 1
        for (int it = 0; it < SPIN_CAP && stale(or4(or4(or4(or4(0, l0), l1), l2), l3)); ++it) {
            BACKOFF();
            l0 = ld128(rws, tb) ^ tl; l1 = ld128(rws, tb + 128) ^ tl; l2 = ld128(rws, tb + 256) ^ tl; l3 = ld128(rws, tb + 384) ^ tl;
        }
        u32x4 t[4];
#pragma unroll
        for (int e = 0; e < 4; ++e) t[e] = pk4(dftA(u32x4{l0[e], l1[e], l2[e], l3[e]}, F));
#pragma unroll
        for (int r = 0; r < 4; ++r) lw128(u2 + r * 16, u32x4{t[0][r], t[1][r], t[2][r], t[3][r]});
    }
    BAR();
    STAMP(11);
    if (w == 7) hint = ld32(rws, ha);
    FENCE();
    {
        const u32x4 d0 = {lr32(ra[0]), lr32(ra[1]), lr32(ra[2]), lr32(ra[3])};
        const u32x4 d1 = {lr32(ra[0] + 592), lr32(ra[1] + 592), lr32(ra[2] + 592), lr32(ra[3] + 592)};
        u32x4 y0 = pk4c(dftA(twpk(dftA(d0, F), tw1), F)), y1 = pk4c(dftA(twpk(dftA(d1, F), tw1), F));
#pragma unroll
        for (int r = 0; r < 4; ++r) {
            y0[r] = __builtin_amdgcn_ds_bpermute(bp, y0[r]); y1[r] = __builtin_amdgcn_ds_bpermute(bp, y1[r]);
        }
        PIN(y0); PIN(y1);
        mid.y0 = y0; mid.y1 = y1;
        STAMP(13);
    }
}

__global__ void __launch_bounds__(512, 2) fused_kernel(Params p) {
    const int b = blockIdx.x ^ (SWAP * 128), xcd = b & 7, i = b >> 3, li = i & 15;
    const int tid = threadIdx.x, lane = tid & 63, w = __builtin_amdgcn_readfirstlane(tid >> 6);
    BIn in;
    {
        size_t code;
        asm volatile("s_getpc_b64 %0" : "=s"(code));
        asm volatile("s_mov_b64 exec, 1\n\tglobal_load_dword v255, %0, %1\n\ts_mov_b64 exec, -1" :: "v"(min((unsigned)(w * 32 + i) * 128u, CODE_BYTES - 128u)), "s"(code & ~(size_t)255) : "memory", "v255");
    }
    __shared__ __attribute__((aligned(16))) char smem[LDS_BYTES];
    asm volatile("" :: "s"((unsigned)(size_t)(LAS char*)smem));
    STAMP(0);
    const int rp = (xcd >> 1) * 4 + (li & 3), hg = (xcd & 1) * 4 + (li >> 2), rt = xcd * 4 + (i & 3), ct = i >> 2;
    if (i < 16 && w >= 4) a_loader(p, rp, hg, lane, w - 4 STP_ARG);
    FENCE();
    if (i >= 16) b_loads(p, in, xcd, li, tid, lane, w);
    FENCE();
    const rsrc_t rws = mkrsrc(p.ws, WS_END);
    unsigned ep;
    epoch_ask(ep, p.ws);
    BMid mid;
    if (i >= 16) b_first(p, rws, ep, in, mid, xcd, li, tid, lane, w, ct STP_ARG);
    else if (w < 4) { epoch_get(ep); a_tile(rws, ep, rp, hg, lane, w STP_ARG); }
    else a_loader_tail(p, rws, ep, hg, lane, w - 4, ct STP_ARG);
    const int g = lane >> 4, c16 = lane & 15, q = c16 >> 2, kh = w >> 2;
    unsigned ob = WS_O + (rt * 4 + (w & 3)) * 16384 + kh * 8192 + lane * 16;
    unsigned wa = kh * 32768 + (16 * (g >> 1) + 4 * (g & 1) + q) * 128 + 8 * (lane & 3) + (((2 * g + (q >> 1)) & 3) << 5);
    const unsigned scr = 81920;
    unsigned rw = scr + kh * 17408 + ((w & 3) * 16 + c16) * 272 + g * 16, rr = scr + (tid >> 3) * 272 + (tid & 7) * 32;
    unsigned oo = (rt * 64 + (tid >> 3)) * 2048 + ct * 128 + (tid & 7) * 16;
    u32x4 tl = tag_bits(ep, lane & 3);
    u32x4 o[8], wf[8][4];
#pragma unroll
    for (int k = 0; k < 8; ++k)
#pragma unroll
        for (int t = 0; t < 4; ++t) { const unsigned a = (wa ^ (t << 5)) + k * 4096; wf[k][t] = ltr2(a, a + 1024); }
    PIN(ob); PIN(rw); PIN(rr); PIN(oo); PIN(tl);
    SCHED_FENCE();
    const unsigned ha = WS_HINT + ((rt >> 1) * 8 + (lane & 7)) * 128;
    unsigned hint = ep + 1;
    if (i >= 16) b_second(p, rws, ep, mid, hint, ha, xcd, li, lane, w STP_ARG);
    if (w == 7) {
#pragma unroll 1
        for (int it = 0; it < SPIN_CAP && !__all(hint == ep); ++it) { if (it) __builtin_amdgcn_s_sleep(2); FENCE(); hint = ld32(rws, ha); }
    }
    if (i < 16) DWAIT();
    BAR();
    STAMP(7);
#pragma unroll
    for (int k = 0; k < 8; ++k) o[k] = ld128s(rws, ob, k * 1024);
    const rsrc_t rout = mkrsrc(p.out, 2048 * 1024 * 2);
    f32x4 acc[4] = {Z4, Z4, Z4, Z4};
    unsigned m = 0;
#pragma unroll
    for (int k = 0; k < 8; ++k) {
        o[k] ^= tl;
#pragma unroll
        for (int t = 0; t < 4; ++t) acc[t] = mma(wf[k][t], o[k], acc[t]);
        m = or4(m, o[k]);
        SCHED_FENCE();
    }
    if (__builtin_expect(stale(m), 0)) {
#pragma unroll 1
        for (int it = 0; it < SPIN_CAP; ++it) {
            BACKOFF();
            m = 0;
#pragma unroll
            for (int k = 0; k < 8; ++k) o[k] = ld128s(rws, ob, k * 1024) ^ tl;
#pragma unroll
            for (int k = 0; k < 8; ++k) m = or4(m, o[k]);
            if (!stale(m)) break;
        }
#pragma unroll
        for (int t = 0; t < 4; ++t) acc[t] = Z4;
#pragma unroll
        for (int k = 0; k < 8; ++k)
#pragma unroll
            for (int t = 0; t < 4; ++t) acc[t] = mma(wf[k][t], o[k], acc[t]);
    }
    STAMP(8);
#pragma unroll
    for (int t = 0; t < 4; ++t) lwf128(rw + t * 64, acc[t]);
    if (i >= 16) {
        __builtin_amdgcn_raw_buffer_store_b128(mid.y0, rout, mid.oo, 0, OUTB_AUX);
        __builtin_amdgcn_raw_buffer_store_b128(mid.y1, rout, mid.oo, 128 * 2048, OUTB_AUX);
    }
    BAR();
    const f32x4 y0 = lrf128(rr) + lrf128(rr + 17408), y1 = lrf128(rr + 16) + lrf128(rr + 17408 + 16);
    __builtin_amdgcn_raw_buffer_store_b128(cat(pkv(y0), pkv(y1)), rout, oo, 0, OUT_AUX);
    if (b == 128 && tid == 256) st32(rws, WS_EPOCH, ep);
    STAMP(9);
}

extern "C" void kernel_launch(void* const* d_in, const int* in_sizes, int n_in,
                              void* d_out, int out_size, void* d_ws, size_t ws_size,
                              hipStream_t stream) {
    Params p;
    p.ax = (const unsigned short*)d_in[0]; p.wqkv = (const unsigned short*)d_in[1]; p.wo = (const unsigned short*)d_in[2];
    p.bx = (const unsigned short*)d_in[3]; p.wr = (const unsigned short*)d_in[4]; p.wi = (const unsigned short*)d_in[5];
    p.out = (unsigned short*)d_out; p.ws = (unsigned char*)d_ws;
    fused_kernel<<<256, 512, 0, stream>>>(p);
}
```
